```python
import torch

def _softmax(x, axis=-1):
    return torch.softmax(x, dim=axis)
def _gelu(x):
    return torch.nn.functional.gelu(x, approximate="tanh")
def _silu(x):
    return torch.nn.functional.silu(x)
def _sigmoid(x):
    return torch.sigmoid(x)
def _layernorm(x, axis=-1, eps=1e-5):
    m = x.mean(dim=axis, keepdim=True)
    v = x.var(dim=axis, keepdim=True, unbiased=False)
    return (x - m) / torch.sqrt(v + eps)
def _scatter_add(tgt, idx, src, axis=0):
    return tgt.clone().index_add_(axis, idx, src)
def _segment_sum(src, seg, n):
    out = torch.zeros((n,) + src.shape[1:], dtype=src.dtype)
    return out.index_add_(0, seg, src)
def _pad(x, pw):
    flat = tuple(v for pair in reversed(pw) for v in pair)
    return torch.nn.functional.pad(x, flat)

kernel_name = 'concat_heads__gqa_block_cached__mha_block_cached__near_ridge'

def setup_inputs():

    _w = (lambda a: (lambda t: t.to(torch.bfloat16) if t.is_floating_point() else t)(torch.as_tensor(a)))
    def _A_setup():
        B, Sq, D = 8, 144, 576
        Hq, Hkv, Sk, Dh = 32, 16, 1024, 128
        assert Hq % Hkv == 0, (Hq, Hkv)
        return {
            "x": _w((lambda *s: torch.randn(*s, dtype=torch.bfloat16) * 0.8)(B, Sq, D)),
            "k_cache": _w((lambda *s: torch.randn(*s, dtype=torch.bfloat16) * 0.8)(B, Hkv, Sk, Dh)),
            "v_cache": _w((lambda *s: torch.randn(*s, dtype=torch.bfloat16) * 0.05)(B, Hkv, Sk, Dh)),
            "wq": _w((lambda *s: torch.randn(*s, dtype=torch.bfloat16) * 0.8)(D, Hq * Dh) * (D ** -0.5)),
            "wo": _w((lambda *s: torch.randn(*s, dtype=torch.bfloat16) * 0.05)(Hq * Dh, D) * ((Hq * Dh) ** -0.5)),
        }
    _A_in = _A_setup()

    def _B_setup():
        B, Sq, D = 8, 144, 576
        H, Sk, Dh = 32, 1024, 128
        return {
            "x": _w((lambda *s: torch.randn(*s, dtype=torch.bfloat16) * 0.8)(B, Sq, D)),
            "k_cache": _w((lambda *s: torch.randn(*s, dtype=torch.bfloat16) * 0.8)(B, H, Sk, Dh)),
            "v_cache": _w((lambda *s: torch.randn(*s, dtype=torch.bfloat16) * 0.05)(B, H, Sk, Dh)),
            "wq": _w((lambda *s: torch.randn(*s, dtype=torch.bfloat16) * 0.8)(D, H * Dh) * (D ** -0.5)),
            "wo": _w((lambda *s: torch.randn(*s, dtype=torch.bfloat16) * 0.05)(H * Dh, D) * ((H * Dh) ** -0.5)),
        }
    _B_in = _B_setup()

    return {
        "A_x": _A_in["x"],
        "A_k_cache": _A_in["k_cache"],
        "A_v_cache": _A_in["v_cache"],
        "A_wq": _A_in["wq"],
        "A_wo": _A_in["wo"],
        "B_x": _B_in["x"],
        "B_k_cache": _B_in["k_cache"],
        "B_v_cache": _B_in["v_cache"],
        "B_wq": _B_in["wq"],
        "B_wo": _B_in["wo"],
    }

def reference(A_x, A_k_cache, A_v_cache, A_wq, A_wo, B_x, B_k_cache, B_v_cache, B_wq, B_wo):
    def _A_ref(x, k_cache, v_cache, wq, wo):
        B, Sq, D = x.shape
        Hq, Dh = 32, 128
        Hkv = k_cache.shape[1]
        rep = Hq // Hkv
        q = (lambda x, *d: x.permute(*d))((x @ wq).reshape(B, Sq, Hq, Dh), 0, 2, 1, 3)
        k = (lambda x, n, axis: torch.repeat_interleave(x, n, dim=axis))(k_cache, rep, axis=1)
        v = (lambda x, n, axis: torch.repeat_interleave(x, n, dim=axis))(v_cache, rep, axis=1)
        scores = torch.einsum("bhqd,bhkd->bhqk", q, k) * (Dh ** -0.5)
        p = _softmax(scores, axis=-1)
        o = torch.einsum("bhqk,bhkd->bhqd", p, v)
        return (lambda x, *d: x.permute(*d))(o, 0, 2, 1, 3).reshape(B, Sq, Hq * Dh) @ wo

    def _B_ref(x, k_cache, v_cache, wq, wo):
        B, Sq, D = x.shape
        H, Dh = 32, 128
        q = (lambda x, *d: x.permute(*d))((x @ wq).reshape(B, Sq, H, Dh), 0, 2, 1, 3)
        scores = torch.einsum("bhqd,bhkd->bhqk", q, k_cache) * (Dh ** -0.5)
        p = _softmax(scores, axis=-1)
        o = torch.einsum("bhqk,bhkd->bhqd", p, v_cache)
        return (lambda x, *d: x.permute(*d))(o, 0, 2, 1, 3).reshape(B, Sq, H * Dh) @ wo

    a = _A_ref(A_x, A_k_cache, A_v_cache, A_wq, A_wo)
    b = _B_ref(B_x, B_k_cache, B_v_cache, B_wq, B_wo)
    return torch.concatenate([a, b], axis=-1)
```

```cpp
#include <hip/hip_runtime.h>
#include <hip/hip_bf16.h>
#include <stdint.h>
typedef unsigned short u16;
typedef unsigned long long u64;
static __device__ __forceinline__ u16 f2bf_(float f) { unsigned u = __float_as_uint(f); u += 0x7fff + ((u >> 16) & 1); return (u16)(u >> 16); }
#define QOFF (32u << 20)
typedef __attribute__((ext_vector_type(8))) short s8v;
typedef __attribute__((ext_vector_type(8))) __bf16 b8v;
typedef __attribute__((ext_vector_type(16))) float f16v;
typedef __attribute__((ext_vector_type(4))) float f4v;
typedef const char* __restrict__ cp;
__global__ __launch_bounds__(256, 1) void attn_k1(cp Ak, cp Av, cp Ax, cp Awq, cp Awo, cp Awo2, char* ws, char* out, cp Bk, cp Bv, cp Bx, cp Bwq, cp Bwo, cp Bwo2, char* ws2, char* out2) {
  __shared__ __attribute__((aligned(16))) char smem[163840];
  unsigned w = __builtin_amdgcn_readfirstlane(threadIdx.x >> 6) + (unsigned)(uintptr_t)smem;
  unsigned id = blockIdx.x;
  u64 ka = (u64)__builtin_amdgcn_kernarg_segment_ptr();
  asm volatile(
    "s_bfe_u32 s65, %1, 0x10003\n\t"
    "s_lshl_b32 s20, s65, 6\n\t"
    "s_load_dwordx16 s[4:19], %0, s20\n\t"
    "v_mbcnt_lo_u32_b32 v222, -1, 0\n\t"
    "s_bfe_u32 s26, %1, 0x20001\n\t"
    "s_lshr_b32 s27, %1, 6\n\t"
    "v_mbcnt_hi_u32_b32 v222, -1, v222\n\t"
    "s_lshl2_add_u32 s28, s26, s27\n\t"
    "s_lshr_b32 s29, %2, 1\n\t"
    "s_lshl1_add_u32 s29, s28, s29\n\t"
    "s_lshl_b32 s30, s29, 8\n\t"
    "s_sub_u32 s90, 4719616, s30\n\t"
    "s_and_b32 s31, %1, 1\n\t"
    "s_bfe_u32 s32, %1, 0x20004\n\t"
    "s_lshl2_add_u32 s31, s31, s32\n\t"
    "s_mul_i32 s33, s31, 165888\n\t"
    "s_mov_b32 s91, 0x00020000\n\t"
    "s_mov_b32 s62, %2\n\t"
    "s_and_b32 s50, s62, 1\n\t"
    "s_lshr_b32 s51, s62, 1\n\t"
    "v_bfe_u32 v63, v222, 2, 3\n\t"
    "v_and_b32 v64, 3, v222\n\t"
    "s_mul_i32 s52, s50, 131104\n\t"
    "v_lshlrev_b32 v64, 4, v64\n\t"
    "v_lshrrev_b32 v65, 5, v222\n\t"
    "v_lshl_add_u32 v64, v65, 6, v64\n\t"
    "s_bfe_u32 s67, %1, 0x20004\n\t"
    "v_lshl_add_u32 v65, v63, 13, v64\n\t"
    "s_lshr_b32 s66, %1, 6\n\t"
    "v_xor_b32 v65, s52, v65\n\t"
    "s_add_u32 s67, s66, s67\n\t"
    "s_and_b32 s67, s67, 3\n\t"
    "s_lshl_b32 s99, s67, 2\n\t"
    "s_lshr_b32 s97, 0, s99\n\t"
    "s_lshl_b32 s70, s62, 12\n\t"
    "s_and_b32 s67, s97, 15\n\t"
    "s_lshr_b32 s97, s97, 4\n\t"
    "s_lshl_b32 s66, s67, 19\n\t"
    "s_lshl_b32 s67, s67, 7\n\t"
    "s_add_u32 m0, s70, 51200\n\t"
    "v_add_u32 v40, 1024, v65\n\t"
    "v_add_u32 v41, 128, v65\n\t"
    "v_add_u32 v42, 64512, v65\n\t"
    "v_add_u32 v43, 63616, v65\n\t"
    "v_lshrrev_b32 v61, 4, v222\n\t"
    "v_bfe_u32 v63, v222, 1, 3\n\t"
    "v_and_b32 v64, 1, v222\n\t"
    "v_lshlrev_b32 v64, 4, v64\n\t"
    "v_lshl_add_u32 v64, v61, 5, v64\n\t"
    "v_lshl_add_u32 v218, v63, 8, v64\n\t"
    "s_mov_b32 s94, 165888\n\t"
    "s_mov_b32 s95, 0x00020000\n\t"
    "v_bfrev_b32 v62, v61\n\t"
    "v_lshrrev_b32 v62, 30, v62\n\t"
    "v_and_b32 v64, 7, v222\n\t"
    "v_xor_b32 v64, v64, v62\n\t"
    "v_lshlrev_b32 v64, 4, v64\n\t"
    "v_lshrrev_b32 v63, 3, v222\n\t"
    "v_lshl_add_u32 v214, v63, 8, v64\n\t"
    "s_mul_i32 s52, s51, 9\n\t"
    "s_mul_i32 s53, s50, 5\n\t"
    "s_add_u32 s52, s52, s53\n\t"
    "s_lshl_b32 s71, s52, 10\n\t"
    "s_add_u32 s71, s71, 32768\n\t"
    "s_and_b32 s53, s52, 1\n\t"
    "s_lshl_b32 s53, s53, 6\n\t"
    "s_mul_i32 s52, s52, 9216\n\t"
    "v_xor_b32 v64, s53, v64\n\t"
    "v_mul_u32_u24 v63, 1152, v63\n\t"
    "v_add3_u32 v44, v63, v64, s52\n\t"
    "v_xor_b32 v64, 64, v44\n\t"
    "v_add_u32 v45, 8192, v64\n\t"
    "v_add_u32 v46, 16384, v44\n\t"
    "v_add_u32 v47, 24576, v64\n\t"
    "v_add_u32 v48, 36864, v44\n\t"
    "s_mov_b32 s97, 2271560481\n\t"
    "s_cmp_eq_u32 s99, 4\n\t"
    "s_cmov_b32 s97, 1484133426\n\t"
    "s_cmp_eq_u32 s99, 8\n\t"
    "s_cmov_b32 s97, 1703354691\n\t"
    "s_cmp_eq_u32 s99, 12\n\t"
    "s_cmov_b32 s97, 1985491476\n\t"
    "s_memrealtime s[34:35]\n\t"
    "s_waitcnt lgkmcnt(0)\n\t"
    "s_add_u32 s88, s10, s30\n\t"
    "s_addc_u32 s89, s11, 0\n\t"
    "s_sub_u32 s88, s88, 1024\n\t"
    "s_subb_u32 s89, s89, 0\n\t"
    "buffer_load_dwordx4 v40, s[88:91], s66 offen offset:0  lds\n\t"
    "s_add_u32 s92, s8, s33\n\t"
    "s_addc_u32 s93, s9, 0\n\t"
    "buffer_load_dwordx4 v41, s[88:91], s66 offen offset:1024  lds\n\t"
    "s_mov_b32 s35, 210\n\t"
    "s_mov_b32 s56, s34\n\t"
    "buffer_load_dwordx4 v42, s[88:91], s66 offen offset:2048  lds\n\t"
    "s_mov_b64 s[60:61], s[16:17]\n\t"
    "buffer_load_dwordx4 v43, s[88:91], s66 offen offset:3072  lds\n\t"
    "s_add_u32 m0, s71, 51200\n\t"
    "s_getpc_b64 s[46:47]\n\t"
    "v_lshlrev_b32 v63, 7, v222\n\t"
    "s_bfe_u32 s52, %1, 196611\n\t"
    "s_lshl2_add_u32 s52, s52, s62\n\t"
    "s_lshl_b32 s52, s52, 11\n\t"
    "v_and_b32 v63, 0x780, v63\n\t"
    "v_add_u32 v63, s52, v63\n\t"
    "v_min_u32 v63, 49920, v63\n\t"
    "buffer_load_dwordx4 v44, s[92:95], s67 offen offset:0  lds\n\t"
    "global_load_dword v246, v63, s[46:47]\n\t"
    "v_mov_b32 v63, 0\n\t"
    "v_accvgpr_write_b32 a248, v63\n\t"
    "v_accvgpr_write_b32 a249, v63\n\t"
    "v_accvgpr_write_b32 a250, v63\n\t"
    "v_accvgpr_write_b32 a251, v63\n\t"
    "s_mov_b32 s38, 263168\n\t"
    "s_mov_b32 s39, 0x00020000\n\t"
    "buffer_load_dwordx4 v45, s[92:95], s67 offen offset:1024  lds\n\t"
    "v_mfma_f32_32x32x16_bf16 v[80:95], a[248:251], a[248:251], 0\n\t"
    "v_mfma_f32_32x32x16_bf16 v[96:111], a[248:251], a[248:251], 0\n\t"
    "v_mfma_f32_32x32x16_bf16 v[112:127], a[248:251], a[248:251], 0\n\t"
    "v_mfma_f32_32x32x16_bf16 v[128:143], a[248:251], a[248:251], 0\n\t"
    "v_mfma_f32_32x32x16_bf16 v[144:159], a[248:251], a[248:251], 0\n\t"
    "v_mfma_f32_32x32x16_bf16 v[160:175], a[248:251], a[248:251], 0\n\t"
    "v_mfma_f32_32x32x16_bf16 v[176:191], a[248:251], a[248:251], 0\n\t"
    "v_mfma_f32_32x32x16_bf16 v[192:207], a[248:251], a[248:251], 0\n\t"
    "buffer_load_dwordx4 v46, s[92:95], s67 offen offset:2048  lds\n\t"
    "v_mfma_f32_32x32x16_bf16 v[224:239], a[248:251], a[248:251], 0\n\t"
    "s_mov_b32 s63, 0\n\t"
    "s_mov_b32 s64, 0x3e0293ee\n\t"
    "s_mov_b32 s68, 51200\n\t"
    "s_mov_b32 s69, -51200\n\t"
    "v_and_b32 v60, 15, v222\n\t"
    "v_lshrrev_b32 v63, 3, v60\n\t"
    "v_and_b32 v64, 7, v60\n\t"
    "buffer_load_dwordx4 v47, s[92:95], s67 offen offset:3072  lds\n\t"
    "v_lshlrev_b32 v64, 7, v64\n\t"
    "v_bfe_u32 v65, v60, 1, 2\n\t"
    "v_bfrev_b32 v65, v65\n\t"
    "v_lshrrev_b32 v65, 30, v65\n\t"
    "v_lshl_add_u32 v65, v63, 2, v65\n\t"
    "v_xor_b32 v65, v65, v62\n\t"
    "v_lshl_add_u32 v64, v65, 4, v64\n\t"
    "v_lshl_add_u32 v211, v63, 11, v64\n\t"
    "s_cmp_eq_u32 s50, 1\n\t"
    "s_cbranch_scc1 XS0_%=\n\t"
    "s_add_u32 m0, m0, 4096\n\t"
    "s_nop 0\n\t"
    "buffer_load_dwordx4 v48, s[92:95], s67 offen offset:0  lds\n\t"
    "XS0_%=:\n\t"
    "v_lshl_add_u32 v52, v63, 10, v64\n\t"
    "v_add_u32 v52, 51200, v52\n\t"
    "v_xor_b32 v212, 64, v211\n\t"
    "v_xor_b32 v53, 64, v52\n\t"
    "v_and_b32 v63, 31, v222\n\t"
    "v_lshlrev_b32 v63, 3, v63\n\t"
    "v_lshrrev_b32 v64, 5, v222\n\t"
    "v_lshl_add_u32 v213, v64, 11, v63\n\t"
    "s_add_u32 s66, s66, 0x40000\n\t"
    "s_add_u32 m0, s70, 67584\n\t"
    "s_lshl_b32 s52, s51, 13\n\t"
    "s_add_u32 s52, s52, 51200\n\t"
    "v_bfe_u32 v63, v222, 2, 2\n\t"
    "v_and_b32 v64, 3, v222\n\t"
    "v_lshlrev_b32 v64, 3, v64\n\t"
    "v_lshl_add_u32 v64, v63, 6, v64\n\t"
    "v_and_b32 v63, 16, v222\n\t"
    "v_lshl_add_u32 v64, v63, 1, v64\n\t"
    "buffer_load_dwordx4 v40, s[88:91], s66 offen offset:0  lds\n\t"
    "v_lshl_add_u32 v63, v62, 11, v64\n\t"
    "v_add_u32 v50, s52, v63\n\t"
    "v_xor_b32 v51, 32, v50\n\t"
    "v_mfma_f32_32x32x16_bf16 a[0:15], a[248:251], a[248:251], 0\n\t"
    "v_mfma_f32_32x32x16_bf16 a[16:31], a[248:251], a[248:251], 0\n\t"
    "v_mfma_f32_32x32x16_bf16 a[32:47], a[248:251], a[248:251], 0\n\t"
    "v_mfma_f32_32x32x16_bf16 a[48:63], a[248:251], a[248:251], 0\n\t"
    "v_mfma_f32_32x32x16_bf16 a[64:79], a[248:251], a[248:251], 0\n\t"
    "buffer_load_dwordx4 v41, s[88:91], s66 offen offset:1024  lds\n\t"
    "v_mfma_f32_32x32x16_bf16 a[80:95], a[248:251], a[248:251], 0\n\t"
    "v_mfma_f32_32x32x16_bf16 a[96:111], a[248:251], a[248:251], 0\n\t"
    "v_mfma_f32_32x32x16_bf16 a[112:127], a[248:251], a[248:251], 0\n\t"
    "v_mfma_f32_32x32x16_bf16 a[128:143], a[248:251], a[248:251], 0\n\t"
    "v_mfma_f32_32x32x16_bf16 a[144:159], a[248:251], a[248:251], 0\n\t"
    "v_mfma_f32_32x32x16_bf16 a[160:175], a[248:251], a[248:251], 0\n\t"
    "v_mfma_f32_16x16x32_bf16 a[176:179], a[248:251], a[248:251], 0\n\t"
    "v_mov_b32 v240, 1.0\n\t"
    "buffer_load_dwordx4 v42, s[88:91], s66 offen offset:2048  lds\n\t"
    "v_mov_b32 v241, 1.0\n\t"
    "v_mov_b32 v242, 1.0\n\t"
    "v_mov_b32 v243, 1.0\n\t"
    "v_mov_b32 v244, 1.0\n\t"
    "s_mov_b32 s44, 0x40000000\n\t"
    "s_lshl_b32 s52, s62, 11\n\t"
    "s_add_u32 s52, s52, 98304\n\t"
    "v_lshlrev_b32 v63, 4, v222\n\t"
    "buffer_load_dwordx4 v43, s[88:91], s66 offen offset:3072  lds\n\t"
    "v_add_u32 v245, s52, v63\n\t"
    "v_xor_b32 v216, 64, v214\n\t"
    "s_cmp_eq_u32 s50, 1\n\t"
    "s_cselect_b64 vcc, -1, 0\n\t"
    "v_cndmask_b32 v214, v214, v218, vcc\n\t"
    "v_cndmask_b32 v216, v216, v218, vcc\n\t"
    "s_lshl_b32 s52, 0x1000, s65\n\t"
    "s_xor_b32 s53, s50, 1\n\t"
    "s_lshl1_add_u32 s53, s53, s51\n\t"
    "s_and_b32 s67, s97, 15\n\t"
    "s_lshr_b32 s97, s97, 4\n\t"
    "s_lshl_b32 s66, s67, 19\n\t"
    "s_lshl_b32 s67, s67, 7\n\t"
    "s_add_u32 m0, s70, 0\n\t"
    "s_mul_i32 s42, s53, s52\n\t"
    "s_mul_i32 s53, s51, s52\n\t"
    "s_cmp_eq_u32 s65, 0\n\t"
    "s_cselect_b32 s54, s53, 0\n\t"
    "s_cselect_b32 s53, 0, s53\n\t"
    "s_lshr_b32 s59, 0x4000, s65\n\t"
    "s_lshr_b32 s58, s59, s65\n\t"
    "s_add_u32 s55, s54, 128\n\t"
    "s_add_u32 s54, s54, 1024\n\t"
    "v_add_u32 v215, s55, v214\n\t"
    "buffer_load_dwordx4 v40, s[88:91], s66 offen offset:0  lds\n\t"
    "v_add_u32 v217, s55, v216\n\t"
    "v_add_u32 v214, s54, v214\n\t"
    "v_add_u32 v216, s54, v216\n\t"
    "s_lshr_b32 s59, s59, 1\n\t"
    "v_add_u32 v218, s59, v214\n\t"
    "v_add_u32 v219, s59, v215\n\t"
    "v_add_u32 v220, s59, v216\n\t"
    "v_add_u32 v221, s59, v217\n\t"
    "buffer_load_dwordx4 v41, s[88:91], s66 offen offset:1024  lds\n\t"
    "s_sub_u32 s53, s53, s42\n\t"
    "v_add_u32 v213, s53, v213\n\t"
    "s_lshl1_add_u32 s53, s52, s53\n\t"
    "v_add_u32 v211, s53, v211\n\t"
    "v_add_u32 v212, s53, v212\n\t"
    "s_add_u32 s43, s42, 163840\n\t"
    "s_lshl_b32 s52, s52, 2\n\t"
    "s_mul_i32 s41, s50, s52\n\t"
    "buffer_load_dwordx4 v42, s[88:91], s66 offen offset:2048  lds\n\t"
    "s_add_u32 s41, s41, s42\n\t"
    "s_add_u32 s41, s41, 98304\n\t"
    "s_mov_b32 s40, 0\n\t"
    "v_mov_b32 v63, 0x3f803f80\n\t"
    "v_accvgpr_write_b32 a244, v63\n\t"
    "v_accvgpr_write_b32 a245, v63\n\t"
    "v_accvgpr_write_b32 a246, v63\n\t"
    "v_accvgpr_write_b32 a247, v63\n\t"
    "buffer_load_dwordx4 v43, s[88:91], s66 offen offset:3072  lds\n\t"
    "s_cmp_lg_u32 s63, 0\n\t"
    "s_cselect_b64 vcc, -1, 0\n\t"
    "v_lshrrev_b32 v63, 5, v222\n\t"
    "v_bfe_u32 v64, v222, 3, 2\n\t"
    "v_mul_u32_u24 v65, 5, v63\n\t"
    "v_sub_u32 v65, 14, v65\n\t"
    "v_lshlrev_b32 v64, v65, v64\n\t"
    "v_and_b32 v65, 7, v222\n\t"
    "v_lshlrev_b32 v65, 2, v65\n\t"
    "s_add_u32 m0, s71, 0\n\t"
    "v_lshl_add_u32 v65, v63, 7, v65\n\t"
    "s_lshl_b32 s52, s62, 8\n\t"
    "s_add_u32 s52, s52, %1\n\t"
    "s_lshl_b32 s52, s52, 8\n\t"
    "s_add_u32 s52, s52, 58720256\n\t"
    "v_add_u32 v65, s52, v65\n\t"
    "v_xor_b32 v252, v64, v65\n\t"
    "v_xor_b32 v253, 16, v65\n\t"
    "buffer_load_dwordx4 v44, s[92:95], s67 offen offset:0  lds\n\t"
    "v_lshrrev_b32 v64, 2, v222\n\t"
    "v_and_b32 v65, 3, v222\n\t"
    "v_lshlrev_b32 v65, 4, v65\n\t"
    "v_and_b32 v63, 16, v222\n\t"
    "v_lshlrev_b32 v63, 1, v63\n\t"
    "v_xor_b32 v65, v63, v65\n\t"
    "v_mul_u32_u24 v64, 1152, v64\n\t"
    "s_mul_i32 s52, s62, 73728\n\t"
    "buffer_load_dwordx4 v45, s[92:95], s67 offen offset:1024  lds\n\t"
    "v_add3_u32 v65, v64, v65, s52\n\t"
    "v_add_u32 v247, 1024, v65\n\t"
    "v_add_u32 v248, 18432, v65\n\t"
    "v_add_u32 v249, 35840, v65\n\t"
    "v_add_u32 v250, 53248, v65\n\t"
    "s_lshl3_add_u32 s53, s26, s27\n\t"
    "s_lshl_b32 s53, s53, 5\n\t"
    "s_sub_u32 s52, s52, s53\n\t"
    "buffer_load_dwordx4 v46, s[92:95], s67 offen offset:2048  lds\n\t"
    "s_add_u32 s52, s52, 2048\n\t"
    "v_lshrrev_b32 v64, 1, v222\n\t"
    "v_mul_u32_u24 v64, 1152, v64\n\t"
    "v_and_b32 v65, 1, v222\n\t"
    "v_lshlrev_b32 v65, 4, v65\n\t"
    "v_add3_u32 v251, v64, v65, s52\n\t"
    "s_cmp_eq_u32 s50, 1\n\t"
    "s_cselect_b64 s[4:5], s[6:7], s[4:5]\n\t"
    "s_cmp_eq_u32 s65, 0\n\t"
    "s_cselect_b32 s28, s28, s29\n\t"
    "buffer_load_dwordx4 v47, s[92:95], s67 offen offset:3072  lds\n\t"
    "s_add_u32 s29, s65, 4\n\t"
    "s_lshl_b32 s29, s31, s29\n\t"
    "s_add_u32 s28, s28, s29\n\t"
    "s_lshl_b32 s28, s28, 18\n\t"
    "s_add_u32 s36, s4, s28\n\t"
    "s_addc_u32 s37, s5, 0\n\t"
    "s_sub_u32 s36, s36, 1024\n\t"
    "s_subb_u32 s37, s37, 0\n\t"
    "s_mul_i32 s23, s26, 1179648\n\t"
    "s_lshl_b32 s22, s27, 6\n\t"
    "s_cmp_eq_u32 s50, 1\n\t"
    "s_cbranch_scc1 XS1_%=\n\t"
    "s_add_u32 m0, m0, 4096\n\t"
    "s_nop 0\n\t"
    "buffer_load_dwordx4 v48, s[92:95], s67 offen offset:0  lds\n\t"
    "XS1_%=:\n\t"
    "s_add_u32 s22, s22, s23\n\t"
    "s_add_u32 s4, s12, s22\n\t"
    "s_addc_u32 s5, s13, 0\n\t"
    "s_sub_u32 s4, s4, 1024\n\t"
    "s_subb_u32 s5, s5, 0\n\t"
    "s_sub_u32 s6, 4719616, s22\n\t"
    "s_mov_b32 s7, 0x00020000\n\t"
    "s_mov_b64 s[8:9], s[16:17]\n\t"
    "s_mov_b32 s10, 67108864\n\t"
    "s_mov_b32 s11, 0x00020000\n\t"
    "s_add_u32 s66, s66, 0x40000\n\t"
    "s_add_u32 m0, s70, 16384\n\t"
    "s_lshl2_add_u32 s32, s32, s27\n\t"
    "s_lshl2_add_u32 s32, s32, %2\n\t"
    "s_mul_i32 s32, s32, 18432\n\t"
    "s_add_u32 s32, s32, s23\n\t"
    "s_add_u32 s12, s12, s32\n\t"
    "s_addc_u32 s13, s13, 0\n\t"
    "s_add_u32 s14, s14, s32\n\t"
    "s_addc_u32 s15, s15, 0\n\t"
    "s_mul_i32 s31, s31, 331776\n\t"
    "s_mul_i32 s23, s65, 1152\n\t"
    "buffer_load_dwordx4 v40, s[88:91], s66 offen offset:0  lds\n\t"
    "s_add_u32 s31, s31, s23\n\t"
    "s_add_u32 s20, s18, s31\n\t"
    "s_addc_u32 s21, s19, 0\n\t"
    "s_sub_u32 s22, 331776, s23\n\t"
    "s_mov_b32 s23, 0x00020000\n\t"
    "s_lshl2_add_u32 s18, %1, %2\n\t"
    "s_lshl_b32 s17, s26, 8\n\t"
    "s_mov_b32 s19, s27\n\t"
    "s_lshl_b32 s16, %2, 11\n\t"
    "buffer_load_dwordx4 v41, s[88:91], s66 offen offset:1024  lds\n\t"
    "s_xor_b32 s26, s19, 0\n\t"
    "s_mul_i32 s26, s26, 294912\n\t"
    "s_add_u32 s26, s26, s17\n\t"
    "s_xor_b32 s30, s18, 0\n\t"
    "s_mul_i32 s30, s30, 18432\n\t"
    "s_xor_b32 s27, s19, 1\n\t"
    "s_mul_i32 s27, s27, 294912\n\t"
    "s_add_u32 s27, s27, s17\n\t"
    "buffer_load_dwordx4 v42, s[88:91], s66 offen offset:2048  lds\n\t"
    "s_xor_b32 s31, s18, 256\n\t"
    "s_mul_i32 s31, s31, 18432\n\t"
    "s_xor_b32 s28, s19, 2\n\t"
    "s_mul_i32 s28, s28, 294912\n\t"
    "s_add_u32 s28, s28, s17\n\t"
    "s_xor_b32 s32, s18, 512\n\t"
    "s_mul_i32 s32, s32, 18432\n\t"
    "s_xor_b32 s29, s19, 3\n\t"
    "buffer_load_dwordx4 v43, s[88:91], s66 offen offset:3072  lds\n\t"
    "s_mul_i32 s29, s29, 294912\n\t"
    "s_add_u32 s29, s29, s17\n\t"
    "s_xor_b32 s33, s18, 768\n\t"
    "s_mul_i32 s33, s33, 18432\n\t"
    "global_load_dword v254, v252, s[60:61] sc1\n\t"
    "s_and_b32 s67, s97, 15\n\t"
    "s_lshr_b32 s97, s97, 4\n\t"
    "s_lshl_b32 s66, s67, 19\n\t"
    "s_lshl_b32 s67, s67, 7\n\t"
    "s_cmp_eq_u32 s50, 1\n\t"
    "s_cbranch_scc1 QP1_%=\n\t"
    "QP0_%=:\n\t"
    "s_mov_b32 s99, 15290\n\t"
    "s_waitcnt vmcnt(17)\n\t"
    "s_barrier\n\t"
    "ds_read_b128 v[0:3], v52 offset:32768\n\t"
    "ds_read_b128 v[4:7], v52 offset:34816\n\t"
    "ds_read_b128 v[8:11], v52 offset:36864\n\t"
    "ds_read_b128 v[12:15], v52 offset:38912\n\t"
    "ds_read_b128 v[16:19], v52 offset:49152\n\t"
    "ds_read_b64_tr_b16 a[180:181], v50 offset:0\n\t"
    "ds_read_b64_tr_b16 a[182:183], v50 offset:256\n\t"
    "ds_read_b64_tr_b16 a[184:185], v51 offset:0\n\t"
    "ds_read_b64_tr_b16 a[186:187], v51 offset:256\n\t"
    "ds_read_b64_tr_b16 a[188:189], v50 offset:512\n\t"
    "ds_read_b64_tr_b16 a[190:191], v50 offset:768\n\t"
    "ds_read_b64_tr_b16 a[192:193], v51 offset:512\n\t"
    "ds_read_b64_tr_b16 a[194:195], v51 offset:768\n\t"
    "ds_read_b64_tr_b16 a[196:197], v50 offset:1024\n\t"
    "ds_read_b64_tr_b16 a[198:199], v50 offset:1280\n\t"
    "ds_read_b64_tr_b16 a[200:201], v51 offset:1024\n\t"
    "ds_read_b64_tr_b16 a[202:203], v51 offset:1280\n\t"
    "ds_read_b64_tr_b16 a[204:205], v50 offset:1536\n\t"
    "ds_read_b64_tr_b16 a[206:207], v50 offset:1792\n\t"
    "ds_read_b64_tr_b16 a[208:209], v51 offset:1536\n\t"
    "ds_read_b64_tr_b16 a[210:211], v51 offset:1792\n\t"
    "s_waitcnt vmcnt(13) lgkmcnt(0)\n\t"
    "s_barrier\n\t"
    "s_branch QL01_%=\n\t"
    "QL00_%=:\n\t"
    "v_mfma_f32_16x16x32_bf16 v[80:83], a[180:183], v[0:3], v[80:83]\n\t"
    "s_add_u32 m0, s68, s70\n\t"
    "ds_read_b128 v[20:23], v53 offset:32768\n\t"
    "v_mfma_f32_16x16x32_bf16 v[112:115], a[180:183], v[4:7], v[112:115]\n\t"
    "ds_read_b128 v[24:27], v53 offset:34816\n\t"
    "ds_read_b128 v[28:31], v53 offset:36864\n\t"
    "v_mfma_f32_16x16x32_bf16 v[144:147], a[180:183], v[8:11], v[144:147]\n\t"
    "ds_read_b128 v[32:35], v53 offset:38912\n\t"
    "ds_read_b128 v[36:39], v53 offset:49152\n\t"
    "v_mfma_f32_16x16x32_bf16 v[176:179], a[180:183], v[12:15], v[176:179]\n\t"
    "ds_read_b64_tr_b16 a[212:213], v50 offset:16384\n\t"
    "ds_read_b64_tr_b16 a[214:215], v50 offset:16640\n\t"
    "v_mfma_f32_16x16x32_bf16 v[224:227], a[180:183], v[16:19], v[224:227]\n\t"
    "buffer_load_dwordx4 v40, s[88:91], s66 offen offset:0  lds\n\t"
    "v_mfma_f32_16x16x32_bf16 v[84:87], a[184:187], v[0:3], v[84:87]\n\t"
    "ds_read_b64_tr_b16 a[216:217], v51 offset:16384\n\t"
    "ds_read_b64_tr_b16 a[218:219], v51 offset:16640\n\t"
    "v_mfma_f32_16x16x32_bf16 v[116:119], a[184:187], v[4:7], v[116:119]\n\t"
    "ds_read_b64_tr_b16 a[220:221], v50 offset:16896\n\t"
    "ds_read_b64_tr_b16 a[222:223], v50 offset:17152\n\t"
    "v_mfma_f32_16x16x32_bf16 v[148:151], a[184:187], v[8:11], v[148:151]\n\t"
    "ds_read_b64_tr_b16 a[224:225], v51 offset:16896\n\t"
    "ds_read_b64_tr_b16 a[226:227], v51 offset:17152\n\t"
    "v_mfma_f32_16x16x32_bf16 v[180:183], a[184:187], v[12:15], v[180:183]\n\t"
    "ds_read_b64_tr_b16 a[228:229], v50 offset:17408\n\t"
    "ds_read_b64_tr_b16 a[230:231], v50 offset:17664\n\t"
    "v_mfma_f32_16x16x32_bf16 v[228:231], a[184:187], v[16:19], v[228:231]\n\t"
    "ds_read_b64_tr_b16 a[232:233], v51 offset:17408\n\t"
    "ds_read_b64_tr_b16 a[234:235], v51 offset:17664\n\t"
    "v_mfma_f32_16x16x32_bf16 v[88:91], a[188:191], v[0:3], v[88:91]\n\t"
    "ds_read_b64_tr_b16 a[236:237], v50 offset:17920\n\t"
    "ds_read_b64_tr_b16 a[238:239], v50 offset:18176\n\t"
    "v_mfma_f32_16x16x32_bf16 v[120:123], a[188:191], v[4:7], v[120:123]\n\t"
    "buffer_load_dwordx4 v41, s[88:91], s66 offen offset:1024  lds\n\t"
    "v_mfma_f32_16x16x32_bf16 v[152:155], a[188:191], v[8:11], v[152:155]\n\t"
    "ds_read_b64_tr_b16 a[240:241], v51 offset:17920\n\t"
    "ds_read_b64_tr_b16 a[242:243], v51 offset:18176\n\t"
    "v_mfma_f32_16x16x32_bf16 v[184:187], a[188:191], v[12:15], v[184:187]\n\t"
    "v_add_u32 v50, s69, v50\n\t"
    "v_add_u32 v51, s69, v51\n\t"
    "v_mfma_f32_16x16x32_bf16 v[232:235], a[188:191], v[16:19], v[232:235]\n\t"
    "v_add_u32 v52, s69, v52\n\t"
    "v_add_u32 v53, s69, v53\n\t"
    "v_mfma_f32_16x16x32_bf16 v[92:95], a[192:195], v[0:3], v[92:95]\n\t"
    "v_mfma_f32_16x16x32_bf16 v[124:127], a[192:195], v[4:7], v[124:127]\n\t"
    "v_mfma_f32_16x16x32_bf16 v[156:159], a[192:195], v[8:11], v[156:159]\n\t"
    "v_mfma_f32_16x16x32_bf16 v[188:191], a[192:195], v[12:15], v[188:191]\n\t"
    "v_mfma_f32_16x16x32_bf16 v[236:239], a[192:195], v[16:19], v[236:239]\n\t"
    "buffer_load_dwordx4 v42, s[88:91], s66 offen offset:2048  lds\n\t"
    "v_mfma_f32_16x16x32_bf16 v[96:99], a[196:199], v[0:3], v[96:99]\n\t"
    "v_mfma_f32_16x16x32_bf16 v[128:131], a[196:199], v[4:7], v[128:131]\n\t"
    "v_mfma_f32_16x16x32_bf16 v[160:163], a[196:199], v[8:11], v[160:163]\n\t"
    "v_mfma_f32_16x16x32_bf16 v[192:195], a[196:199], v[12:15], v[192:195]\n\t"
    "v_mfma_f32_16x16x32_bf16 v[100:103], a[200:203], v[0:3], v[100:103]\n\t"
    "v_mfma_f32_16x16x32_bf16 v[132:135], a[200:203], v[4:7], v[132:135]\n\t"
    "v_mfma_f32_16x16x32_bf16 v[164:167], a[200:203], v[8:11], v[164:167]\n\t"
    "buffer_load_dwordx4 v43, s[88:91], s66 offen offset:3072  lds\n\t"
    "v_mfma_f32_16x16x32_bf16 v[196:199], a[200:203], v[12:15], v[196:199]\n\t"
    "v_mfma_f32_16x16x32_bf16 v[104:107], a[204:207], v[0:3], v[104:107]\n\t"
    "v_mfma_f32_16x16x32_bf16 v[136:139], a[204:207], v[4:7], v[136:139]\n\t"
    "s_add_u32 s66, s66, 0x40000\n\t"
    "v_mfma_f32_16x16x32_bf16 v[168:171], a[204:207], v[8:11], v[168:171]\n\t"
    "v_mfma_f32_16x16x32_bf16 v[200:203], a[204:207], v[12:15], v[200:203]\n\t"
    "v_mfma_f32_16x16x32_bf16 v[108:111], a[208:211], v[0:3], v[108:111]\n\t"
    "v_mfma_f32_16x16x32_bf16 v[140:143], a[208:211], v[4:7], v[140:143]\n\t"
    "s_waitcnt vmcnt(8) lgkmcnt(0)\n\t"
    "s_barrier\n\t"
    "v_mfma_f32_16x16x32_bf16 v[172:175], a[208:211], v[8:11], v[172:175]\n\t"
    "v_mfma_f32_16x16x32_bf16 v[204:207], a[208:211], v[12:15], v[204:207]\n\t"
    "v_mfma_f32_16x16x32_bf16 v[80:83], a[212:215], v[20:23], v[80:83]\n\t"
    "s_add_u32 m0, s68, s71\n\t"
    "ds_read_b128 v[0:3], v52 offset:32768\n\t"
    "v_mfma_f32_16x16x32_bf16 v[112:115], a[212:215], v[24:27], v[112:115]\n\t"
    "ds_read_b128 v[4:7], v52 offset:34816\n\t"
    "ds_read_b128 v[8:11], v52 offset:36864\n\t"
    "v_mfma_f32_16x16x32_bf16 v[144:147], a[212:215], v[28:31], v[144:147]\n\t"
    "ds_read_b128 v[12:15], v52 offset:38912\n\t"
    "ds_read_b128 v[16:19], v52 offset:49152\n\t"
    "v_mfma_f32_16x16x32_bf16 v[176:179], a[212:215], v[32:35], v[176:179]\n\t"
    "ds_read_b64_tr_b16 a[180:181], v50 offset:0\n\t"
    "ds_read_b64_tr_b16 a[182:183], v50 offset:256\n\t"
    "v_mfma_f32_16x16x32_bf16 v[224:227], a[212:215], v[36:39], v[224:227]\n\t"
    "buffer_load_dwordx4 v44, s[92:95], s67 offen offset:0  lds\n\t"
    "v_mfma_f32_16x16x32_bf16 v[84:87], a[216:219], v[20:23], v[84:87]\n\t"
    "ds_read_b64_tr_b16 a[184:185], v51 offset:0\n\t"
    "ds_read_b64_tr_b16 a[186:187], v51 offset:256\n\t"
    "v_mfma_f32_16x16x32_bf16 v[116:119], a[216:219], v[24:27], v[116:119]\n\t"
    "ds_read_b64_tr_b16 a[188:189], v50 offset:512\n\t"
    "ds_read_b64_tr_b16 a[190:191], v50 offset:768\n\t"
    "v_mfma_f32_16x16x32_bf16 v[148:151], a[216:219], v[28:31], v[148:151]\n\t"
    "buffer_load_dwordx4 v45, s[92:95], s67 offen offset:1024  lds\n\t"
    "v_mfma_f32_16x16x32_bf16 v[180:183], a[216:219], v[32:35], v[180:183]\n\t"
    "ds_read_b64_tr_b16 a[192:193], v51 offset:512\n\t"
    "ds_read_b64_tr_b16 a[194:195], v51 offset:768\n\t"
    "v_mfma_f32_16x16x32_bf16 v[228:231], a[216:219], v[36:39], v[228:231]\n\t"
    "ds_read_b64_tr_b16 a[196:197], v50 offset:1024\n\t"
    "ds_read_b64_tr_b16 a[198:199], v50 offset:1280\n\t"
    "v_mfma_f32_16x16x32_bf16 v[88:91], a[220:223], v[20:23], v[88:91]\n\t"
    "buffer_load_dwordx4 v46, s[92:95], s67 offen offset:2048  lds\n\t"
    "v_mfma_f32_16x16x32_bf16 v[120:123], a[220:223], v[24:27], v[120:123]\n\t"
    "ds_read_b64_tr_b16 a[200:201], v51 offset:1024\n\t"
    "ds_read_b64_tr_b16 a[202:203], v51 offset:1280\n\t"
    "v_mfma_f32_16x16x32_bf16 v[152:155], a[220:223], v[28:31], v[152:155]\n\t"
    "ds_read_b64_tr_b16 a[204:205], v50 offset:1536\n\t"
    "ds_read_b64_tr_b16 a[206:207], v50 offset:1792\n\t"
    "v_mfma_f32_16x16x32_bf16 v[184:187], a[220:223], v[32:35], v[184:187]\n\t"
    "ds_read_b64_tr_b16 a[208:209], v51 offset:1536\n\t"
    "ds_read_b64_tr_b16 a[210:211], v51 offset:1792\n\t"
    "v_mfma_f32_16x16x32_bf16 v[232:235], a[220:223], v[36:39], v[232:235]\n\t"
    "buffer_load_dwordx4 v47, s[92:95], s67 offen offset:3072  lds\n\t"
    "v_mfma_f32_16x16x32_bf16 v[92:95], a[224:227], v[20:23], v[92:95]\n\t"
    "s_add_u32 m0, m0, 4096\n\t"
    "v_mfma_f32_16x16x32_bf16 v[124:127], a[224:227], v[24:27], v[124:127]\n\t"
    "buffer_load_dwordx4 v48, s[92:95], s67 offen offset:0  lds\n\t"
    "v_mfma_f32_16x16x32_bf16 v[156:159], a[224:227], v[28:31], v[156:159]\n\t"
    "s_add_u32 s52, s68, s70\n\t"
    "s_add_u32 m0, s52, 16384\n\t"
    "v_mfma_f32_16x16x32_bf16 v[188:191], a[224:227], v[32:35], v[188:191]\n\t"
    "v_mfma_f32_16x16x32_bf16 v[236:239], a[224:227], v[36:39], v[236:239]\n\t"
    "v_mfma_f32_16x16x32_bf16 v[96:99], a[228:231], v[20:23], v[96:99]\n\t"
    "buffer_load_dwordx4 v40, s[88:91], s66 offen offset:0  lds\n\t"
    "v_mfma_f32_16x16x32_bf16 v[128:131], a[228:231], v[24:27], v[128:131]\n\t"
    "v_mfma_f32_16x16x32_bf16 v[160:163], a[228:231], v[28:31], v[160:163]\n\t"
    "v_mfma_f32_16x16x32_bf16 v[192:195], a[228:231], v[32:35], v[192:195]\n\t"
    "v_mfma_f32_16x16x32_bf16 v[100:103], a[232:235], v[20:23], v[100:103]\n\t"
    "buffer_load_dwordx4 v41, s[88:91], s66 offen offset:1024  lds\n\t"
    "v_mfma_f32_16x16x32_bf16 v[132:135], a[232:235], v[24:27], v[132:135]\n\t"
    "v_mfma_f32_16x16x32_bf16 v[164:167], a[232:235], v[28:31], v[164:167]\n\t"
    "buffer_load_dwordx4 v42, s[88:91], s66 offen offset:2048  lds\n\t"
    "v_mfma_f32_16x16x32_bf16 v[196:199], a[232:235], v[32:35], v[196:199]\n\t"
    "v_mfma_f32_16x16x32_bf16 v[104:107], a[236:239], v[20:23], v[104:107]\n\t"
    "buffer_load_dwordx4 v43, s[88:91], s66 offen offset:3072  lds\n\t"
    "v_mfma_f32_16x16x32_bf16 v[136:139], a[236:239], v[24:27], v[136:139]\n\t"
    "s_add_u32 s68, s68, s69\n\t"
    "s_sub_u32 s69, 0, s69\n\t"
    "v_mfma_f32_16x16x32_bf16 v[168:171], a[236:239], v[28:31], v[168:171]\n\t"
    "s_and_b32 s67, s97, 15\n\t"
    "s_lshr_b32 s97, s97, 4\n\t"
    "v_mfma_f32_16x16x32_bf16 v[200:203], a[236:239], v[32:35], v[200:203]\n\t"
    "s_lshl_b32 s66, s67, 19\n\t"
    "s_lshl_b32 s67, s67, 7\n\t"
    "v_mfma_f32_16x16x32_bf16 v[108:111], a[240:243], v[20:23], v[108:111]\n\t"
    "s_lshr_b32 s99, s99, 2\n\t"
    "v_mfma_f32_16x16x32_bf16 v[140:143], a[240:243], v[24:27], v[140:143]\n\t"
    "s_waitcnt vmcnt(13) lgkmcnt(0)\n\t"
    "s_barrier\n\t"
    "v_mfma_f32_16x16x32_bf16 v[172:175], a[240:243], v[28:31], v[172:175]\n\t"
    "v_mfma_f32_16x16x32_bf16 v[204:207], a[240:243], v[32:35], v[204:207]\n\t"
    "s_bitcmp1_b32 s99, 0\n\t"
    "s_cbranch_scc1 QL00_%=\n\t"
    "s_cmp_eq_u32 s99, 0\n\t"
    "s_cbranch_scc1 QT0_%=\n\t"
    "QL01_%=:\n\t"
    "v_mfma_f32_16x16x32_bf16 v[80:83], a[180:183], v[0:3], v[80:83]\n\t"
    "s_mov_b32 m0, s41\n\t"
    "ds_read_b128 v[20:23], v53 offset:32768\n\t"
    "v_mfma_f32_16x16x32_bf16 v[112:115], a[180:183], v[4:7], v[112:115]\n\t"
    "ds_read_b128 v[24:27], v53 offset:34816\n\t"
    "ds_read_b128 v[28:31], v53 offset:36864\n\t"
    "v_mfma_f32_16x16x32_bf16 v[144:147], a[180:183], v[8:11], v[144:147]\n\t"
    "ds_read_b128 v[32:35], v53 offset:38912\n\t"
    "ds_read_b128 v[36:39], v53 offset:49152\n\t"
    "v_mfma_f32_16x16x32_bf16 v[176:179], a[180:183], v[12:15], v[176:179]\n\t"
    "ds_read_b64_tr_b16 a[212:213], v50 offset:16384\n\t"
    "ds_read_b64_tr_b16 a[214:215], v50 offset:16640\n\t"
    "v_mfma_f32_16x16x32_bf16 v[224:227], a[180:183], v[16:19], v[224:227]\n\t"
    "buffer_load_dwordx4 v214, s[36:39], s40 offen offset:0 nt lds\n\t"
    "v_mfma_f32_16x16x32_bf16 v[84:87], a[184:187], v[0:3], v[84:87]\n\t"
    "ds_read_b64_tr_b16 a[216:217], v51 offset:16384\n\t"
    "ds_read_b64_tr_b16 a[218:219], v51 offset:16640\n\t"
    "v_mfma_f32_16x16x32_bf16 v[116:119], a[184:187], v[4:7], v[116:119]\n\t"
    "ds_read_b64_tr_b16 a[220:221], v50 offset:16896\n\t"
    "ds_read_b64_tr_b16 a[222:223], v50 offset:17152\n\t"
    "v_mfma_f32_16x16x32_bf16 v[148:151], a[184:187], v[8:11], v[148:151]\n\t"
    "buffer_load_dwordx4 v215, s[36:39], s40 offen offset:1024 nt lds\n\t"
    "v_mfma_f32_16x16x32_bf16 v[180:183], a[184:187], v[12:15], v[180:183]\n\t"
    "ds_read_b64_tr_b16 a[224:225], v51 offset:16896\n\t"
    "ds_read_b64_tr_b16 a[226:227], v51 offset:17152\n\t"
    "v_mfma_f32_16x16x32_bf16 v[228:231], a[184:187], v[16:19], v[228:231]\n\t"
    "ds_read_b64_tr_b16 a[228:229], v50 offset:17408\n\t"
    "ds_read_b64_tr_b16 a[230:231], v50 offset:17664\n\t"
    "v_mfma_f32_16x16x32_bf16 v[88:91], a[188:191], v[0:3], v[88:91]\n\t"
    "ds_read_b64_tr_b16 a[232:233], v51 offset:17408\n\t"
    "ds_read_b64_tr_b16 a[234:235], v51 offset:17664\n\t"
    "v_mfma_f32_16x16x32_bf16 v[120:123], a[188:191], v[4:7], v[120:123]\n\t"
    "buffer_load_dwordx4 v216, s[36:39], s40 offen offset:2048 nt lds\n\t"
    "v_mfma_f32_16x16x32_bf16 v[152:155], a[188:191], v[8:11], v[152:155]\n\t"
    "ds_read_b64_tr_b16 a[236:237], v50 offset:17920\n\t"
    "ds_read_b64_tr_b16 a[238:239], v50 offset:18176\n\t"
    "v_mfma_f32_16x16x32_bf16 v[184:187], a[188:191], v[12:15], v[184:187]\n\t"
    "buffer_load_dwordx4 v217, s[36:39], s40 offen offset:3072 nt lds\n\t"
    "v_mfma_f32_16x16x32_bf16 v[232:235], a[188:191], v[16:19], v[232:235]\n\t"
    "s_add_u32 s41, s41, s58\n\t"
    "s_sub_u32 s58, 0x8000, s58\n\t"
    "v_mfma_f32_16x16x32_bf16 v[92:95], a[192:195], v[0:3], v[92:95]\n\t"
    "s_cmp_eq_u32 s41, s43\n\t"
    "s_cselect_b32 s41, s42, s41\n\t"
    "v_mfma_f32_16x16x32_bf16 v[124:127], a[192:195], v[4:7], v[124:127]\n\t"
    "s_add_u32 s40, s40, s59\n\t"
    "s_add_u32 m0, s68, s70\n\t"
    "v_mfma_f32_16x16x32_bf16 v[156:159], a[192:195], v[8:11], v[156:159]\n\t"
    "ds_read_b64_tr_b16 a[240:241], v51 offset:17920\n\t"
    "ds_read_b64_tr_b16 a[242:243], v51 offset:18176\n\t"
    "v_mfma_f32_16x16x32_bf16 v[188:191], a[192:195], v[12:15], v[188:191]\n\t"
    "v_add_u32 v50, s69, v50\n\t"
    "v_add_u32 v51, s69, v51\n\t"
    "v_mfma_f32_16x16x32_bf16 v[236:239], a[192:195], v[16:19], v[236:239]\n\t"
    "buffer_load_dwordx4 v40, s[88:91], s66 offen offset:0  lds\n\t"
    "v_mfma_f32_16x16x32_bf16 v[96:99], a[196:199], v[0:3], v[96:99]\n\t"
    "v_add_u32 v52, s69, v52\n\t"
    "v_add_u32 v53, s69, v53\n\t"
    "v_mfma_f32_16x16x32_bf16 v[128:131], a[196:199], v[4:7], v[128:131]\n\t"
    "v_mfma_f32_16x16x32_bf16 v[160:163], a[196:199], v[8:11], v[160:163]\n\t"
    "buffer_load_dwordx4 v41, s[88:91], s66 offen offset:1024  lds\n\t"
    "v_mfma_f32_16x16x32_bf16 v[192:195], a[196:199], v[12:15], v[192:195]\n\t"
    "v_mfma_f32_16x16x32_bf16 v[100:103], a[200:203], v[0:3], v[100:103]\n\t"
    "v_mfma_f32_16x16x32_bf16 v[132:135], a[200:203], v[4:7], v[132:135]\n\t"
    "v_mfma_f32_16x16x32_bf16 v[164:167], a[200:203], v[8:11], v[164:167]\n\t"
    "buffer_load_dwordx4 v42, s[88:91], s66 offen offset:2048  lds\n\t"
    "v_mfma_f32_16x16x32_bf16 v[196:199], a[200:203], v[12:15], v[196:199]\n\t"
    "v_mfma_f32_16x16x32_bf16 v[104:107], a[204:207], v[0:3], v[104:107]\n\t"
    "v_mfma_f32_16x16x32_bf16 v[136:139], a[204:207], v[4:7], v[136:139]\n\t"
    "v_mfma_f32_16x16x32_bf16 v[168:171], a[204:207], v[8:11], v[168:171]\n\t"
    "buffer_load_dwordx4 v43, s[88:91], s66 offen offset:3072  lds\n\t"
    "v_mfma_f32_16x16x32_bf16 v[200:203], a[204:207], v[12:15], v[200:203]\n\t"
    "s_add_u32 s66, s66, 0x40000\n\t"
    "v_mfma_f32_16x16x32_bf16 v[108:111], a[208:211], v[0:3], v[108:111]\n\t"
    "v_mfma_f32_16x16x32_bf16 v[140:143], a[208:211], v[4:7], v[140:143]\n\t"
    "s_waitcnt vmcnt(12) lgkmcnt(0)\n\t"
    "s_barrier\n\t"
    "v_mfma_f32_16x16x32_bf16 v[172:175], a[208:211], v[8:11], v[172:175]\n\t"
    "v_mfma_f32_16x16x32_bf16 v[204:207], a[208:211], v[12:15], v[204:207]\n\t"
    "v_mfma_f32_16x16x32_bf16 v[80:83], a[212:215], v[20:23], v[80:83]\n\t"
    "s_add_u32 m0, s68, s71\n\t"
    "ds_read_b128 v[0:3], v52 offset:32768\n\t"
    "v_mfma_f32_16x16x32_bf16 v[112:115], a[212:215], v[24:27], v[112:115]\n\t"
    "ds_read_b128 v[4:7], v52 offset:34816\n\t"
    "ds_read_b128 v[8:11], v52 offset:36864\n\t"
    "v_mfma_f32_16x16x32_bf16 v[144:147], a[212:215], v[28:31], v[144:147]\n\t"
    "ds_read_b128 v[12:15], v52 offset:38912\n\t"
    "ds_read_b128 v[16:19], v52 offset:49152\n\t"
    "v_mfma_f32_16x16x32_bf16 v[176:179], a[212:215], v[32:35], v[176:179]\n\t"
    "ds_read_b64_tr_b16 a[180:181], v50 offset:0\n\t"
    "ds_read_b64_tr_b16 a[182:183], v50 offset:256\n\t"
    "v_mfma_f32_16x16x32_bf16 v[224:227], a[212:215], v[36:39], v[224:227]\n\t"
    "buffer_load_dwordx4 v44, s[92:95], s67 offen offset:0  lds\n\t"
    "v_mfma_f32_16x16x32_bf16 v[84:87], a[216:219], v[20:23], v[84:87]\n\t"
    "ds_read_b64_tr_b16 a[184:185], v51 offset:0\n\t"
    "ds_read_b64_tr_b16 a[186:187], v51 offset:256\n\t"
    "v_mfma_f32_16x16x32_bf16 v[116:119], a[216:219], v[24:27], v[116:119]\n\t"
    "ds_read_b64_tr_b16 a[188:189], v50 offset:512\n\t"
    "ds_read_b64_tr_b16 a[190:191], v50 offset:768\n\t"
    "v_mfma_f32_16x16x32_bf16 v[148:151], a[216:219], v[28:31], v[148:151]\n\t"
    "buffer_load_dwordx4 v45, s[92:95], s67 offen offset:1024  lds\n\t"
    "v_mfma_f32_16x16x32_bf16 v[180:183], a[216:219], v[32:35], v[180:183]\n\t"
    "ds_read_b64_tr_b16 a[192:193], v51 offset:512\n\t"
    "ds_read_b64_tr_b16 a[194:195], v51 offset:768\n\t"
    "v_mfma_f32_16x16x32_bf16 v[228:231], a[216:219], v[36:39], v[228:231]\n\t"
    "ds_read_b64_tr_b16 a[196:197], v50 offset:1024\n\t"
    "ds_read_b64_tr_b16 a[198:199], v50 offset:1280\n\t"
    "v_mfma_f32_16x16x32_bf16 v[88:91], a[220:223], v[20:23], v[88:91]\n\t"
    "buffer_load_dwordx4 v46, s[92:95], s67 offen offset:2048  lds\n\t"
    "v_mfma_f32_16x16x32_bf16 v[120:123], a[220:223], v[24:27], v[120:123]\n\t"
    "ds_read_b64_tr_b16 a[200:201], v51 offset:1024\n\t"
    "ds_read_b64_tr_b16 a[202:203], v51 offset:1280\n\t"
    "v_mfma_f32_16x16x32_bf16 v[152:155], a[220:223], v[28:31], v[152:155]\n\t"
    "ds_read_b64_tr_b16 a[204:205], v50 offset:1536\n\t"
    "ds_read_b64_tr_b16 a[206:207], v50 offset:1792\n\t"
    "v_mfma_f32_16x16x32_bf16 v[184:187], a[220:223], v[32:35], v[184:187]\n\t"
    "ds_read_b64_tr_b16 a[208:209], v51 offset:1536\n\t"
    "ds_read_b64_tr_b16 a[210:211], v51 offset:1792\n\t"
    "v_mfma_f32_16x16x32_bf16 v[232:235], a[220:223], v[36:39], v[232:235]\n\t"
    "buffer_load_dwordx4 v47, s[92:95], s67 offen offset:3072  lds\n\t"
    "v_mfma_f32_16x16x32_bf16 v[92:95], a[224:227], v[20:23], v[92:95]\n\t"
    "s_add_u32 m0, m0, 4096\n\t"
    "v_mfma_f32_16x16x32_bf16 v[124:127], a[224:227], v[24:27], v[124:127]\n\t"
    "buffer_load_dwordx4 v48, s[92:95], s67 offen offset:0  lds\n\t"
    "v_mfma_f32_16x16x32_bf16 v[156:159], a[224:227], v[28:31], v[156:159]\n\t"
    "s_add_u32 s52, s68, s70\n\t"
    "s_add_u32 m0, s52, 16384\n\t"
    "v_mfma_f32_16x16x32_bf16 v[188:191], a[224:227], v[32:35], v[188:191]\n\t"
    "v_mfma_f32_16x16x32_bf16 v[236:239], a[224:227], v[36:39], v[236:239]\n\t"
    "v_mfma_f32_16x16x32_bf16 v[96:99], a[228:231], v[20:23], v[96:99]\n\t"
    "buffer_load_dwordx4 v40, s[88:91], s66 offen offset:0  lds\n\t"
    "v_mfma_f32_16x16x32_bf16 v[128:131], a[228:231], v[24:27], v[128:131]\n\t"
    "v_mfma_f32_16x16x32_bf16 v[160:163], a[228:231], v[28:31], v[160:163]\n\t"
    "v_mfma_f32_16x16x32_bf16 v[192:195], a[228:231], v[32:35], v[192:195]\n\t"
    "v_mfma_f32_16x16x32_bf16 v[100:103], a[232:235], v[20:23], v[100:103]\n\t"
    "buffer_load_dwordx4 v41, s[88:91], s66 offen offset:1024  lds\n\t"
    "v_mfma_f32_16x16x32_bf16 v[132:135], a[232:235], v[24:27], v[132:135]\n\t"
    "v_mfma_f32_16x16x32_bf16 v[164:167], a[232:235], v[28:31], v[164:167]\n\t"
    "buffer_load_dwordx4 v42, s[88:91], s66 offen offset:2048  lds\n\t"
    "v_mfma_f32_16x16x32_bf16 v[196:199], a[232:235], v[32:35], v[196:199]\n\t"
    "v_mfma_f32_16x16x32_bf16 v[104:107], a[236:239], v[20:23], v[104:107]\n\t"
    "buffer_load_dwordx4 v43, s[88:91], s66 offen offset:3072  lds\n\t"
    "v_mfma_f32_16x16x32_bf16 v[136:139], a[236:239], v[24:27], v[136:139]\n\t"
    "s_add_u32 s68, s68, s69\n\t"
    "s_sub_u32 s69, 0, s69\n\t"
    "v_mfma_f32_16x16x32_bf16 v[168:171], a[236:239], v[28:31], v[168:171]\n\t"
    "s_and_b32 s67, s97, 15\n\t"
    "s_lshr_b32 s97, s97, 4\n\t"
    "v_mfma_f32_16x16x32_bf16 v[200:203], a[236:239], v[32:35], v[200:203]\n\t"
    "s_lshl_b32 s66, s67, 19\n\t"
    "s_lshl_b32 s67, s67, 7\n\t"
    "v_mfma_f32_16x16x32_bf16 v[108:111], a[240:243], v[20:23], v[108:111]\n\t"
    "s_lshr_b32 s99, s99, 2\n\t"
    "v_mfma_f32_16x16x32_bf16 v[140:143], a[240:243], v[24:27], v[140:143]\n\t"
    "s_waitcnt vmcnt(17) lgkmcnt(0)\n\t"
    "s_barrier\n\t"
    "v_mfma_f32_16x16x32_bf16 v[172:175], a[240:243], v[28:31], v[172:175]\n\t"
    "v_mfma_f32_16x16x32_bf16 v[204:207], a[240:243], v[32:35], v[204:207]\n\t"
    "s_bitcmp1_b32 s99, 0\n\t"
    "s_cbranch_scc1 QL00_%=\n\t"
    "s_cmp_eq_u32 s99, 0\n\t"
    "s_cbranch_scc0 QL01_%=\n\t"
    "QT0_%=:\n\t"
    "v_add_u32 v223, 1, v254\n\t"
    "s_mov_b32 exec_lo, 0x80402000\n\t"
    "s_mov_b32 exec_hi, 0x80402000\n\t"
    "global_store_dword v253, v223, s[60:61] sc1\n\t"
    "s_mov_b64 exec, -1\n\t"
    "v_mfma_f32_16x16x32_bf16 v[80:83], a[180:183], v[0:3], v[80:83]\n\t"
    "ds_read_b128 v[20:23], v53 offset:32768\n\t"
    "ds_read_b128 v[24:27], v53 offset:34816\n\t"
    "v_mfma_f32_16x16x32_bf16 v[112:115], a[180:183], v[4:7], v[112:115]\n\t"
    "ds_read_b128 v[28:31], v53 offset:36864\n\t"
    "ds_read_b128 v[32:35], v53 offset:38912\n\t"
    "v_mfma_f32_16x16x32_bf16 v[144:147], a[180:183], v[8:11], v[144:147]\n\t"
    "ds_read_b128 v[36:39], v53 offset:49152\n\t"
    "ds_read_b64_tr_b16 a[212:213], v50 offset:16384\n\t"
    "v_mfma_f32_16x16x32_bf16 v[176:179], a[180:183], v[12:15], v[176:179]\n\t"
    "ds_read_b64_tr_b16 a[214:215], v50 offset:16640\n\t"
    "ds_read_b64_tr_b16 a[216:217], v51 offset:16384\n\t"
    "v_mfma_f32_16x16x32_bf16 v[224:227], a[180:183], v[16:19], v[224:227]\n\t"
    "ds_read_b64_tr_b16 a[218:219], v51 offset:16640\n\t"
    "ds_read_b64_tr_b16 a[220:221], v50 offset:16896\n\t"
    "v_mfma_f32_16x16x32_bf16 v[84:87], a[184:187], v[0:3], v[84:87]\n\t"
    "ds_read_b64_tr_b16 a[222:223], v50 offset:17152\n\t"
    "ds_read_b64_tr_b16 a[224:225], v51 offset:16896\n\t"
    "v_mfma_f32_16x16x32_bf16 v[116:119], a[184:187], v[4:7], v[116:119]\n\t"
    "ds_read_b64_tr_b16 a[226:227], v51 offset:17152\n\t"
    "ds_read_b64_tr_b16 a[228:229], v50 offset:17408\n\t"
    "v_mfma_f32_16x16x32_bf16 v[148:151], a[184:187], v[8:11], v[148:151]\n\t"
    "ds_read_b64_tr_b16 a[230:231], v50 offset:17664\n\t"
    "ds_read_b64_tr_b16 a[232:233], v51 offset:17408\n\t"
    "v_mfma_f32_16x16x32_bf16 v[180:183], a[184:187], v[12:15], v[180:183]\n\t"
    "ds_read_b64_tr_b16 a[234:235], v51 offset:17664\n\t"
    "ds_read_b64_tr_b16 a[236:237], v50 offset:17920\n\t"
    "v_mfma_f32_16x16x32_bf16 v[228:231], a[184:187], v[16:19], v[228:231]\n\t"
    "ds_read_b64_tr_b16 a[238:239], v50 offset:18176\n\t"
    "ds_read_b64_tr_b16 a[240:241], v51 offset:17920\n\t"
    "v_mfma_f32_16x16x32_bf16 v[88:91], a[188:191], v[0:3], v[88:91]\n\t"
    "ds_read_b64_tr_b16 a[242:243], v51 offset:18176\n\t"
    "v_add_u32 v50, 51200, v50\n\t"
    "v_mfma_f32_16x16x32_bf16 v[120:123], a[188:191], v[4:7], v[120:123]\n\t"
    "v_add_u32 v51, 51200, v51\n\t"
    "v_add_u32 v52, 51200, v52\n\t"
    "v_mfma_f32_16x16x32_bf16 v[152:155], a[188:191], v[8:11], v[152:155]\n\t"
    "v_add_u32 v53, 51200, v53\n\t"
    "v_mfma_f32_16x16x32_bf16 v[184:187], a[188:191], v[12:15], v[184:187]\n\t"
    "v_mfma_f32_16x16x32_bf16 v[232:235], a[188:191], v[16:19], v[232:235]\n\t"
    "v_mfma_f32_16x16x32_bf16 v[92:95], a[192:195], v[0:3], v[92:95]\n\t"
    "v_mfma_f32_16x16x32_bf16 v[124:127], a[192:195], v[4:7], v[124:127]\n\t"
    "v_mfma_f32_16x16x32_bf16 v[156:159], a[192:195], v[8:11], v[156:159]\n\t"
    "v_mfma_f32_16x16x32_bf16 v[188:191], a[192:195], v[12:15], v[188:191]\n\t"
    "v_mfma_f32_16x16x32_bf16 v[236:239], a[192:195], v[16:19], v[236:239]\n\t"
    "v_mfma_f32_16x16x32_bf16 v[96:99], a[196:199], v[0:3], v[96:99]\n\t"
    "v_mfma_f32_16x16x32_bf16 v[128:131], a[196:199], v[4:7], v[128:131]\n\t"
    "v_mfma_f32_16x16x32_bf16 v[160:163], a[196:199], v[8:11], v[160:163]\n\t"
    "v_mfma_f32_16x16x32_bf16 v[192:195], a[196:199], v[12:15], v[192:195]\n\t"
    "v_mfma_f32_16x16x32_bf16 v[100:103], a[200:203], v[0:3], v[100:103]\n\t"
    "v_mfma_f32_16x16x32_bf16 v[132:135], a[200:203], v[4:7], v[132:135]\n\t"
    "v_mfma_f32_16x16x32_bf16 v[164:167], a[200:203], v[8:11], v[164:167]\n\t"
    "v_mfma_f32_16x16x32_bf16 v[196:199], a[200:203], v[12:15], v[196:199]\n\t"
    "v_mfma_f32_16x16x32_bf16 v[104:107], a[204:207], v[0:3], v[104:107]\n\t"
    "v_mfma_f32_16x16x32_bf16 v[136:139], a[204:207], v[4:7], v[136:139]\n\t"
    "v_mfma_f32_16x16x32_bf16 v[168:171], a[204:207], v[8:11], v[168:171]\n\t"
    "v_mfma_f32_16x16x32_bf16 v[200:203], a[204:207], v[12:15], v[200:203]\n\t"
    "v_mfma_f32_16x16x32_bf16 v[108:111], a[208:211], v[0:3], v[108:111]\n\t"
    "v_mfma_f32_16x16x32_bf16 v[140:143], a[208:211], v[4:7], v[140:143]\n\t"
    "s_waitcnt vmcnt(4) lgkmcnt(0)\n\t"
    "s_barrier\n\t"
    "v_mfma_f32_16x16x32_bf16 v[172:175], a[208:211], v[8:11], v[172:175]\n\t"
    "v_mfma_f32_16x16x32_bf16 v[204:207], a[208:211], v[12:15], v[204:207]\n\t"
    "v_mfma_f32_16x16x32_bf16 v[80:83], a[212:215], v[20:23], v[80:83]\n\t"
    "s_mov_b32 m0, s41\n\t"
    "ds_read_b128 v[0:3], v52 offset:32768\n\t"
    "v_mfma_f32_16x16x32_bf16 v[112:115], a[212:215], v[24:27], v[112:115]\n\t"
    "ds_read_b128 v[4:7], v52 offset:34816\n\t"
    "ds_read_b128 v[8:11], v52 offset:36864\n\t"
    "v_mfma_f32_16x16x32_bf16 v[144:147], a[212:215], v[28:31], v[144:147]\n\t"
    "ds_read_b128 v[12:15], v52 offset:38912\n\t"
    "ds_read_b128 v[16:19], v52 offset:49152\n\t"
    "v_mfma_f32_16x16x32_bf16 v[176:179], a[212:215], v[32:35], v[176:179]\n\t"
    "ds_read_b64_tr_b16 a[180:181], v50 offset:0\n\t"
    "ds_read_b64_tr_b16 a[182:183], v50 offset:256\n\t"
    "v_mfma_f32_16x16x32_bf16 v[224:227], a[212:215], v[36:39], v[224:227]\n\t"
    "buffer_load_dwordx4 v214, s[36:39], s40 offen offset:0 nt lds\n\t"
    "v_mfma_f32_16x16x32_bf16 v[84:87], a[216:219], v[20:23], v[84:87]\n\t"
    "ds_read_b64_tr_b16 a[184:185], v51 offset:0\n\t"
    "ds_read_b64_tr_b16 a[186:187], v51 offset:256\n\t"
    "v_mfma_f32_16x16x32_bf16 v[116:119], a[216:219], v[24:27], v[116:119]\n\t"
    "ds_read_b64_tr_b16 a[188:189], v50 offset:512\n\t"
    "ds_read_b64_tr_b16 a[190:191], v50 offset:768\n\t"
    "v_mfma_f32_16x16x32_bf16 v[148:151], a[216:219], v[28:31], v[148:151]\n\t"
    "buffer_load_dwordx4 v215, s[36:39], s40 offen offset:1024 nt lds\n\t"
    "v_mfma_f32_16x16x32_bf16 v[180:183], a[216:219], v[32:35], v[180:183]\n\t"
    "ds_read_b64_tr_b16 a[192:193], v51 offset:512\n\t"
    "ds_read_b64_tr_b16 a[194:195], v51 offset:768\n\t"
    "v_mfma_f32_16x16x32_bf16 v[228:231], a[216:219], v[36:39], v[228:231]\n\t"
    "ds_read_b64_tr_b16 a[196:197], v50 offset:1024\n\t"
    "ds_read_b64_tr_b16 a[198:199], v50 offset:1280\n\t"
    "v_mfma_f32_16x16x32_bf16 v[88:91], a[220:223], v[20:23], v[88:91]\n\t"
    "ds_read_b64_tr_b16 a[200:201], v51 offset:1024\n\t"
    "ds_read_b64_tr_b16 a[202:203], v51 offset:1280\n\t"
    "v_mfma_f32_16x16x32_bf16 v[120:123], a[220:223], v[24:27], v[120:123]\n\t"
    "buffer_load_dwordx4 v216, s[36:39], s40 offen offset:2048 nt lds\n\t"
    "v_mfma_f32_16x16x32_bf16 v[152:155], a[220:223], v[28:31], v[152:155]\n\t"
    "ds_read_b64_tr_b16 a[204:205], v50 offset:1536\n\t"
    "ds_read_b64_tr_b16 a[206:207], v50 offset:1792\n\t"
    "v_mfma_f32_16x16x32_bf16 v[184:187], a[220:223], v[32:35], v[184:187]\n\t"
    "buffer_load_dwordx4 v217, s[36:39], s40 offen offset:3072 nt lds\n\t"
    "v_mfma_f32_16x16x32_bf16 v[232:235], a[220:223], v[36:39], v[232:235]\n\t"
    "s_add_u32 s41, s41, s58\n\t"
    "s_sub_u32 s58, 0x8000, s58\n\t"
    "v_mfma_f32_16x16x32_bf16 v[92:95], a[224:227], v[20:23], v[92:95]\n\t"
    "s_cmp_eq_u32 s41, s43\n\t"
    "s_cselect_b32 s41, s42, s41\n\t"
    "v_mfma_f32_16x16x32_bf16 v[124:127], a[224:227], v[24:27], v[124:127]\n\t"
    "s_add_u32 s40, s40, s59\n\t"
    "s_mov_b32 m0, s41\n\t"
    "v_mfma_f32_16x16x32_bf16 v[156:159], a[224:227], v[28:31], v[156:159]\n\t"
    "ds_read_b64_tr_b16 a[208:209], v51 offset:1536\n\t"
    "ds_read_b64_tr_b16 a[210:211], v51 offset:1792\n\t"
    "v_mfma_f32_16x16x32_bf16 v[188:191], a[224:227], v[32:35], v[188:191]\n\t"
    "v_mfma_f32_16x16x32_bf16 v[236:239], a[224:227], v[36:39], v[236:239]\n\t"
    "buffer_load_dwordx4 v214, s[36:39], s40 offen offset:0 nt lds\n\t"
    "v_mfma_f32_16x16x32_bf16 v[96:99], a[228:231], v[20:23], v[96:99]\n\t"
    "v_mfma_f32_16x16x32_bf16 v[128:131], a[228:231], v[24:27], v[128:131]\n\t"
    "v_mfma_f32_16x16x32_bf16 v[160:163], a[228:231], v[28:31], v[160:163]\n\t"
    "buffer_load_dwordx4 v215, s[36:39], s40 offen offset:1024 nt lds\n\t"
    "v_mfma_f32_16x16x32_bf16 v[192:195], a[228:231], v[32:35], v[192:195]\n\t"
    "v_mfma_f32_16x16x32_bf16 v[100:103], a[232:235], v[20:23], v[100:103]\n\t"
    "v_mfma_f32_16x16x32_bf16 v[132:135], a[232:235], v[24:27], v[132:135]\n\t"
    "v_mfma_f32_16x16x32_bf16 v[164:167], a[232:235], v[28:31], v[164:167]\n\t"
    "buffer_load_dwordx4 v216, s[36:39], s40 offen offset:2048 nt lds\n\t"
    "v_mfma_f32_16x16x32_bf16 v[196:199], a[232:235], v[32:35], v[196:199]\n\t"
    "v_mfma_f32_16x16x32_bf16 v[104:107], a[236:239], v[20:23], v[104:107]\n\t"
    "v_mfma_f32_16x16x32_bf16 v[136:139], a[236:239], v[24:27], v[136:139]\n\t"
    "buffer_load_dwordx4 v217, s[36:39], s40 offen offset:3072 nt lds\n\t"
    "v_mfma_f32_16x16x32_bf16 v[168:171], a[236:239], v[28:31], v[168:171]\n\t"
    "s_add_u32 s41, s41, s58\n\t"
    "s_sub_u32 s58, 0x8000, s58\n\t"
    "v_mfma_f32_16x16x32_bf16 v[200:203], a[236:239], v[32:35], v[200:203]\n\t"
    "s_cmp_eq_u32 s41, s43\n\t"
    "s_cselect_b32 s41, s42, s41\n\t"
    "v_mfma_f32_16x16x32_bf16 v[108:111], a[240:243], v[20:23], v[108:111]\n\t"
    "s_add_u32 s40, s40, s59\n\t"
    "v_mfma_f32_16x16x32_bf16 v[140:143], a[240:243], v[24:27], v[140:143]\n\t"
    "s_waitcnt vmcnt(8) lgkmcnt(0)\n\t"
    "s_barrier\n\t"
    "v_mfma_f32_16x16x32_bf16 v[172:175], a[240:243], v[28:31], v[172:175]\n\t"
    "v_mfma_f32_16x16x32_bf16 v[204:207], a[240:243], v[32:35], v[204:207]\n\t"
    "v_mfma_f32_16x16x32_bf16 v[80:83], a[180:183], v[0:3], v[80:83]\n\t"
    "s_mov_b32 m0, s41\n\t"
    "ds_read_b128 v[20:23], v53 offset:32768\n\t"
    "v_mfma_f32_16x16x32_bf16 v[112:115], a[180:183], v[4:7], v[112:115]\n\t"
    "ds_read_b128 v[24:27], v53 offset:34816\n\t"
    "ds_read_b128 v[28:31], v53 offset:36864\n\t"
    "v_mfma_f32_16x16x32_bf16 v[144:147], a[180:183], v[8:11], v[144:147]\n\t"
    "ds_read_b128 v[32:35], v53 offset:38912\n\t"
    "ds_read_b128 v[36:39], v53 offset:49152\n\t"
    "v_mfma_f32_16x16x32_bf16 v[176:179], a[180:183], v[12:15], v[176:179]\n\t"
    "ds_read_b64_tr_b16 a[212:213], v50 offset:16384\n\t"
    "ds_read_b64_tr_b16 a[214:215], v50 offset:16640\n\t"
    "v_mfma_f32_16x16x32_bf16 v[224:227], a[180:183], v[16:19], v[224:227]\n\t"
    "buffer_load_dwordx4 v214, s[36:39], s40 offen offset:0 nt lds\n\t"
    "v_mfma_f32_16x16x32_bf16 v[84:87], a[184:187], v[0:3], v[84:87]\n\t"
    "ds_read_b64_tr_b16 a[216:217], v51 offset:16384\n\t"
    "ds_read_b64_tr_b16 a[218:219], v51 offset:16640\n\t"
    "v_mfma_f32_16x16x32_bf16 v[116:119], a[184:187], v[4:7], v[116:119]\n\t"
    "ds_read_b64_tr_b16 a[220:221], v50 offset:16896\n\t"
    "ds_read_b64_tr_b16 a[222:223], v50 offset:17152\n\t"
    "v_mfma_f32_16x16x32_bf16 v[148:151], a[184:187], v[8:11], v[148:151]\n\t"
    "buffer_load_dwordx4 v215, s[36:39], s40 offen offset:1024 nt lds\n\t"
    "v_mfma_f32_16x16x32_bf16 v[180:183], a[184:187], v[12:15], v[180:183]\n\t"
    "ds_read_b64_tr_b16 a[224:225], v51 offset:16896\n\t"
    "ds_read_b64_tr_b16 a[226:227], v51 offset:17152\n\t"
    "v_mfma_f32_16x16x32_bf16 v[228:231], a[184:187], v[16:19], v[228:231]\n\t"
    "ds_read_b64_tr_b16 a[228:229], v50 offset:17408\n\t"
    "ds_read_b64_tr_b16 a[230:231], v50 offset:17664\n\t"
    "v_mfma_f32_16x16x32_bf16 v[88:91], a[188:191], v[0:3], v[88:91]\n\t"
    "ds_read_b64_tr_b16 a[232:233], v51 offset:17408\n\t"
    "ds_read_b64_tr_b16 a[234:235], v51 offset:17664\n\t"
    "v_mfma_f32_16x16x32_bf16 v[120:123], a[188:191], v[4:7], v[120:123]\n\t"
    "buffer_load_dwordx4 v216, s[36:39], s40 offen offset:2048 nt lds\n\t"
    "v_mfma_f32_16x16x32_bf16 v[152:155], a[188:191], v[8:11], v[152:155]\n\t"
    "ds_read_b64_tr_b16 a[236:237], v50 offset:17920\n\t"
    "ds_read_b64_tr_b16 a[238:239], v50 offset:18176\n\t"
    "v_mfma_f32_16x16x32_bf16 v[184:187], a[188:191], v[12:15], v[184:187]\n\t"
    "buffer_load_dwordx4 v217, s[36:39], s40 offen offset:3072 nt lds\n\t"
    "v_mfma_f32_16x16x32_bf16 v[232:235], a[188:191], v[16:19], v[232:235]\n\t"
    "s_add_u32 s41, s41, s58\n\t"
    "s_sub_u32 s58, 0x8000, s58\n\t"
    "v_mfma_f32_16x16x32_bf16 v[92:95], a[192:195], v[0:3], v[92:95]\n\t"
    "s_cmp_eq_u32 s41, s43\n\t"
    "s_cselect_b32 s41, s42, s41\n\t"
    "v_mfma_f32_16x16x32_bf16 v[124:127], a[192:195], v[4:7], v[124:127]\n\t"
    "s_add_u32 s40, s40, s59\n\t"
    "s_mov_b32 m0, s41\n\t"
    "v_mfma_f32_16x16x32_bf16 v[156:159], a[192:195], v[8:11], v[156:159]\n\t"
    "ds_read_b64_tr_b16 a[240:241], v51 offset:17920\n\t"
    "ds_read_b64_tr_b16 a[242:243], v51 offset:18176\n\t"
    "v_mfma_f32_16x16x32_bf16 v[188:191], a[192:195], v[12:15], v[188:191]\n\t"
    "v_mfma_f32_16x16x32_bf16 v[236:239], a[192:195], v[16:19], v[236:239]\n\t"
    "buffer_load_dwordx4 v214, s[36:39], s40 offen offset:0 nt lds\n\t"
    "v_mfma_f32_16x16x32_bf16 v[96:99], a[196:199], v[0:3], v[96:99]\n\t"
    "v_mfma_f32_16x16x32_bf16 v[128:131], a[196:199], v[4:7], v[128:131]\n\t"
    "v_mfma_f32_16x16x32_bf16 v[160:163], a[196:199], v[8:11], v[160:163]\n\t"
    "buffer_load_dwordx4 v215, s[36:39], s40 offen offset:1024 nt lds\n\t"
    "v_mfma_f32_16x16x32_bf16 v[192:195], a[196:199], v[12:15], v[192:195]\n\t"
    "v_mfma_f32_16x16x32_bf16 v[100:103], a[200:203], v[0:3], v[100:103]\n\t"
    "v_mfma_f32_16x16x32_bf16 v[132:135], a[200:203], v[4:7], v[132:135]\n\t"
    "v_mfma_f32_16x16x32_bf16 v[164:167], a[200:203], v[8:11], v[164:167]\n\t"
    "buffer_load_dwordx4 v216, s[36:39], s40 offen offset:2048 nt lds\n\t"
    "v_mfma_f32_16x16x32_bf16 v[196:199], a[200:203], v[12:15], v[196:199]\n\t"
    "v_mfma_f32_16x16x32_bf16 v[104:107], a[204:207], v[0:3], v[104:107]\n\t"
    "v_mfma_f32_16x16x32_bf16 v[136:139], a[204:207], v[4:7], v[136:139]\n\t"
    "buffer_load_dwordx4 v217, s[36:39], s40 offen offset:3072 nt lds\n\t"
    "v_mfma_f32_16x16x32_bf16 v[168:171], a[204:207], v[8:11], v[168:171]\n\t"
    "s_add_u32 s41, s41, s58\n\t"
    "s_sub_u32 s58, 0x8000, s58\n\t"
    "v_mfma_f32_16x16x32_bf16 v[200:203], a[204:207], v[12:15], v[200:203]\n\t"
    "s_cmp_eq_u32 s41, s43\n\t"
    "s_cselect_b32 s41, s42, s41\n\t"
    "v_mfma_f32_16x16x32_bf16 v[108:111], a[208:211], v[0:3], v[108:111]\n\t"
    "s_add_u32 s40, s40, s59\n\t"
    "v_mfma_f32_16x16x32_bf16 v[140:143], a[208:211], v[4:7], v[140:143]\n\t"
    "s_waitcnt lgkmcnt(0)\n\t"
    "s_barrier\n\t"
    "v_mfma_f32_16x16x32_bf16 v[172:175], a[208:211], v[8:11], v[172:175]\n\t"
    "v_mfma_f32_16x16x32_bf16 v[204:207], a[208:211], v[12:15], v[204:207]\n\t"
    "v_mfma_f32_16x16x32_bf16 v[224:227], a[212:215], v[36:39], v[224:227]\n\t"
    "v_mfma_f32_16x16x32_bf16 v[228:231], a[216:219], v[36:39], v[228:231]\n\t"
    "v_mfma_f32_16x16x32_bf16 v[232:235], a[220:223], v[36:39], v[232:235]\n\t"
    "v_mfma_f32_16x16x32_bf16 v[236:239], a[224:227], v[36:39], v[236:239]\n\t"
    "v_mfma_f32_16x16x32_bf16 v[176:179], a[212:215], v[32:35], v[176:179]\n\t"
    "v_mfma_f32_16x16x32_bf16 v[180:183], a[216:219], v[32:35], v[180:183]\n\t"
    "v_mul_f32 v224, s64, v224\n\t"
    "v_mul_f32 v225, s64, v225\n\t"
    "v_mfma_f32_16x16x32_bf16 v[184:187], a[220:223], v[32:35], v[184:187]\n\t"
    "v_mul_f32 v226, s64, v226\n\t"
    "v_mul_f32 v227, s64, v227\n\t"
    "v_mfma_f32_16x16x32_bf16 v[188:191], a[224:227], v[32:35], v[188:191]\n\t"
    "v_mul_f32 v228, s64, v228\n\t"
    "v_mul_f32 v229, s64, v229\n\t"
    "v_mfma_f32_16x16x32_bf16 v[192:195], a[228:231], v[32:35], v[192:195]\n\t"
    "v_mul_f32 v230, s64, v230\n\t"
    "v_mul_f32 v231, s64, v231\n\t"
    "v_mfma_f32_16x16x32_bf16 v[196:199], a[232:235], v[32:35], v[196:199]\n\t"
    "v_cvt_pk_bf16_f32 v64, v224, v225\n\t"
    "v_cvt_pk_bf16_f32 v65, v226, v227\n\t"
    "v_mfma_f32_16x16x32_bf16 v[200:203], a[236:239], v[32:35], v[200:203]\n\t"
    "v_cvt_pk_bf16_f32 v66, v228, v229\n\t"
    "v_cvt_pk_bf16_f32 v67, v230, v231\n\t"
    "v_mfma_f32_16x16x32_bf16 v[204:207], a[240:243], v[32:35], v[204:207]\n\t"
    "v_mul_f32 v232, s64, v232\n\t"
    "v_mul_f32 v233, s64, v233\n\t"
    "v_mfma_f32_16x16x32_bf16 v[80:83], a[212:215], v[20:23], v[80:83]\n\t"
    "v_mul_f32 v234, s64, v234\n\t"
    "v_mul_f32 v235, s64, v235\n\t"
    "v_mfma_f32_16x16x32_bf16 v[84:87], a[216:219], v[20:23], v[84:87]\n\t"
    "v_mul_f32 v236, s64, v236\n\t"
    "v_mul_f32 v237, s64, v237\n\t"
    "v_mfma_f32_16x16x32_bf16 v[88:91], a[220:223], v[20:23], v[88:91]\n\t"
    "v_mul_f32 v238, s64, v238\n\t"
    "v_mul_f32 v239, s64, v239\n\t"
    "v_mfma_f32_16x16x32_bf16 v[92:95], a[224:227], v[20:23], v[92:95]\n\t"
    "v_cvt_pk_bf16_f32 v68, v232, v233\n\t"
    "v_cvt_pk_bf16_f32 v69, v234, v235\n\t"
    "v_mfma_f32_16x16x32_bf16 v[96:99], a[228:231], v[20:23], v[96:99]\n\t"
    "v_cvt_pk_bf16_f32 v70, v236, v237\n\t"
    "v_cvt_pk_bf16_f32 v71, v238, v239\n\t"
    "v_mfma_f32_16x16x32_bf16 v[100:103], a[232:235], v[20:23], v[100:103]\n\t"
    "v_permlane16_swap_b32 v64, v66\n\t"
    "v_permlane16_swap_b32 v65, v67\n\t"
    "v_mfma_f32_16x16x32_bf16 v[104:107], a[236:239], v[20:23], v[104:107]\n\t"
    "v_permlane16_swap_b32 v68, v70\n\t"
    "v_permlane16_swap_b32 v69, v71\n\t"
    "v_mfma_f32_16x16x32_bf16 v[108:111], a[240:243], v[20:23], v[108:111]\n\t"
    "ds_write_b128 v245, v[64:67] offset:0\n\t"
    "v_mfma_f32_16x16x32_bf16 v[144:147], a[212:215], v[28:31], v[144:147]\n\t"
    "ds_write_b128 v245, v[68:71] offset:1024\n\t"
    "v_mfma_f32_16x16x32_bf16 v[148:151], a[216:219], v[28:31], v[148:151]\n\t"
    "v_mul_f32 v176, s64, v176\n\t"
    "v_mul_f32 v177, s64, v177\n\t"
    "v_mfma_f32_16x16x32_bf16 v[152:155], a[220:223], v[28:31], v[152:155]\n\t"
    "v_mul_f32 v178, s64, v178\n\t"
    "v_mul_f32 v179, s64, v179\n\t"
    "v_mfma_f32_16x16x32_bf16 v[156:159], a[224:227], v[28:31], v[156:159]\n\t"
    "v_mul_f32 v180, s64, v180\n\t"
    "v_mul_f32 v181, s64, v181\n\t"
    "v_mfma_f32_16x16x32_bf16 v[160:163], a[228:231], v[28:31], v[160:163]\n\t"
    "v_mul_f32 v182, s64, v182\n\t"
    "v_mul_f32 v183, s64, v183\n\t"
    "v_mfma_f32_16x16x32_bf16 v[164:167], a[232:235], v[28:31], v[164:167]\n\t"
    "v_cvt_pk_bf16_f32 v48, v176, v177\n\t"
    "v_cvt_pk_bf16_f32 v49, v178, v179\n\t"
    "v_mfma_f32_16x16x32_bf16 v[168:171], a[236:239], v[28:31], v[168:171]\n\t"
    "v_cvt_pk_bf16_f32 v50, v180, v181\n\t"
    "v_cvt_pk_bf16_f32 v51, v182, v183\n\t"
    "v_mfma_f32_16x16x32_bf16 v[172:175], a[240:243], v[28:31], v[172:175]\n\t"
    "v_mul_f32 v184, s64, v184\n\t"
    "v_mul_f32 v185, s64, v185\n\t"
    "v_mfma_f32_16x16x32_bf16 v[112:115], a[212:215], v[24:27], v[112:115]\n\t"
    "v_mul_f32 v186, s64, v186\n\t"
    "v_mul_f32 v187, s64, v187\n\t"
    "v_mfma_f32_16x16x32_bf16 v[116:119], a[216:219], v[24:27], v[116:119]\n\t"
    "v_mul_f32 v188, s64, v188\n\t"
    "v_mul_f32 v189, s64, v189\n\t"
    "v_mfma_f32_16x16x32_bf16 v[120:123], a[220:223], v[24:27], v[120:123]\n\t"
    "v_mul_f32 v190, s64, v190\n\t"
    "v_mul_f32 v191, s64, v191\n\t"
    "v_mfma_f32_16x16x32_bf16 v[124:127], a[224:227], v[24:27], v[124:127]\n\t"
    "v_cvt_pk_bf16_f32 v52, v184, v185\n\t"
    "v_cvt_pk_bf16_f32 v53, v186, v187\n\t"
    "v_mfma_f32_16x16x32_bf16 v[128:131], a[228:231], v[24:27], v[128:131]\n\t"
    "v_cvt_pk_bf16_f32 v54, v188, v189\n\t"
    "v_cvt_pk_bf16_f32 v55, v190, v191\n\t"
    "v_mfma_f32_16x16x32_bf16 v[132:135], a[232:235], v[24:27], v[132:135]\n\t"
    "v_mul_f32 v192, s64, v192\n\t"
    "v_mul_f32 v193, s64, v193\n\t"
    "v_mfma_f32_16x16x32_bf16 v[136:139], a[236:239], v[24:27], v[136:139]\n\t"
    "v_mul_f32 v194, s64, v194\n\t"
    "v_mul_f32 v195, s64, v195\n\t"
    "v_mfma_f32_16x16x32_bf16 v[140:143], a[240:243], v[24:27], v[140:143]\n\t"
    "v_mul_f32 v196, s64, v196\n\t"
    "v_mul_f32 v197, s64, v197\n\t"
    "s_add_u32 s53, s42, 98304\n\t"
    "v_add_u32 v208, s53, v211\n\t"
    "v_add_u32 v209, s53, v212\n\t"
    "v_xor_b32 v245, 0x800, v245\n\t"
    "s_waitcnt lgkmcnt(0)\n\t"
    "s_barrier\n\t"
    "ds_read_b128 a[180:183], v208 offset:0\n\t"
    "ds_read_b128 a[184:187], v209 offset:0\n\t"
    "ds_read_b128 a[188:191], v208 offset:1024\n\t"
    "ds_read_b128 a[192:195], v209 offset:1024\n\t"
    "ds_read_b128 a[196:199], v208 offset:4096\n\t"
    "ds_read_b128 a[200:203], v209 offset:4096\n\t"
    "ds_read_b128 a[204:207], v208 offset:5120\n\t"
    "ds_read_b128 a[208:211], v209 offset:5120\n\t"
    "ds_read_b128 v[72:75], v245 offset:0\n\t"
    "ds_read_b128 v[76:79], v245 offset:1024\n\t"
    "s_mov_b32 m0, s41\n\t"
    "v_mul_f32 v198, s64, v198\n\t"
    "v_mul_f32 v199, s64, v199\n\t"
    "v_cvt_pk_bf16_f32 v56, v192, v193\n\t"
    "v_cvt_pk_bf16_f32 v57, v194, v195\n\t"
    "v_cvt_pk_bf16_f32 v58, v196, v197\n\t"
    "v_cvt_pk_bf16_f32 v59, v198, v199\n\t"
    "v_mul_f32 v200, s64, v200\n\t"
    "v_mul_f32 v201, s64, v201\n\t"
    "v_mul_f32 v202, s64, v202\n\t"
    "v_mul_f32 v203, s64, v203\n\t"
    "buffer_load_dwordx4 v214, s[36:39], s40 offen offset:0 nt lds\n\t"
    "v_mul_f32 v204, s64, v204\n\t"
    "v_mul_f32 v205, s64, v205\n\t"
    "v_mul_f32 v206, s64, v206\n\t"
    "v_mul_f32 v207, s64, v207\n\t"
    "v_cvt_pk_bf16_f32 v60, v200, v201\n\t"
    "v_cvt_pk_bf16_f32 v61, v202, v203\n\t"
    "v_cvt_pk_bf16_f32 v62, v204, v205\n\t"
    "v_cvt_pk_bf16_f32 v63, v206, v207\n\t"
    "v_permlane16_swap_b32 v48, v50\n\t"
    "v_permlane16_swap_b32 v49, v51\n\t"
    "buffer_load_dwordx4 v215, s[36:39], s40 offen offset:1024 nt lds\n\t"
    "v_permlane16_swap_b32 v52, v54\n\t"
    "v_permlane16_swap_b32 v53, v55\n\t"
    "v_permlane16_swap_b32 v56, v58\n\t"
    "v_permlane16_swap_b32 v57, v59\n\t"
    "v_permlane16_swap_b32 v60, v62\n\t"
    "v_permlane16_swap_b32 v61, v63\n\t"
    "v_mul_f32 v80, s64, v80\n\t"
    "v_mul_f32 v81, s64, v81\n\t"
    "v_mul_f32 v82, s64, v82\n\t"
    "v_mul_f32 v83, s64, v83\n\t"
    "buffer_load_dwordx4 v216, s[36:39], s40 offen offset:2048 nt lds\n\t"
    "v_mul_f32 v84, s64, v84\n\t"
    "v_mul_f32 v85, s64, v85\n\t"
    "v_mul_f32 v86, s64, v86\n\t"
    "v_mul_f32 v87, s64, v87\n\t"
    "v_cvt_pk_bf16_f32 v0, v80, v81\n\t"
    "v_cvt_pk_bf16_f32 v1, v82, v83\n\t"
    "v_cvt_pk_bf16_f32 v2, v84, v85\n\t"
    "v_cvt_pk_bf16_f32 v3, v86, v87\n\t"
    "v_mul_f32 v88, s64, v88\n\t"
    "v_mul_f32 v89, s64, v89\n\t"
    "buffer_load_dwordx4 v217, s[36:39], s40 offen offset:3072 nt lds\n\t"
    "v_mul_f32 v90, s64, v90\n\t"
    "v_mul_f32 v91, s64, v91\n\t"
    "v_mul_f32 v92, s64, v92\n\t"
    "v_mul_f32 v93, s64, v93\n\t"
    "v_mul_f32 v94, s64, v94\n\t"
    "v_mul_f32 v95, s64, v95\n\t"
    "v_cvt_pk_bf16_f32 v4, v88, v89\n\t"
    "v_cvt_pk_bf16_f32 v5, v90, v91\n\t"
    "v_cvt_pk_bf16_f32 v6, v92, v93\n\t"
    "v_cvt_pk_bf16_f32 v7, v94, v95\n\t"
    "s_add_u32 s41, s41, s58\n\t"
    "v_mul_f32 v96, s64, v96\n\t"
    "v_mul_f32 v97, s64, v97\n\t"
    "v_mul_f32 v98, s64, v98\n\t"
    "v_mul_f32 v99, s64, v99\n\t"
    "v_mul_f32 v100, s64, v100\n\t"
    "v_mul_f32 v101, s64, v101\n\t"
    "v_mul_f32 v102, s64, v102\n\t"
    "v_mul_f32 v103, s64, v103\n\t"
    "v_cvt_pk_bf16_f32 v8, v96, v97\n\t"
    "v_cvt_pk_bf16_f32 v9, v98, v99\n\t"
    "s_sub_u32 s58, 0x8000, s58\n\t"
    "v_cvt_pk_bf16_f32 v10, v100, v101\n\t"
    "v_cvt_pk_bf16_f32 v11, v102, v103\n\t"
    "v_mul_f32 v104, s64, v104\n\t"
    "v_mul_f32 v105, s64, v105\n\t"
    "v_mul_f32 v106, s64, v106\n\t"
    "v_mul_f32 v107, s64, v107\n\t"
    "v_mul_f32 v108, s64, v108\n\t"
    "v_mul_f32 v109, s64, v109\n\t"
    "v_mul_f32 v110, s64, v110\n\t"
    "v_mul_f32 v111, s64, v111\n\t"
    "s_cmp_eq_u32 s41, s43\n\t"
    "s_cselect_b32 s41, s42, s41\n\t"
    "v_cvt_pk_bf16_f32 v12, v104, v105\n\t"
    "v_cvt_pk_bf16_f32 v13, v106, v107\n\t"
    "v_cvt_pk_bf16_f32 v14, v108, v109\n\t"
    "v_cvt_pk_bf16_f32 v15, v110, v111\n\t"
    "v_permlane16_swap_b32 v0, v2\n\t"
    "v_permlane16_swap_b32 v1, v3\n\t"
    "v_permlane16_swap_b32 v4, v6\n\t"
    "v_permlane16_swap_b32 v5, v7\n\t"
    "v_permlane16_swap_b32 v8, v10\n\t"
    "v_permlane16_swap_b32 v9, v11\n\t"
    "s_add_u32 s40, s40, s59\n\t"
    "v_permlane16_swap_b32 v12, v14\n\t"
    "v_permlane16_swap_b32 v13, v15\n\t"
    "v_mul_f32 v144, s64, v144\n\t"
    "v_mul_f32 v145, s64, v145\n\t"
    "v_mul_f32 v146, s64, v146\n\t"
    "v_mul_f32 v147, s64, v147\n\t"
    "v_mul_f32 v148, s64, v148\n\t"
    "v_mul_f32 v149, s64, v149\n\t"
    "v_mul_f32 v150, s64, v150\n\t"
    "v_mul_f32 v151, s64, v151\n\t"
    "s_mov_b32 m0, s41\n\t"
    "v_cvt_pk_bf16_f32 v32, v144, v145\n\t"
    "v_cvt_pk_bf16_f32 v33, v146, v147\n\t"
    "v_cvt_pk_bf16_f32 v34, v148, v149\n\t"
    "v_cvt_pk_bf16_f32 v35, v150, v151\n\t"
    "v_mul_f32 v152, s64, v152\n\t"
    "v_mul_f32 v153, s64, v153\n\t"
    "v_mul_f32 v154, s64, v154\n\t"
    "v_mul_f32 v155, s64, v155\n\t"
    "v_mul_f32 v156, s64, v156\n\t"
    "v_mul_f32 v157, s64, v157\n\t"
    "buffer_load_dwordx4 v214, s[36:39], s40 offen offset:0 nt lds\n\t"
    "v_mul_f32 v158, s64, v158\n\t"
    "v_mul_f32 v159, s64, v159\n\t"
    "v_cvt_pk_bf16_f32 v36, v152, v153\n\t"
    "v_cvt_pk_bf16_f32 v37, v154, v155\n\t"
    "v_cvt_pk_bf16_f32 v38, v156, v157\n\t"
    "v_cvt_pk_bf16_f32 v39, v158, v159\n\t"
    "v_mul_f32 v160, s64, v160\n\t"
    "v_mul_f32 v161, s64, v161\n\t"
    "v_mul_f32 v162, s64, v162\n\t"
    "v_mul_f32 v163, s64, v163\n\t"
    "buffer_load_dwordx4 v215, s[36:39], s40 offen offset:1024 nt lds\n\t"
    "v_mul_f32 v164, s64, v164\n\t"
    "v_mul_f32 v165, s64, v165\n\t"
    "v_mul_f32 v166, s64, v166\n\t"
    "v_mul_f32 v167, s64, v167\n\t"
    "v_cvt_pk_bf16_f32 v40, v160, v161\n\t"
    "v_cvt_pk_bf16_f32 v41, v162, v163\n\t"
    "v_cvt_pk_bf16_f32 v42, v164, v165\n\t"
    "v_cvt_pk_bf16_f32 v43, v166, v167\n\t"
    "v_mul_f32 v168, s64, v168\n\t"
    "v_mul_f32 v169, s64, v169\n\t"
    "buffer_load_dwordx4 v216, s[36:39], s40 offen offset:2048 nt lds\n\t"
    "v_mul_f32 v170, s64, v170\n\t"
    "v_mul_f32 v171, s64, v171\n\t"
    "v_mul_f32 v172, s64, v172\n\t"
    "v_mul_f32 v173, s64, v173\n\t"
    "v_mul_f32 v174, s64, v174\n\t"
    "v_mul_f32 v175, s64, v175\n\t"
    "v_cvt_pk_bf16_f32 v44, v168, v169\n\t"
    "v_cvt_pk_bf16_f32 v45, v170, v171\n\t"
    "v_cvt_pk_bf16_f32 v46, v172, v173\n\t"
    "v_cvt_pk_bf16_f32 v47, v174, v175\n\t"
    "buffer_load_dwordx4 v217, s[36:39], s40 offen offset:3072 nt lds\n\t"
    "v_permlane16_swap_b32 v32, v34\n\t"
    "v_permlane16_swap_b32 v33, v35\n\t"
    "v_permlane16_swap_b32 v36, v38\n\t"
    "v_permlane16_swap_b32 v37, v39\n\t"
    "v_permlane16_swap_b32 v40, v42\n\t"
    "v_permlane16_swap_b32 v41, v43\n\t"
    "v_permlane16_swap_b32 v44, v46\n\t"
    "v_permlane16_swap_b32 v45, v47\n\t"
    "v_mul_f32 v112, s64, v112\n\t"
    "v_mul_f32 v113, s64, v113\n\t"
    "s_add_u32 s41, s41, s58\n\t"
    "v_mul_f32 v114, s64, v114\n\t"
    "v_mul_f32 v115, s64, v115\n\t"
    "v_mul_f32 v116, s64, v116\n\t"
    "v_mul_f32 v117, s64, v117\n\t"
    "v_mul_f32 v118, s64, v118\n\t"
    "v_mul_f32 v119, s64, v119\n\t"
    "v_cvt_pk_bf16_f32 v16, v112, v113\n\t"
    "v_cvt_pk_bf16_f32 v17, v114, v115\n\t"
    "v_cvt_pk_bf16_f32 v18, v116, v117\n\t"
    "v_cvt_pk_bf16_f32 v19, v118, v119\n\t"
    "s_sub_u32 s58, 0x8000, s58\n\t"
    "v_mul_f32 v120, s64, v120\n\t"
    "v_mul_f32 v121, s64, v121\n\t"
    "v_mul_f32 v122, s64, v122\n\t"
    "v_mul_f32 v123, s64, v123\n\t"
    "v_mul_f32 v124, s64, v124\n\t"
    "v_mul_f32 v125, s64, v125\n\t"
    "v_mul_f32 v126, s64, v126\n\t"
    "v_mul_f32 v127, s64, v127\n\t"
    "v_cvt_pk_bf16_f32 v20, v120, v121\n\t"
    "v_cvt_pk_bf16_f32 v21, v122, v123\n\t"
    "s_cmp_eq_u32 s41, s43\n\t"
    "s_cselect_b32 s41, s42, s41\n\t"
    "v_cvt_pk_bf16_f32 v22, v124, v125\n\t"
    "v_cvt_pk_bf16_f32 v23, v126, v127\n\t"
    "v_mul_f32 v128, s64, v128\n\t"
    "v_mul_f32 v129, s64, v129\n\t"
    "v_mul_f32 v130, s64, v130\n\t"
    "v_mul_f32 v131, s64, v131\n\t"
    "v_mul_f32 v132, s64, v132\n\t"
    "v_mul_f32 v133, s64, v133\n\t"
    "v_mul_f32 v134, s64, v134\n\t"
    "v_mul_f32 v135, s64, v135\n\t"
    "s_add_u32 s40, s40, s59\n\t"
    "v_cvt_pk_bf16_f32 v24, v128, v129\n\t"
    "v_cvt_pk_bf16_f32 v25, v130, v131\n\t"
    "v_cvt_pk_bf16_f32 v26, v132, v133\n\t"
    "v_cvt_pk_bf16_f32 v27, v134, v135\n\t"
    "v_mul_f32 v136, s64, v136\n\t"
    "v_mul_f32 v137, s64, v137\n\t"
    "v_mul_f32 v138, s64, v138\n\t"
    "v_mul_f32 v139, s64, v139\n\t"
    "v_mul_f32 v140, s64, v140\n\t"
    "v_mul_f32 v141, s64, v141\n\t"
    "v_mul_f32 v142, s64, v142\n\t"
    "v_mul_f32 v143, s64, v143\n\t"
    "v_cvt_pk_bf16_f32 v28, v136, v137\n\t"
    "v_cvt_pk_bf16_f32 v29, v138, v139\n\t"
    "v_cvt_pk_bf16_f32 v30, v140, v141\n\t"
    "v_cvt_pk_bf16_f32 v31, v142, v143\n\t"
    "v_permlane16_swap_b32 v16, v18\n\t"
    "v_permlane16_swap_b32 v17, v19\n\t"
    "v_permlane16_swap_b32 v20, v22\n\t"
    "v_permlane16_swap_b32 v21, v23\n\t"
    "v_permlane16_swap_b32 v24, v26\n\t"
    "v_permlane16_swap_b32 v25, v27\n\t"
    "v_permlane16_swap_b32 v28, v30\n\t"
    "v_permlane16_swap_b32 v29, v31\n\t"
    "s_lshl_b32 s52, 0x4000, s65\n\t"
    "s_mov_b32 m0, s53\n\t"
    "s_add_u32 s41, s53, s52\n\t"
    "v_add_u32 v210, s41, v213\n\t"
    "v_add_u32 v208, s41, v211\n\t"
    "v_add_u32 v209, s41, v212\n\t"
    "s_cmp_eq_u32 s65, 0\n\t"
    "s_movk_i32 s98, 1350\n\t"
    "s_cmovk_i32 s98, 1550\n\t"
    "s_add_u32 s98, s98, s34\n\t"
    "s_cmp_eq_u32 s65, 0\n\t"
    "s_cbranch_scc1 PEEL_E_A_%=\n\t"
    "s_branch PEEL_E_B_%=\n\t"
    "QP1_%=:\n\t"
    "s_mov_b32 s99, 15355\n\t"
    "s_waitcnt vmcnt(16)\n\t"
    "s_barrier\n\t"
    "ds_read_b128 v[0:3], v52 offset:40960\n\t"
    "ds_read_b128 v[4:7], v52 offset:43008\n\t"
    "ds_read_b128 v[8:11], v52 offset:45056\n\t"
    "ds_read_b128 v[12:15], v52 offset:47104\n\t"
    "ds_read_b128 v[16:19], v52 offset:49152\n\t"
    "ds_read_b64_tr_b16 a[180:181], v50 offset:0\n\t"
    "ds_read_b64_tr_b16 a[182:183], v50 offset:256\n\t"
    "ds_read_b64_tr_b16 a[184:185], v51 offset:0\n\t"
    "ds_read_b64_tr_b16 a[186:187], v51 offset:256\n\t"
    "ds_read_b64_tr_b16 a[188:189], v50 offset:512\n\t"
    "ds_read_b64_tr_b16 a[190:191], v50 offset:768\n\t"
    "ds_read_b64_tr_b16 a[192:193], v51 offset:512\n\t"
    "ds_read_b64_tr_b16 a[194:195], v51 offset:768\n\t"
    "ds_read_b64_tr_b16 a[196:197], v50 offset:1024\n\t"
    "ds_read_b64_tr_b16 a[198:199], v50 offset:1280\n\t"
    "ds_read_b64_tr_b16 a[200:201], v51 offset:1024\n\t"
    "ds_read_b64_tr_b16 a[202:203], v51 offset:1280\n\t"
    "ds_read_b64_tr_b16 a[204:205], v50 offset:1536\n\t"
    "ds_read_b64_tr_b16 a[206:207], v50 offset:1792\n\t"
    "ds_read_b64_tr_b16 a[208:209], v51 offset:1536\n\t"
    "ds_read_b64_tr_b16 a[210:211], v51 offset:1792\n\t"
    "s_waitcnt vmcnt(12) lgkmcnt(0)\n\t"
    "s_barrier\n\t"
    "QL10_%=:\n\t"
    "v_mfma_f32_16x16x32_bf16 v[80:83], a[180:183], v[0:3], v[80:83]\n\t"
    "s_add_u32 m0, s68, s70\n\t"
    "ds_read_b128 v[20:23], v53 offset:40960\n\t"
    "v_mfma_f32_16x16x32_bf16 v[112:115], a[180:183], v[4:7], v[112:115]\n\t"
    "ds_read_b128 v[24:27], v53 offset:43008\n\t"
    "ds_read_b128 v[28:31], v53 offset:45056\n\t"
    "v_mfma_f32_16x16x32_bf16 v[144:147], a[180:183], v[8:11], v[144:147]\n\t"
    "ds_read_b128 v[32:35], v53 offset:47104\n\t"
    "ds_read_b128 v[36:39], v53 offset:49152\n\t"
    "v_mfma_f32_16x16x32_bf16 v[176:179], a[180:183], v[12:15], v[176:179]\n\t"
    "ds_read_b64_tr_b16 a[212:213], v50 offset:16384\n\t"
    "ds_read_b64_tr_b16 a[214:215], v50 offset:16640\n\t"
    "v_mfma_f32_16x16x32_bf16 v[84:87], a[184:187], v[0:3], v[84:87]\n\t"
    "buffer_load_dwordx4 v40, s[88:91], s66 offen offset:0  lds\n\t"
    "v_mfma_f32_16x16x32_bf16 v[116:119], a[184:187], v[4:7], v[116:119]\n\t"
    "ds_read_b64_tr_b16 a[216:217], v51 offset:16384\n\t"
    "ds_read_b64_tr_b16 a[218:219], v51 offset:16640\n\t"
    "v_mfma_f32_16x16x32_bf16 v[148:151], a[184:187], v[8:11], v[148:151]\n\t"
    "ds_read_b64_tr_b16 a[220:221], v50 offset:16896\n\t"
    "ds_read_b64_tr_b16 a[222:223], v50 offset:17152\n\t"
    "v_mfma_f32_16x16x32_bf16 v[180:183], a[184:187], v[12:15], v[180:183]\n\t"
    "ds_read_b64_tr_b16 a[224:225], v51 offset:16896\n\t"
    "ds_read_b64_tr_b16 a[226:227], v51 offset:17152\n\t"
    "v_mfma_f32_16x16x32_bf16 v[88:91], a[188:191], v[0:3], v[88:91]\n\t"
    "ds_read_b64_tr_b16 a[228:229], v50 offset:17408\n\t"
    "ds_read_b64_tr_b16 a[230:231], v50 offset:17664\n\t"
    "v_mfma_f32_16x16x32_bf16 v[120:123], a[188:191], v[4:7], v[120:123]\n\t"
    "ds_read_b64_tr_b16 a[232:233], v51 offset:17408\n\t"
    "ds_read_b64_tr_b16 a[234:235], v51 offset:17664\n\t"
    "v_mfma_f32_16x16x32_bf16 v[152:155], a[188:191], v[8:11], v[152:155]\n\t"
    "ds_read_b64_tr_b16 a[236:237], v50 offset:17920\n\t"
    "ds_read_b64_tr_b16 a[238:239], v50 offset:18176\n\t"
    "v_mfma_f32_16x16x32_bf16 v[184:187], a[188:191], v[12:15], v[184:187]\n\t"
    "buffer_load_dwordx4 v41, s[88:91], s66 offen offset:1024  lds\n\t"
    "v_mfma_f32_16x16x32_bf16 v[92:95], a[192:195], v[0:3], v[92:95]\n\t"
    "ds_read_b64_tr_b16 a[240:241], v51 offset:17920\n\t"
    "ds_read_b64_tr_b16 a[242:243], v51 offset:18176\n\t"
    "v_mfma_f32_16x16x32_bf16 v[124:127], a[192:195], v[4:7], v[124:127]\n\t"
    "v_add_u32 v50, s69, v50\n\t"
    "v_add_u32 v51, s69, v51\n\t"
    "v_mfma_f32_16x16x32_bf16 v[156:159], a[192:195], v[8:11], v[156:159]\n\t"
    "v_add_u32 v52, s69, v52\n\t"
    "v_add_u32 v53, s69, v53\n\t"
    "v_mfma_f32_16x16x32_bf16 v[188:191], a[192:195], v[12:15], v[188:191]\n\t"
    "v_mfma_f32_16x16x32_bf16 v[96:99], a[196:199], v[0:3], v[96:99]\n\t"
    "v_mfma_f32_16x16x32_bf16 v[128:131], a[196:199], v[4:7], v[128:131]\n\t"
    "v_mfma_f32_16x16x32_bf16 v[160:163], a[196:199], v[8:11], v[160:163]\n\t"
    "v_mfma_f32_16x16x32_bf16 v[192:195], a[196:199], v[12:15], v[192:195]\n\t"
    "buffer_load_dwordx4 v42, s[88:91], s66 offen offset:2048  lds\n\t"
    "v_mfma_f32_16x16x32_bf16 v[224:227], a[196:199], v[16:19], v[224:227]\n\t"
    "v_mfma_f32_16x16x32_bf16 v[100:103], a[200:203], v[0:3], v[100:103]\n\t"
    "v_mfma_f32_16x16x32_bf16 v[132:135], a[200:203], v[4:7], v[132:135]\n\t"
    "v_mfma_f32_16x16x32_bf16 v[164:167], a[200:203], v[8:11], v[164:167]\n\t"
    "v_mfma_f32_16x16x32_bf16 v[196:199], a[200:203], v[12:15], v[196:199]\n\t"
    "v_mfma_f32_16x16x32_bf16 v[228:231], a[200:203], v[16:19], v[228:231]\n\t"
    "v_mfma_f32_16x16x32_bf16 v[104:107], a[204:207], v[0:3], v[104:107]\n\t"
    "buffer_load_dwordx4 v43, s[88:91], s66 offen offset:3072  lds\n\t"
    "v_mfma_f32_16x16x32_bf16 v[136:139], a[204:207], v[4:7], v[136:139]\n\t"
    "v_mfma_f32_16x16x32_bf16 v[168:171], a[204:207], v[8:11], v[168:171]\n\t"
    "v_mfma_f32_16x16x32_bf16 v[200:203], a[204:207], v[12:15], v[200:203]\n\t"
    "s_add_u32 s66, s66, 0x40000\n\t"
    "v_mfma_f32_16x16x32_bf16 v[232:235], a[204:207], v[16:19], v[232:235]\n\t"
    "v_mfma_f32_16x16x32_bf16 v[108:111], a[208:211], v[0:3], v[108:111]\n\t"
    "v_mfma_f32_16x16x32_bf16 v[140:143], a[208:211], v[4:7], v[140:143]\n\t"
    "v_mfma_f32_16x16x32_bf16 v[172:175], a[208:211], v[8:11], v[172:175]\n\t"
    "s_waitcnt vmcnt(8) lgkmcnt(0)\n\t"
    "s_barrier\n\t"
    "v_mfma_f32_16x16x32_bf16 v[204:207], a[208:211], v[12:15], v[204:207]\n\t"
    "v_mfma_f32_16x16x32_bf16 v[236:239], a[208:211], v[16:19], v[236:239]\n\t"
    "v_mfma_f32_16x16x32_bf16 v[80:83], a[212:215], v[20:23], v[80:83]\n\t"
    "s_add_u32 m0, s68, s71\n\t"
    "ds_read_b128 v[0:3], v52 offset:40960\n\t"
    "v_mfma_f32_16x16x32_bf16 v[112:115], a[212:215], v[24:27], v[112:115]\n\t"
    "ds_read_b128 v[4:7], v52 offset:43008\n\t"
    "ds_read_b128 v[8:11], v52 offset:45056\n\t"
    "v_mfma_f32_16x16x32_bf16 v[144:147], a[212:215], v[28:31], v[144:147]\n\t"
    "ds_read_b128 v[12:15], v52 offset:47104\n\t"
    "ds_read_b128 v[16:19], v52 offset:49152\n\t"
    "v_mfma_f32_16x16x32_bf16 v[176:179], a[212:215], v[32:35], v[176:179]\n\t"
    "ds_read_b64_tr_b16 a[180:181], v50 offset:0\n\t"
    "ds_read_b64_tr_b16 a[182:183], v50 offset:256\n\t"
    "v_mfma_f32_16x16x32_bf16 v[84:87], a[216:219], v[20:23], v[84:87]\n\t"
    "buffer_load_dwordx4 v44, s[92:95], s67 offen offset:0  lds\n\t"
    "v_mfma_f32_16x16x32_bf16 v[116:119], a[216:219], v[24:27], v[116:119]\n\t"
    "ds_read_b64_tr_b16 a[184:185], v51 offset:0\n\t"
    "ds_read_b64_tr_b16 a[186:187], v51 offset:256\n\t"
    "v_mfma_f32_16x16x32_bf16 v[148:151], a[216:219], v[28:31], v[148:151]\n\t"
    "ds_read_b64_tr_b16 a[188:189], v50 offset:512\n\t"
    "ds_read_b64_tr_b16 a[190:191], v50 offset:768\n\t"
    "v_mfma_f32_16x16x32_bf16 v[180:183], a[216:219], v[32:35], v[180:183]\n\t"
    "buffer_load_dwordx4 v45, s[92:95], s67 offen offset:1024  lds\n\t"
    "v_mfma_f32_16x16x32_bf16 v[88:91], a[220:223], v[20:23], v[88:91]\n\t"
    "ds_read_b64_tr_b16 a[192:193], v51 offset:512\n\t"
    "ds_read_b64_tr_b16 a[194:195], v51 offset:768\n\t"
    "v_mfma_f32_16x16x32_bf16 v[120:123], a[220:223], v[24:27], v[120:123]\n\t"
    "ds_read_b64_tr_b16 a[196:197], v50 offset:1024\n\t"
    "ds_read_b64_tr_b16 a[198:199], v50 offset:1280\n\t"
    "v_mfma_f32_16x16x32_bf16 v[152:155], a[220:223], v[28:31], v[152:155]\n\t"
    "ds_read_b64_tr_b16 a[200:201], v51 offset:1024\n\t"
    "ds_read_b64_tr_b16 a[202:203], v51 offset:1280\n\t"
    "v_mfma_f32_16x16x32_bf16 v[184:187], a[220:223], v[32:35], v[184:187]\n\t"
    "buffer_load_dwordx4 v46, s[92:95], s67 offen offset:2048  lds\n\t"
    "v_mfma_f32_16x16x32_bf16 v[92:95], a[224:227], v[20:23], v[92:95]\n\t"
    "ds_read_b64_tr_b16 a[204:205], v50 offset:1536\n\t"
    "ds_read_b64_tr_b16 a[206:207], v50 offset:1792\n\t"
    "v_mfma_f32_16x16x32_bf16 v[124:127], a[224:227], v[24:27], v[124:127]\n\t"
    "ds_read_b64_tr_b16 a[208:209], v51 offset:1536\n\t"
    "ds_read_b64_tr_b16 a[210:211], v51 offset:1792\n\t"
    "v_mfma_f32_16x16x32_bf16 v[156:159], a[224:227], v[28:31], v[156:159]\n\t"
    "v_mfma_f32_16x16x32_bf16 v[188:191], a[224:227], v[32:35], v[188:191]\n\t"
    "buffer_load_dwordx4 v47, s[92:95], s67 offen offset:3072  lds\n\t"
    "v_mfma_f32_16x16x32_bf16 v[96:99], a[228:231], v[20:23], v[96:99]\n\t"
    "s_add_u32 s52, s68, s70\n\t"
    "s_add_u32 m0, s52, 16384\n\t"
    "v_mfma_f32_16x16x32_bf16 v[128:131], a[228:231], v[24:27], v[128:131]\n\t"
    "v_mfma_f32_16x16x32_bf16 v[160:163], a[228:231], v[28:31], v[160:163]\n\t"
    "v_mfma_f32_16x16x32_bf16 v[192:195], a[228:231], v[32:35], v[192:195]\n\t"
    "buffer_load_dwordx4 v40, s[88:91], s66 offen offset:0  lds\n\t"
    "v_mfma_f32_16x16x32_bf16 v[224:227], a[228:231], v[36:39], v[224:227]\n\t"
    "v_mfma_f32_16x16x32_bf16 v[100:103], a[232:235], v[20:23], v[100:103]\n\t"
    "v_mfma_f32_16x16x32_bf16 v[132:135], a[232:235], v[24:27], v[132:135]\n\t"
    "buffer_load_dwordx4 v41, s[88:91], s66 offen offset:1024  lds\n\t"
    "v_mfma_f32_16x16x32_bf16 v[164:167], a[232:235], v[28:31], v[164:167]\n\t"
    "v_mfma_f32_16x16x32_bf16 v[196:199], a[232:235], v[32:35], v[196:199]\n\t"
    "v_mfma_f32_16x16x32_bf16 v[228:231], a[232:235], v[36:39], v[228:231]\n\t"
    "v_mfma_f32_16x16x32_bf16 v[104:107], a[236:239], v[20:23], v[104:107]\n\t"
    "buffer_load_dwordx4 v42, s[88:91], s66 offen offset:2048  lds\n\t"
    "v_mfma_f32_16x16x32_bf16 v[136:139], a[236:239], v[24:27], v[136:139]\n\t"
    "v_mfma_f32_16x16x32_bf16 v[168:171], a[236:239], v[28:31], v[168:171]\n\t"
    "buffer_load_dwordx4 v43, s[88:91], s66 offen offset:3072  lds\n\t"
    "v_mfma_f32_16x16x32_bf16 v[200:203], a[236:239], v[32:35], v[200:203]\n\t"
    "s_add_u32 s68, s68, s69\n\t"
    "s_sub_u32 s69, 0, s69\n\t"
    "v_mfma_f32_16x16x32_bf16 v[232:235], a[236:239], v[36:39], v[232:235]\n\t"
    "s_and_b32 s67, s97, 15\n\t"
    "s_lshr_b32 s97, s97, 4\n\t"
    "v_mfma_f32_16x16x32_bf16 v[108:111], a[240:243], v[20:23], v[108:111]\n\t"
    "s_lshl_b32 s66, s67, 19\n\t"
    "s_lshl_b32 s67, s67, 7\n\t"
    "v_mfma_f32_16x16x32_bf16 v[140:143], a[240:243], v[24:27], v[140:143]\n\t"
    "s_lshr_b32 s99, s99, 2\n\t"
    "v_mfma_f32_16x16x32_bf16 v[172:175], a[240:243], v[28:31], v[172:175]\n\t"
    "s_waitcnt vmcnt(12) lgkmcnt(0)\n\t"
    "s_barrier\n\t"
    "v_mfma_f32_16x16x32_bf16 v[204:207], a[240:243], v[32:35], v[204:207]\n\t"
    "v_mfma_f32_16x16x32_bf16 v[236:239], a[240:243], v[36:39], v[236:239]\n\t"
    "s_bitcmp1_b32 s99, 0\n\t"
    "s_cbranch_scc1 QL10_%=\n\t"
    "s_cmp_eq_u32 s99, 0\n\t"
    "s_cbranch_scc1 QT1_%=\n\t"
    "QL11_%=:\n\t"
    "v_mfma_f32_16x16x32_bf16 v[80:83], a[180:183], v[0:3], v[80:83]\n\t"
    "s_mov_b32 m0, s41\n\t"
    "ds_read_b128 v[20:23], v53 offset:40960\n\t"
    "v_mfma_f32_16x16x32_bf16 v[112:115], a[180:183], v[4:7], v[112:115]\n\t"
    "ds_read_b128 v[24:27], v53 offset:43008\n\t"
    "ds_read_b128 v[28:31], v53 offset:45056\n\t"
    "v_mfma_f32_16x16x32_bf16 v[144:147], a[180:183], v[8:11], v[144:147]\n\t"
    "ds_read_b128 v[32:35], v53 offset:47104\n\t"
    "ds_read_b128 v[36:39], v53 offset:49152\n\t"
    "v_mfma_f32_16x16x32_bf16 v[176:179], a[180:183], v[12:15], v[176:179]\n\t"
    "ds_read_b64_tr_b16 a[212:213], v50 offset:16384\n\t"
    "ds_read_b64_tr_b16 a[214:215], v50 offset:16640\n\t"
    "v_mfma_f32_16x16x32_bf16 v[84:87], a[184:187], v[0:3], v[84:87]\n\t"
    "buffer_load_dwordx4 v214, s[36:39], s40 offen offset:0 nt lds\n\t"
    "v_mfma_f32_16x16x32_bf16 v[116:119], a[184:187], v[4:7], v[116:119]\n\t"
    "ds_read_b64_tr_b16 a[216:217], v51 offset:16384\n\t"
    "ds_read_b64_tr_b16 a[218:219], v51 offset:16640\n\t"
    "v_mfma_f32_16x16x32_bf16 v[148:151], a[184:187], v[8:11], v[148:151]\n\t"
    "ds_read_b64_tr_b16 a[220:221], v50 offset:16896\n\t"
    "ds_read_b64_tr_b16 a[222:223], v50 offset:17152\n\t"
    "v_mfma_f32_16x16x32_bf16 v[180:183], a[184:187], v[12:15], v[180:183]\n\t"
    "buffer_load_dwordx4 v215, s[36:39], s40 offen offset:1024 nt lds\n\t"
    "v_mfma_f32_16x16x32_bf16 v[88:91], a[188:191], v[0:3], v[88:91]\n\t"
    "ds_read_b64_tr_b16 a[224:225], v51 offset:16896\n\t"
    "ds_read_b64_tr_b16 a[226:227], v51 offset:17152\n\t"
    "v_mfma_f32_16x16x32_bf16 v[120:123], a[188:191], v[4:7], v[120:123]\n\t"
    "ds_read_b64_tr_b16 a[228:229], v50 offset:17408\n\t"
    "ds_read_b64_tr_b16 a[230:231], v50 offset:17664\n\t"
    "v_mfma_f32_16x16x32_bf16 v[152:155], a[188:191], v[8:11], v[152:155]\n\t"
    "ds_read_b64_tr_b16 a[232:233], v51 offset:17408\n\t"
    "ds_read_b64_tr_b16 a[234:235], v51 offset:17664\n\t"
    "v_mfma_f32_16x16x32_bf16 v[184:187], a[188:191], v[12:15], v[184:187]\n\t"
    "buffer_load_dwordx4 v216, s[36:39], s40 offen offset:2048 nt lds\n\t"
    "v_mfma_f32_16x16x32_bf16 v[92:95], a[192:195], v[0:3], v[92:95]\n\t"
    "ds_read_b64_tr_b16 a[236:237], v50 offset:17920\n\t"
    "ds_read_b64_tr_b16 a[238:239], v50 offset:18176\n\t"
    "v_mfma_f32_16x16x32_bf16 v[124:127], a[192:195], v[4:7], v[124:127]\n\t"
    "buffer_load_dwordx4 v217, s[36:39], s40 offen offset:3072 nt lds\n\t"
    "v_mfma_f32_16x16x32_bf16 v[156:159], a[192:195], v[8:11], v[156:159]\n\t"
    "s_add_u32 s41, s41, s58\n\t"
    "s_sub_u32 s58, 0x8000, s58\n\t"
    "v_mfma_f32_16x16x32_bf16 v[188:191], a[192:195], v[12:15], v[188:191]\n\t"
    "s_cmp_eq_u32 s41, s43\n\t"
    "s_cselect_b32 s41, s42, s41\n\t"
    "v_mfma_f32_16x16x32_bf16 v[96:99], a[196:199], v[0:3], v[96:99]\n\t"
    "s_add_u32 s40, s40, s59\n\t"
    "s_add_u32 m0, s68, s70\n\t"
    "v_mfma_f32_16x16x32_bf16 v[128:131], a[196:199], v[4:7], v[128:131]\n\t"
    "ds_read_b64_tr_b16 a[240:241], v51 offset:17920\n\t"
    "ds_read_b64_tr_b16 a[242:243], v51 offset:18176\n\t"
    "v_mfma_f32_16x16x32_bf16 v[160:163], a[196:199], v[8:11], v[160:163]\n\t"
    "v_add_u32 v50, s69, v50\n\t"
    "v_add_u32 v51, s69, v51\n\t"
    "v_mfma_f32_16x16x32_bf16 v[192:195], a[196:199], v[12:15], v[192:195]\n\t"
    "buffer_load_dwordx4 v40, s[88:91], s66 offen offset:0  lds\n\t"
    "v_mfma_f32_16x16x32_bf16 v[224:227], a[196:199], v[16:19], v[224:227]\n\t"
    "v_add_u32 v52, s69, v52\n\t"
    "v_add_u32 v53, s69, v53\n\t"
    "v_mfma_f32_16x16x32_bf16 v[100:103], a[200:203], v[0:3], v[100:103]\n\t"
    "v_mfma_f32_16x16x32_bf16 v[132:135], a[200:203], v[4:7], v[132:135]\n\t"
    "buffer_load_dwordx4 v41, s[88:91], s66 offen offset:1024  lds\n\t"
    "v_mfma_f32_16x16x32_bf16 v[164:167], a[200:203], v[8:11], v[164:167]\n\t"
    "v_mfma_f32_16x16x32_bf16 v[196:199], a[200:203], v[12:15], v[196:199]\n\t"
    "v_mfma_f32_16x16x32_bf16 v[228:231], a[200:203], v[16:19], v[228:231]\n\t"
    "v_mfma_f32_16x16x32_bf16 v[104:107], a[204:207], v[0:3], v[104:107]\n\t"
    "buffer_load_dwordx4 v42, s[88:91], s66 offen offset:2048  lds\n\t"
    "v_mfma_f32_16x16x32_bf16 v[136:139], a[204:207], v[4:7], v[136:139]\n\t"
    "v_mfma_f32_16x16x32_bf16 v[168:171], a[204:207], v[8:11], v[168:171]\n\t"
    "v_mfma_f32_16x16x32_bf16 v[200:203], a[204:207], v[12:15], v[200:203]\n\t"
    "v_mfma_f32_16x16x32_bf16 v[232:235], a[204:207], v[16:19], v[232:235]\n\t"
    "buffer_load_dwordx4 v43, s[88:91], s66 offen offset:3072  lds\n\t"
    "v_mfma_f32_16x16x32_bf16 v[108:111], a[208:211], v[0:3], v[108:111]\n\t"
    "s_add_u32 s66, s66, 0x40000\n\t"
    "v_mfma_f32_16x16x32_bf16 v[140:143], a[208:211], v[4:7], v[140:143]\n\t"
    "v_mfma_f32_16x16x32_bf16 v[172:175], a[208:211], v[8:11], v[172:175]\n\t"
    "s_waitcnt vmcnt(12) lgkmcnt(0)\n\t"
    "s_barrier\n\t"
    "v_mfma_f32_16x16x32_bf16 v[204:207], a[208:211], v[12:15], v[204:207]\n\t"
    "v_mfma_f32_16x16x32_bf16 v[236:239], a[208:211], v[16:19], v[236:239]\n\t"
    "v_mfma_f32_16x16x32_bf16 v[80:83], a[212:215], v[20:23], v[80:83]\n\t"
    "s_add_u32 m0, s68, s71\n\t"
    "ds_read_b128 v[0:3], v52 offset:40960\n\t"
    "v_mfma_f32_16x16x32_bf16 v[112:115], a[212:215], v[24:27], v[112:115]\n\t"
    "ds_read_b128 v[4:7], v52 offset:43008\n\t"
    "ds_read_b128 v[8:11], v52 offset:45056\n\t"
    "v_mfma_f32_16x16x32_bf16 v[144:147], a[212:215], v[28:31], v[144:147]\n\t"
    "ds_read_b128 v[12:15], v52 offset:47104\n\t"
    "ds_read_b128 v[16:19], v52 offset:49152\n\t"
    "v_mfma_f32_16x16x32_bf16 v[176:179], a[212:215], v[32:35], v[176:179]\n\t"
    "ds_read_b64_tr_b16 a[180:181], v50 offset:0\n\t"
    "ds_read_b64_tr_b16 a[182:183], v50 offset:256\n\t"
    "v_mfma_f32_16x16x32_bf16 v[84:87], a[216:219], v[20:23], v[84:87]\n\t"
    "buffer_load_dwordx4 v44, s[92:95], s67 offen offset:0  lds\n\t"
    "v_mfma_f32_16x16x32_bf16 v[116:119], a[216:219], v[24:27], v[116:119]\n\t"
    "ds_read_b64_tr_b16 a[184:185], v51 offset:0\n\t"
    "ds_read_b64_tr_b16 a[186:187], v51 offset:256\n\t"
    "v_mfma_f32_16x16x32_bf16 v[148:151], a[216:219], v[28:31], v[148:151]\n\t"
    "ds_read_b64_tr_b16 a[188:189], v50 offset:512\n\t"
    "ds_read_b64_tr_b16 a[190:191], v50 offset:768\n\t"
    "v_mfma_f32_16x16x32_bf16 v[180:183], a[216:219], v[32:35], v[180:183]\n\t"
    "buffer_load_dwordx4 v45, s[92:95], s67 offen offset:1024  lds\n\t"
    "v_mfma_f32_16x16x32_bf16 v[88:91], a[220:223], v[20:23], v[88:91]\n\t"
    "ds_read_b64_tr_b16 a[192:193], v51 offset:512\n\t"
    "ds_read_b64_tr_b16 a[194:195], v51 offset:768\n\t"
    "v_mfma_f32_16x16x32_bf16 v[120:123], a[220:223], v[24:27], v[120:123]\n\t"
    "ds_read_b64_tr_b16 a[196:197], v50 offset:1024\n\t"
    "ds_read_b64_tr_b16 a[198:199], v50 offset:1280\n\t"
    "v_mfma_f32_16x16x32_bf16 v[152:155], a[220:223], v[28:31], v[152:155]\n\t"
    "ds_read_b64_tr_b16 a[200:201], v51 offset:1024\n\t"
    "ds_read_b64_tr_b16 a[202:203], v51 offset:1280\n\t"
    "v_mfma_f32_16x16x32_bf16 v[184:187], a[220:223], v[32:35], v[184:187]\n\t"
    "buffer_load_dwordx4 v46, s[92:95], s67 offen offset:2048  lds\n\t"
    "v_mfma_f32_16x16x32_bf16 v[92:95], a[224:227], v[20:23], v[92:95]\n\t"
    "ds_read_b64_tr_b16 a[204:205], v50 offset:1536\n\t"
    "ds_read_b64_tr_b16 a[206:207], v50 offset:1792\n\t"
    "v_mfma_f32_16x16x32_bf16 v[124:127], a[224:227], v[24:27], v[124:127]\n\t"
    "ds_read_b64_tr_b16 a[208:209], v51 offset:1536\n\t"
    "ds_read_b64_tr_b16 a[210:211], v51 offset:1792\n\t"
    "v_mfma_f32_16x16x32_bf16 v[156:159], a[224:227], v[28:31], v[156:159]\n\t"
    "v_mfma_f32_16x16x32_bf16 v[188:191], a[224:227], v[32:35], v[188:191]\n\t"
    "buffer_load_dwordx4 v47, s[92:95], s67 offen offset:3072  lds\n\t"
    "v_mfma_f32_16x16x32_bf16 v[96:99], a[228:231], v[20:23], v[96:99]\n\t"
    "s_add_u32 s52, s68, s70\n\t"
    "s_add_u32 m0, s52, 16384\n\t"
    "v_mfma_f32_16x16x32_bf16 v[128:131], a[228:231], v[24:27], v[128:131]\n\t"
    "v_mfma_f32_16x16x32_bf16 v[160:163], a[228:231], v[28:31], v[160:163]\n\t"
    "v_mfma_f32_16x16x32_bf16 v[192:195], a[228:231], v[32:35], v[192:195]\n\t"
    "buffer_load_dwordx4 v40, s[88:91], s66 offen offset:0  lds\n\t"
    "v_mfma_f32_16x16x32_bf16 v[224:227], a[228:231], v[36:39], v[224:227]\n\t"
    "v_mfma_f32_16x16x32_bf16 v[100:103], a[232:235], v[20:23], v[100:103]\n\t"
    "v_mfma_f32_16x16x32_bf16 v[132:135], a[232:235], v[24:27], v[132:135]\n\t"
    "buffer_load_dwordx4 v41, s[88:91], s66 offen offset:1024  lds\n\t"
    "v_mfma_f32_16x16x32_bf16 v[164:167], a[232:235], v[28:31], v[164:167]\n\t"
    "v_mfma_f32_16x16x32_bf16 v[196:199], a[232:235], v[32:35], v[196:199]\n\t"
    "v_mfma_f32_16x16x32_bf16 v[228:231], a[232:235], v[36:39], v[228:231]\n\t"
    "v_mfma_f32_16x16x32_bf16 v[104:107], a[236:239], v[20:23], v[104:107]\n\t"
    "buffer_load_dwordx4 v42, s[88:91], s66 offen offset:2048  lds\n\t"
    "v_mfma_f32_16x16x32_bf16 v[136:139], a[236:239], v[24:27], v[136:139]\n\t"
    "v_mfma_f32_16x16x32_bf16 v[168:171], a[236:239], v[28:31], v[168:171]\n\t"
    "buffer_load_dwordx4 v43, s[88:91], s66 offen offset:3072  lds\n\t"
    "v_mfma_f32_16x16x32_bf16 v[200:203], a[236:239], v[32:35], v[200:203]\n\t"
    "s_add_u32 s68, s68, s69\n\t"
    "s_sub_u32 s69, 0, s69\n\t"
    "v_mfma_f32_16x16x32_bf16 v[232:235], a[236:239], v[36:39], v[232:235]\n\t"
    "s_and_b32 s67, s97, 15\n\t"
    "s_lshr_b32 s97, s97, 4\n\t"
    "v_mfma_f32_16x16x32_bf16 v[108:111], a[240:243], v[20:23], v[108:111]\n\t"
    "s_lshl_b32 s66, s67, 19\n\t"
    "s_lshl_b32 s67, s67, 7\n\t"
    "v_mfma_f32_16x16x32_bf16 v[140:143], a[240:243], v[24:27], v[140:143]\n\t"
    "s_lshr_b32 s99, s99, 2\n\t"
    "v_mfma_f32_16x16x32_bf16 v[172:175], a[240:243], v[28:31], v[172:175]\n\t"
    "s_waitcnt vmcnt(16) lgkmcnt(0)\n\t"
    "s_barrier\n\t"
    "v_mfma_f32_16x16x32_bf16 v[204:207], a[240:243], v[32:35], v[204:207]\n\t"
    "v_mfma_f32_16x16x32_bf16 v[236:239], a[240:243], v[36:39], v[236:239]\n\t"
    "s_bitcmp1_b32 s99, 0\n\t"
    "s_cbranch_scc1 QL10_%=\n\t"
    "s_cmp_eq_u32 s99, 0\n\t"
    "s_cbranch_scc0 QL11_%=\n\t"
    "QT1_%=:\n\t"
    "v_add_u32 v223, 1, v254\n\t"
    "s_mov_b32 exec_lo, 0x80402000\n\t"
    "s_mov_b32 exec_hi, 0x80402000\n\t"
    "global_store_dword v253, v223, s[60:61] sc1\n\t"
    "s_mov_b64 exec, -1\n\t"
    "v_mfma_f32_16x16x32_bf16 v[80:83], a[180:183], v[0:3], v[80:83]\n\t"
    "s_mov_b32 m0, s41\n\t"
    "ds_read_b128 v[20:23], v53 offset:40960\n\t"
    "v_mfma_f32_16x16x32_bf16 v[112:115], a[180:183], v[4:7], v[112:115]\n\t"
    "ds_read_b128 v[24:27], v53 offset:43008\n\t"
    "ds_read_b128 v[28:31], v53 offset:45056\n\t"
    "v_mfma_f32_16x16x32_bf16 v[144:147], a[180:183], v[8:11], v[144:147]\n\t"
    "ds_read_b128 v[32:35], v53 offset:47104\n\t"
    "ds_read_b128 v[36:39], v53 offset:49152\n\t"
    "v_mfma_f32_16x16x32_bf16 v[176:179], a[180:183], v[12:15], v[176:179]\n\t"
    "ds_read_b64_tr_b16 a[212:213], v50 offset:16384\n\t"
    "ds_read_b64_tr_b16 a[214:215], v50 offset:16640\n\t"
    "v_mfma_f32_16x16x32_bf16 v[84:87], a[184:187], v[0:3], v[84:87]\n\t"
    "buffer_load_dwordx4 v214, s[36:39], s40 offen offset:0 nt lds\n\t"
    "v_mfma_f32_16x16x32_bf16 v[116:119], a[184:187], v[4:7], v[116:119]\n\t"
    "ds_read_b64_tr_b16 a[216:217], v51 offset:16384\n\t"
    "ds_read_b64_tr_b16 a[218:219], v51 offset:16640\n\t"
    "v_mfma_f32_16x16x32_bf16 v[148:151], a[184:187], v[8:11], v[148:151]\n\t"
    "ds_read_b64_tr_b16 a[220:221], v50 offset:16896\n\t"
    "ds_read_b64_tr_b16 a[222:223], v50 offset:17152\n\t"
    "v_mfma_f32_16x16x32_bf16 v[180:183], a[184:187], v[12:15], v[180:183]\n\t"
    "buffer_load_dwordx4 v215, s[36:39], s40 offen offset:1024 nt lds\n\t"
    "v_mfma_f32_16x16x32_bf16 v[88:91], a[188:191], v[0:3], v[88:91]\n\t"
    "ds_read_b64_tr_b16 a[224:225], v51 offset:16896\n\t"
    "ds_read_b64_tr_b16 a[226:227], v51 offset:17152\n\t"
    "v_mfma_f32_16x16x32_bf16 v[120:123], a[188:191], v[4:7], v[120:123]\n\t"
    "ds_read_b64_tr_b16 a[228:229], v50 offset:17408\n\t"
    "ds_read_b64_tr_b16 a[230:231], v50 offset:17664\n\t"
    "v_mfma_f32_16x16x32_bf16 v[152:155], a[188:191], v[8:11], v[152:155]\n\t"
    "ds_read_b64_tr_b16 a[232:233], v51 offset:17408\n\t"
    "ds_read_b64_tr_b16 a[234:235], v51 offset:17664\n\t"
    "v_mfma_f32_16x16x32_bf16 v[184:187], a[188:191], v[12:15], v[184:187]\n\t"
    "buffer_load_dwordx4 v216, s[36:39], s40 offen offset:2048 nt lds\n\t"
    "v_mfma_f32_16x16x32_bf16 v[92:95], a[192:195], v[0:3], v[92:95]\n\t"
    "ds_read_b64_tr_b16 a[236:237], v50 offset:17920\n\t"
    "ds_read_b64_tr_b16 a[238:239], v50 offset:18176\n\t"
    "v_mfma_f32_16x16x32_bf16 v[124:127], a[192:195], v[4:7], v[124:127]\n\t"
    "buffer_load_dwordx4 v217, s[36:39], s40 offen offset:3072 nt lds\n\t"
    "v_mfma_f32_16x16x32_bf16 v[156:159], a[192:195], v[8:11], v[156:159]\n\t"
    "s_add_u32 s41, s41, s58\n\t"
    "s_sub_u32 s58, 0x8000, s58\n\t"
    "v_mfma_f32_16x16x32_bf16 v[188:191], a[192:195], v[12:15], v[188:191]\n\t"
    "s_cmp_eq_u32 s41, s43\n\t"
    "s_cselect_b32 s41, s42, s41\n\t"
    "v_mfma_f32_16x16x32_bf16 v[96:99], a[196:199], v[0:3], v[96:99]\n\t"
    "s_add_u32 s40, s40, s59\n\t"
    "s_mov_b32 m0, s41\n\t"
    "v_mfma_f32_16x16x32_bf16 v[128:131], a[196:199], v[4:7], v[128:131]\n\t"
    "ds_read_b64_tr_b16 a[240:241], v51 offset:17920\n\t"
    "ds_read_b64_tr_b16 a[242:243], v51 offset:18176\n\t"
    "v_mfma_f32_16x16x32_bf16 v[160:163], a[196:199], v[8:11], v[160:163]\n\t"
    "v_add_u32 v50, 51200, v50\n\t"
    "v_add_u32 v51, 51200, v51\n\t"
    "v_mfma_f32_16x16x32_bf16 v[192:195], a[196:199], v[12:15], v[192:195]\n\t"
    "buffer_load_dwordx4 v214, s[36:39], s40 offen offset:0 nt lds\n\t"
    "v_mfma_f32_16x16x32_bf16 v[224:227], a[196:199], v[16:19], v[224:227]\n\t"
    "v_add_u32 v52, 51200, v52\n\t"
    "v_add_u32 v53, 51200, v53\n\t"
    "v_mfma_f32_16x16x32_bf16 v[100:103], a[200:203], v[0:3], v[100:103]\n\t"
    "v_mfma_f32_16x16x32_bf16 v[132:135], a[200:203], v[4:7], v[132:135]\n\t"
    "buffer_load_dwordx4 v215, s[36:39], s40 offen offset:1024 nt lds\n\t"
    "v_mfma_f32_16x16x32_bf16 v[164:167], a[200:203], v[8:11], v[164:167]\n\t"
    "v_mfma_f32_16x16x32_bf16 v[196:199], a[200:203], v[12:15], v[196:199]\n\t"
    "v_mfma_f32_16x16x32_bf16 v[228:231], a[200:203], v[16:19], v[228:231]\n\t"
    "v_mfma_f32_16x16x32_bf16 v[104:107], a[204:207], v[0:3], v[104:107]\n\t"
    "buffer_load_dwordx4 v216, s[36:39], s40 offen offset:2048 nt lds\n\t"
    "v_mfma_f32_16x16x32_bf16 v[136:139], a[204:207], v[4:7], v[136:139]\n\t"
    "v_mfma_f32_16x16x32_bf16 v[168:171], a[204:207], v[8:11], v[168:171]\n\t"
    "v_mfma_f32_16x16x32_bf16 v[200:203], a[204:207], v[12:15], v[200:203]\n\t"
    "buffer_load_dwordx4 v217, s[36:39], s40 offen offset:3072 nt lds\n\t"
    "v_mfma_f32_16x16x32_bf16 v[232:235], a[204:207], v[16:19], v[232:235]\n\t"
    "s_add_u32 s41, s41, s58\n\t"
    "s_sub_u32 s58, 0x8000, s58\n\t"
    "v_mfma_f32_16x16x32_bf16 v[108:111], a[208:211], v[0:3], v[108:111]\n\t"
    "s_cmp_eq_u32 s41, s43\n\t"
    "s_cselect_b32 s41, s42, s41\n\t"
    "v_mfma_f32_16x16x32_bf16 v[140:143], a[208:211], v[4:7], v[140:143]\n\t"
    "s_add_u32 s40, s40, s59\n\t"
    "v_mfma_f32_16x16x32_bf16 v[172:175], a[208:211], v[8:11], v[172:175]\n\t"
    "s_waitcnt vmcnt(12) lgkmcnt(0)\n\t"
    "s_barrier\n\t"
    "v_mfma_f32_16x16x32_bf16 v[204:207], a[208:211], v[12:15], v[204:207]\n\t"
    "v_mfma_f32_16x16x32_bf16 v[236:239], a[208:211], v[16:19], v[236:239]\n\t"
    "v_mfma_f32_16x16x32_bf16 v[80:83], a[212:215], v[20:23], v[80:83]\n\t"
    "s_mov_b32 m0, s41\n\t"
    "ds_read_b128 v[0:3], v52 offset:40960\n\t"
    "v_mfma_f32_16x16x32_bf16 v[112:115], a[212:215], v[24:27], v[112:115]\n\t"
    "ds_read_b128 v[4:7], v52 offset:43008\n\t"
    "ds_read_b128 v[8:11], v52 offset:45056\n\t"
    "v_mfma_f32_16x16x32_bf16 v[144:147], a[212:215], v[28:31], v[144:147]\n\t"
    "ds_read_b128 v[12:15], v52 offset:47104\n\t"
    "ds_read_b128 v[16:19], v52 offset:49152\n\t"
    "v_mfma_f32_16x16x32_bf16 v[176:179], a[212:215], v[32:35], v[176:179]\n\t"
    "ds_read_b64_tr_b16 a[180:181], v50 offset:0\n\t"
    "ds_read_b64_tr_b16 a[182:183], v50 offset:256\n\t"
    "v_mfma_f32_16x16x32_bf16 v[84:87], a[216:219], v[20:23], v[84:87]\n\t"
    "buffer_load_dwordx4 v214, s[36:39], s40 offen offset:0 nt lds\n\t"
    "v_mfma_f32_16x16x32_bf16 v[116:119], a[216:219], v[24:27], v[116:119]\n\t"
    "ds_read_b64_tr_b16 a[184:185], v51 offset:0\n\t"
    "ds_read_b64_tr_b16 a[186:187], v51 offset:256\n\t"
    "v_mfma_f32_16x16x32_bf16 v[148:151], a[216:219], v[28:31], v[148:151]\n\t"
    "ds_read_b64_tr_b16 a[188:189], v50 offset:512\n\t"
    "ds_read_b64_tr_b16 a[190:191], v50 offset:768\n\t"
    "v_mfma_f32_16x16x32_bf16 v[180:183], a[216:219], v[32:35], v[180:183]\n\t"
    "ds_read_b64_tr_b16 a[192:193], v51 offset:512\n\t"
    "ds_read_b64_tr_b16 a[194:195], v51 offset:768\n\t"
    "v_mfma_f32_16x16x32_bf16 v[88:91], a[220:223], v[20:23], v[88:91]\n\t"
    "ds_read_b64_tr_b16 a[196:197], v50 offset:1024\n\t"
    "ds_read_b64_tr_b16 a[198:199], v50 offset:1280\n\t"
    "v_mfma_f32_16x16x32_bf16 v[120:123], a[220:223], v[24:27], v[120:123]\n\t"
    "ds_read_b64_tr_b16 a[200:201], v51 offset:1024\n\t"
    "ds_read_b64_tr_b16 a[202:203], v51 offset:1280\n\t"
    "v_mfma_f32_16x16x32_bf16 v[152:155], a[220:223], v[28:31], v[152:155]\n\t"
    "ds_read_b64_tr_b16 a[204:205], v50 offset:1536\n\t"
    "ds_read_b64_tr_b16 a[206:207], v50 offset:1792\n\t"
    "v_mfma_f32_16x16x32_bf16 v[184:187], a[220:223], v[32:35], v[184:187]\n\t"
    "buffer_load_dwordx4 v215, s[36:39], s40 offen offset:1024 nt lds\n\t"
    "v_mfma_f32_16x16x32_bf16 v[92:95], a[224:227], v[20:23], v[92:95]\n\t"
    "ds_read_b64_tr_b16 a[208:209], v51 offset:1536\n\t"
    "ds_read_b64_tr_b16 a[210:211], v51 offset:1792\n\t"
    "v_mfma_f32_16x16x32_bf16 v[124:127], a[224:227], v[24:27], v[124:127]\n\t"
    "v_mfma_f32_16x16x32_bf16 v[156:159], a[224:227], v[28:31], v[156:159]\n\t"
    "v_mfma_f32_16x16x32_bf16 v[188:191], a[224:227], v[32:35], v[188:191]\n\t"
    "v_mfma_f32_16x16x32_bf16 v[96:99], a[228:231], v[20:23], v[96:99]\n\t"
    "v_mfma_f32_16x16x32_bf16 v[128:131], a[228:231], v[24:27], v[128:131]\n\t"
    "v_mfma_f32_16x16x32_bf16 v[160:163], a[228:231], v[28:31], v[160:163]\n\t"
    "v_mfma_f32_16x16x32_bf16 v[192:195], a[228:231], v[32:35], v[192:195]\n\t"
    "buffer_load_dwordx4 v216, s[36:39], s40 offen offset:2048 nt lds\n\t"
    "v_mfma_f32_16x16x32_bf16 v[224:227], a[228:231], v[36:39], v[224:227]\n\t"
    "v_mfma_f32_16x16x32_bf16 v[100:103], a[232:235], v[20:23], v[100:103]\n\t"
    "v_mfma_f32_16x16x32_bf16 v[132:135], a[232:235], v[24:27], v[132:135]\n\t"
    "v_mfma_f32_16x16x32_bf16 v[164:167], a[232:235], v[28:31], v[164:167]\n\t"
    "v_mfma_f32_16x16x32_bf16 v[196:199], a[232:235], v[32:35], v[196:199]\n\t"
    "v_mfma_f32_16x16x32_bf16 v[228:231], a[232:235], v[36:39], v[228:231]\n\t"
    "v_mfma_f32_16x16x32_bf16 v[104:107], a[236:239], v[20:23], v[104:107]\n\t"
    "buffer_load_dwordx4 v217, s[36:39], s40 offen offset:3072 nt lds\n\t"
    "v_mfma_f32_16x16x32_bf16 v[136:139], a[236:239], v[24:27], v[136:139]\n\t"
    "v_mfma_f32_16x16x32_bf16 v[168:171], a[236:239], v[28:31], v[168:171]\n\t"
    "v_mfma_f32_16x16x32_bf16 v[200:203], a[236:239], v[32:35], v[200:203]\n\t"
    "s_add_u32 s41, s41, s58\n\t"
    "s_sub_u32 s58, 0x8000, s58\n\t"
    "v_mfma_f32_16x16x32_bf16 v[232:235], a[236:239], v[36:39], v[232:235]\n\t"
    "s_cmp_eq_u32 s41, s43\n\t"
    "s_cselect_b32 s41, s42, s41\n\t"
    "v_mfma_f32_16x16x32_bf16 v[108:111], a[240:243], v[20:23], v[108:111]\n\t"
    "s_add_u32 s40, s40, s59\n\t"
    "v_mfma_f32_16x16x32_bf16 v[140:143], a[240:243], v[24:27], v[140:143]\n\t"
    "v_mfma_f32_16x16x32_bf16 v[172:175], a[240:243], v[28:31], v[172:175]\n\t"
    "s_waitcnt vmcnt(12) lgkmcnt(0)\n\t"
    "s_barrier\n\t"
    "v_mfma_f32_16x16x32_bf16 v[204:207], a[240:243], v[32:35], v[204:207]\n\t"
    "v_mfma_f32_16x16x32_bf16 v[236:239], a[240:243], v[36:39], v[236:239]\n\t"
    "v_mfma_f32_16x16x32_bf16 v[80:83], a[180:183], v[0:3], v[80:83]\n\t"
    "s_mov_b32 m0, s41\n\t"
    "ds_read_b128 v[20:23], v53 offset:40960\n\t"
    "v_mfma_f32_16x16x32_bf16 v[112:115], a[180:183], v[4:7], v[112:115]\n\t"
    "ds_read_b128 v[24:27], v53 offset:43008\n\t"
    "ds_read_b128 v[28:31], v53 offset:45056\n\t"
    "v_mfma_f32_16x16x32_bf16 v[144:147], a[180:183], v[8:11], v[144:147]\n\t"
    "ds_read_b128 v[32:35], v53 offset:47104\n\t"
    "ds_read_b128 v[36:39], v53 offset:49152\n\t"
    "v_mfma_f32_16x16x32_bf16 v[176:179], a[180:183], v[12:15], v[176:179]\n\t"
    "ds_read_b64_tr_b16 a[212:213], v50 offset:16384\n\t"
    "ds_read_b64_tr_b16 a[214:215], v50 offset:16640\n\t"
    "v_mfma_f32_16x16x32_bf16 v[84:87], a[184:187], v[0:3], v[84:87]\n\t"
    "buffer_load_dwordx4 v214, s[36:39], s40 offen offset:0 nt lds\n\t"
    "v_mfma_f32_16x16x32_bf16 v[116:119], a[184:187], v[4:7], v[116:119]\n\t"
    "ds_read_b64_tr_b16 a[216:217], v51 offset:16384\n\t"
    "ds_read_b64_tr_b16 a[218:219], v51 offset:16640\n\t"
    "v_mfma_f32_16x16x32_bf16 v[148:151], a[184:187], v[8:11], v[148:151]\n\t"
    "ds_read_b64_tr_b16 a[220:221], v50 offset:16896\n\t"
    "ds_read_b64_tr_b16 a[222:223], v50 offset:17152\n\t"
    "v_mfma_f32_16x16x32_bf16 v[180:183], a[184:187], v[12:15], v[180:183]\n\t"
    "ds_read_b64_tr_b16 a[224:225], v51 offset:16896\n\t"
    "ds_read_b64_tr_b16 a[226:227], v51 offset:17152\n\t"
    "v_mfma_f32_16x16x32_bf16 v[88:91], a[188:191], v[0:3], v[88:91]\n\t"
    "ds_read_b64_tr_b16 a[228:229], v50 offset:17408\n\t"
    "ds_read_b64_tr_b16 a[230:231], v50 offset:17664\n\t"
    "v_mfma_f32_16x16x32_bf16 v[120:123], a[188:191], v[4:7], v[120:123]\n\t"
    "ds_read_b64_tr_b16 a[232:233], v51 offset:17408\n\t"
    "ds_read_b64_tr_b16 a[234:235], v51 offset:17664\n\t"
    "v_mfma_f32_16x16x32_bf16 v[152:155], a[188:191], v[8:11], v[152:155]\n\t"
    "ds_read_b64_tr_b16 a[236:237], v50 offset:17920\n\t"
    "ds_read_b64_tr_b16 a[238:239], v50 offset:18176\n\t"
    "v_mfma_f32_16x16x32_bf16 v[184:187], a[188:191], v[12:15], v[184:187]\n\t"
    "buffer_load_dwordx4 v215, s[36:39], s40 offen offset:1024 nt lds\n\t"
    "v_mfma_f32_16x16x32_bf16 v[92:95], a[192:195], v[0:3], v[92:95]\n\t"
    "ds_read_b64_tr_b16 a[240:241], v51 offset:17920\n\t"
    "ds_read_b64_tr_b16 a[242:243], v51 offset:18176\n\t"
    "v_mfma_f32_16x16x32_bf16 v[124:127], a[192:195], v[4:7], v[124:127]\n\t"
    "v_mfma_f32_16x16x32_bf16 v[156:159], a[192:195], v[8:11], v[156:159]\n\t"
    "v_mfma_f32_16x16x32_bf16 v[188:191], a[192:195], v[12:15], v[188:191]\n\t"
    "v_mfma_f32_16x16x32_bf16 v[96:99], a[196:199], v[0:3], v[96:99]\n\t"
    "v_mfma_f32_16x16x32_bf16 v[128:131], a[196:199], v[4:7], v[128:131]\n\t"
    "v_mfma_f32_16x16x32_bf16 v[160:163], a[196:199], v[8:11], v[160:163]\n\t"
    "v_mfma_f32_16x16x32_bf16 v[192:195], a[196:199], v[12:15], v[192:195]\n\t"
    "buffer_load_dwordx4 v216, s[36:39], s40 offen offset:2048 nt lds\n\t"
    "v_mfma_f32_16x16x32_bf16 v[224:227], a[196:199], v[16:19], v[224:227]\n\t"
    "v_mfma_f32_16x16x32_bf16 v[100:103], a[200:203], v[0:3], v[100:103]\n\t"
    "v_mfma_f32_16x16x32_bf16 v[132:135], a[200:203], v[4:7], v[132:135]\n\t"
    "v_mfma_f32_16x16x32_bf16 v[164:167], a[200:203], v[8:11], v[164:167]\n\t"
    "v_mfma_f32_16x16x32_bf16 v[196:199], a[200:203], v[12:15], v[196:199]\n\t"
    "v_mfma_f32_16x16x32_bf16 v[228:231], a[200:203], v[16:19], v[228:231]\n\t"
    "v_mfma_f32_16x16x32_bf16 v[104:107], a[204:207], v[0:3], v[104:107]\n\t"
    "buffer_load_dwordx4 v217, s[36:39], s40 offen offset:3072 nt lds\n\t"
    "v_mfma_f32_16x16x32_bf16 v[136:139], a[204:207], v[4:7], v[136:139]\n\t"
    "v_mfma_f32_16x16x32_bf16 v[168:171], a[204:207], v[8:11], v[168:171]\n\t"
    "v_mfma_f32_16x16x32_bf16 v[200:203], a[204:207], v[12:15], v[200:203]\n\t"
    "s_add_u32 s41, s41, s58\n\t"
    "s_sub_u32 s58, 0x8000, s58\n\t"
    "v_mfma_f32_16x16x32_bf16 v[232:235], a[204:207], v[16:19], v[232:235]\n\t"
    "s_cmp_eq_u32 s41, s43\n\t"
    "s_cselect_b32 s41, s42, s41\n\t"
    "v_mfma_f32_16x16x32_bf16 v[108:111], a[208:211], v[0:3], v[108:111]\n\t"
    "s_add_u32 s40, s40, s59\n\t"
    "v_mfma_f32_16x16x32_bf16 v[140:143], a[208:211], v[4:7], v[140:143]\n\t"
    "v_mfma_f32_16x16x32_bf16 v[172:175], a[208:211], v[8:11], v[172:175]\n\t"
    "s_waitcnt lgkmcnt(0)\n\t"
    "s_barrier\n\t"
    "v_mfma_f32_16x16x32_bf16 v[204:207], a[208:211], v[12:15], v[204:207]\n\t"
    "v_mfma_f32_16x16x32_bf16 v[236:239], a[208:211], v[16:19], v[236:239]\n\t"
    "v_mfma_f32_16x16x32_bf16 v[224:227], a[228:231], v[36:39], v[224:227]\n\t"
    "s_mov_b32 m0, s41\n\t"
    "v_mfma_f32_16x16x32_bf16 v[228:231], a[232:235], v[36:39], v[228:231]\n\t"
    "v_mfma_f32_16x16x32_bf16 v[232:235], a[236:239], v[36:39], v[232:235]\n\t"
    "v_mfma_f32_16x16x32_bf16 v[236:239], a[240:243], v[36:39], v[236:239]\n\t"
    "v_mfma_f32_16x16x32_bf16 v[176:179], a[212:215], v[32:35], v[176:179]\n\t"
    "buffer_load_dwordx4 v214, s[36:39], s40 offen offset:0 nt lds\n\t"
    "v_mfma_f32_16x16x32_bf16 v[180:183], a[216:219], v[32:35], v[180:183]\n\t"
    "v_mul_f32 v224, s64, v224\n\t"
    "v_mul_f32 v225, s64, v225\n\t"
    "v_mfma_f32_16x16x32_bf16 v[184:187], a[220:223], v[32:35], v[184:187]\n\t"
    "v_mul_f32 v226, s64, v226\n\t"
    "v_mul_f32 v227, s64, v227\n\t"
    "v_mfma_f32_16x16x32_bf16 v[188:191], a[224:227], v[32:35], v[188:191]\n\t"
    "buffer_load_dwordx4 v215, s[36:39], s40 offen offset:1024 nt lds\n\t"
    "v_mfma_f32_16x16x32_bf16 v[192:195], a[228:231], v[32:35], v[192:195]\n\t"
    "v_mul_f32 v228, s64, v228\n\t"
    "v_mul_f32 v229, s64, v229\n\t"
    "v_mfma_f32_16x16x32_bf16 v[196:199], a[232:235], v[32:35], v[196:199]\n\t"
    "v_mul_f32 v230, s64, v230\n\t"
    "v_mul_f32 v231, s64, v231\n\t"
    "v_mfma_f32_16x16x32_bf16 v[200:203], a[236:239], v[32:35], v[200:203]\n\t"
    "v_cvt_pk_bf16_f32 v72, v224, v225\n\t"
    "v_cvt_pk_bf16_f32 v73, v226, v227\n\t"
    "v_mfma_f32_16x16x32_bf16 v[204:207], a[240:243], v[32:35], v[204:207]\n\t"
    "buffer_load_dwordx4 v216, s[36:39], s40 offen offset:2048 nt lds\n\t"
    "v_mfma_f32_16x16x32_bf16 v[80:83], a[212:215], v[20:23], v[80:83]\n\t"
    "v_cvt_pk_bf16_f32 v74, v228, v229\n\t"
    "v_cvt_pk_bf16_f32 v75, v230, v231\n\t"
    "v_mfma_f32_16x16x32_bf16 v[84:87], a[216:219], v[20:23], v[84:87]\n\t"
    "buffer_load_dwordx4 v217, s[36:39], s40 offen offset:3072 nt lds\n\t"
    "v_mfma_f32_16x16x32_bf16 v[88:91], a[220:223], v[20:23], v[88:91]\n\t"
    "s_add_u32 s41, s41, s58\n\t"
    "s_sub_u32 s58, 0x8000, s58\n\t"
    "v_mfma_f32_16x16x32_bf16 v[92:95], a[224:227], v[20:23], v[92:95]\n\t"
    "s_cmp_eq_u32 s41, s43\n\t"
    "s_cselect_b32 s41, s42, s41\n\t"
    "v_mfma_f32_16x16x32_bf16 v[96:99], a[228:231], v[20:23], v[96:99]\n\t"
    "s_add_u32 s40, s40, s59\n\t"
    "s_mov_b32 m0, s41\n\t"
    "v_mfma_f32_16x16x32_bf16 v[100:103], a[232:235], v[20:23], v[100:103]\n\t"
    "v_mul_f32 v232, s64, v232\n\t"
    "v_mul_f32 v233, s64, v233\n\t"
    "v_mfma_f32_16x16x32_bf16 v[104:107], a[236:239], v[20:23], v[104:107]\n\t"
    "v_mul_f32 v234, s64, v234\n\t"
    "v_mul_f32 v235, s64, v235\n\t"
    "v_mfma_f32_16x16x32_bf16 v[108:111], a[240:243], v[20:23], v[108:111]\n\t"
    "buffer_load_dwordx4 v214, s[36:39], s40 offen offset:0 nt lds\n\t"
    "v_mfma_f32_16x16x32_bf16 v[144:147], a[212:215], v[28:31], v[144:147]\n\t"
    "v_mul_f32 v236, s64, v236\n\t"
    "v_mul_f32 v237, s64, v237\n\t"
    "v_mfma_f32_16x16x32_bf16 v[148:151], a[216:219], v[28:31], v[148:151]\n\t"
    "v_mul_f32 v238, s64, v238\n\t"
    "v_mul_f32 v239, s64, v239\n\t"
    "v_mfma_f32_16x16x32_bf16 v[152:155], a[220:223], v[28:31], v[152:155]\n\t"
    "buffer_load_dwordx4 v215, s[36:39], s40 offen offset:1024 nt lds\n\t"
    "v_mfma_f32_16x16x32_bf16 v[156:159], a[224:227], v[28:31], v[156:159]\n\t"
    "v_cvt_pk_bf16_f32 v76, v232, v233\n\t"
    "v_cvt_pk_bf16_f32 v77, v234, v235\n\t"
    "v_mfma_f32_16x16x32_bf16 v[160:163], a[228:231], v[28:31], v[160:163]\n\t"
    "v_cvt_pk_bf16_f32 v78, v236, v237\n\t"
    "v_cvt_pk_bf16_f32 v79, v238, v239\n\t"
    "v_mfma_f32_16x16x32_bf16 v[164:167], a[232:235], v[28:31], v[164:167]\n\t"
    "v_permlane16_swap_b32 v72, v74\n\t"
    "v_permlane16_swap_b32 v73, v75\n\t"
    "v_mfma_f32_16x16x32_bf16 v[168:171], a[236:239], v[28:31], v[168:171]\n\t"
    "buffer_load_dwordx4 v216, s[36:39], s40 offen offset:2048 nt lds\n\t"
    "v_mfma_f32_16x16x32_bf16 v[172:175], a[240:243], v[28:31], v[172:175]\n\t"
    "v_permlane16_swap_b32 v76, v78\n\t"
    "v_permlane16_swap_b32 v77, v79\n\t"
    "v_mfma_f32_16x16x32_bf16 v[112:115], a[212:215], v[24:27], v[112:115]\n\t"
    "ds_write_b128 v245, v[72:75] offset:0\n\t"
    "v_mfma_f32_16x16x32_bf16 v[116:119], a[216:219], v[24:27], v[116:119]\n\t"
    "buffer_load_dwordx4 v217, s[36:39], s40 offen offset:3072 nt lds\n\t"
    "v_mfma_f32_16x16x32_bf16 v[120:123], a[220:223], v[24:27], v[120:123]\n\t"
    "s_add_u32 s41, s41, s58\n\t"
    "s_sub_u32 s58, 0x8000, s58\n\t"
    "v_mfma_f32_16x16x32_bf16 v[124:127], a[224:227], v[24:27], v[124:127]\n\t"
    "s_cmp_eq_u32 s41, s43\n\t"
    "s_cselect_b32 s41, s42, s41\n\t"
    "v_mfma_f32_16x16x32_bf16 v[128:131], a[228:231], v[24:27], v[128:131]\n\t"
    "s_add_u32 s40, s40, s59\n\t"
    "v_mfma_f32_16x16x32_bf16 v[132:135], a[232:235], v[24:27], v[132:135]\n\t"
    "ds_write_b128 v245, v[76:79] offset:1024\n\t"
    "v_mfma_f32_16x16x32_bf16 v[136:139], a[236:239], v[24:27], v[136:139]\n\t"
    "v_mul_f32 v176, s64, v176\n\t"
    "v_mul_f32 v177, s64, v177\n\t"
    "v_mfma_f32_16x16x32_bf16 v[140:143], a[240:243], v[24:27], v[140:143]\n\t"
    "v_mul_f32 v178, s64, v178\n\t"
    "v_mul_f32 v179, s64, v179\n\t"
    "s_add_u32 s53, s42, 98304\n\t"
    "v_add_u32 v208, s53, v211\n\t"
    "v_add_u32 v209, s53, v212\n\t"
    "v_xor_b32 v245, 0x800, v245\n\t"
    "s_waitcnt lgkmcnt(0)\n\t"
    "s_barrier\n\t"
    "ds_read_b128 a[180:183], v208 offset:0\n\t"
    "ds_read_b128 a[184:187], v209 offset:0\n\t"
    "ds_read_b128 a[188:191], v208 offset:1024\n\t"
    "ds_read_b128 a[192:195], v209 offset:1024\n\t"
    "ds_read_b128 a[196:199], v208 offset:4096\n\t"
    "ds_read_b128 a[200:203], v209 offset:4096\n\t"
    "ds_read_b128 a[204:207], v208 offset:5120\n\t"
    "ds_read_b128 a[208:211], v209 offset:5120\n\t"
    "ds_read_b128 v[64:67], v245 offset:0\n\t"
    "ds_read_b128 v[68:71], v245 offset:1024\n\t"
    "s_cmp_lg_u32 s65, 0\n\t"
    "s_mov_b32 m0, s41\n\t"
    "s_cbranch_scc1 QSK_%=\n\t"
    "buffer_load_dwordx4 v214, s[36:39], s40 offen offset:0 nt lds\n\t"
    "buffer_load_dwordx4 v215, s[36:39], s40 offen offset:1024 nt lds\n\t"
    "buffer_load_dwordx4 v216, s[36:39], s40 offen offset:2048 nt lds\n\t"
    "buffer_load_dwordx4 v217, s[36:39], s40 offen offset:3072 nt lds\n\t"
    "s_add_u32 s40, s40, 8192\n\t"
    "QSK_%=:\n\t"
    "v_mul_f32 v180, s64, v180\n\t"
    "v_mul_f32 v181, s64, v181\n\t"
    "v_mul_f32 v182, s64, v182\n\t"
    "v_mul_f32 v183, s64, v183\n\t"
    "v_cvt_pk_bf16_f32 v48, v176, v177\n\t"
    "v_cvt_pk_bf16_f32 v49, v178, v179\n\t"
    "v_cvt_pk_bf16_f32 v50, v180, v181\n\t"
    "v_cvt_pk_bf16_f32 v51, v182, v183\n\t"
    "v_mul_f32 v184, s64, v184\n\t"
    "v_mul_f32 v185, s64, v185\n\t"
    "v_mul_f32 v186, s64, v186\n\t"
    "v_mul_f32 v187, s64, v187\n\t"
    "v_mul_f32 v188, s64, v188\n\t"
    "v_mul_f32 v189, s64, v189\n\t"
    "v_mul_f32 v190, s64, v190\n\t"
    "v_mul_f32 v191, s64, v191\n\t"
    "v_cvt_pk_bf16_f32 v52, v184, v185\n\t"
    "v_cvt_pk_bf16_f32 v53, v186, v187\n\t"
    "v_cvt_pk_bf16_f32 v54, v188, v189\n\t"
    "v_cvt_pk_bf16_f32 v55, v190, v191\n\t"
    "v_mul_f32 v192, s64, v192\n\t"
    "v_mul_f32 v193, s64, v193\n\t"
    "v_mul_f32 v194, s64, v194\n\t"
    "v_mul_f32 v195, s64, v195\n\t"
    "v_mul_f32 v196, s64, v196\n\t"
    "v_mul_f32 v197, s64, v197\n\t"
    "v_mul_f32 v198, s64, v198\n\t"
    "v_mul_f32 v199, s64, v199\n\t"
    "v_cvt_pk_bf16_f32 v56, v192, v193\n\t"
    "v_cvt_pk_bf16_f32 v57, v194, v195\n\t"
    "v_cvt_pk_bf16_f32 v58, v196, v197\n\t"
    "v_cvt_pk_bf16_f32 v59, v198, v199\n\t"
    "v_mul_f32 v200, s64, v200\n\t"
    "v_mul_f32 v201, s64, v201\n\t"
    "v_mul_f32 v202, s64, v202\n\t"
    "v_mul_f32 v203, s64, v203\n\t"
    "v_mul_f32 v204, s64, v204\n\t"
    "v_mul_f32 v205, s64, v205\n\t"
    "v_mul_f32 v206, s64, v206\n\t"
    "v_mul_f32 v207, s64, v207\n\t"
    "v_cvt_pk_bf16_f32 v60, v200, v201\n\t"
    "v_cvt_pk_bf16_f32 v61, v202, v203\n\t"
    "v_cvt_pk_bf16_f32 v62, v204, v205\n\t"
    "v_cvt_pk_bf16_f32 v63, v206, v207\n\t"
    "v_permlane16_swap_b32 v48, v50\n\t"
    "v_permlane16_swap_b32 v49, v51\n\t"
    "v_permlane16_swap_b32 v52, v54\n\t"
    "v_permlane16_swap_b32 v53, v55\n\t"
    "v_permlane16_swap_b32 v56, v58\n\t"
    "v_permlane16_swap_b32 v57, v59\n\t"
    "v_permlane16_swap_b32 v60, v62\n\t"
    "v_permlane16_swap_b32 v61, v63\n\t"
    "v_mul_f32 v80, s64, v80\n\t"
    "v_mul_f32 v81, s64, v81\n\t"
    "v_mul_f32 v82, s64, v82\n\t"
    "v_mul_f32 v83, s64, v83\n\t"
    "v_mul_f32 v84, s64, v84\n\t"
    "v_mul_f32 v85, s64, v85\n\t"
    "v_mul_f32 v86, s64, v86\n\t"
    "v_mul_f32 v87, s64, v87\n\t"
    "v_cvt_pk_bf16_f32 v0, v80, v81\n\t"
    "v_cvt_pk_bf16_f32 v1, v82, v83\n\t"
    "v_cvt_pk_bf16_f32 v2, v84, v85\n\t"
    "v_cvt_pk_bf16_f32 v3, v86, v87\n\t"
    "v_mul_f32 v88, s64, v88\n\t"
    "v_mul_f32 v89, s64, v89\n\t"
    "v_mul_f32 v90, s64, v90\n\t"
    "v_mul_f32 v91, s64, v91\n\t"
    "v_mul_f32 v92, s64, v92\n\t"
    "v_mul_f32 v93, s64, v93\n\t"
    "v_mul_f32 v94, s64, v94\n\t"
    "v_mul_f32 v95, s64, v95\n\t"
    "v_cvt_pk_bf16_f32 v4, v88, v89\n\t"
    "v_cvt_pk_bf16_f32 v5, v90, v91\n\t"
    "v_cvt_pk_bf16_f32 v6, v92, v93\n\t"
    "v_cvt_pk_bf16_f32 v7, v94, v95\n\t"
    "v_mul_f32 v96, s64, v96\n\t"
    "v_mul_f32 v97, s64, v97\n\t"
    "v_mul_f32 v98, s64, v98\n\t"
    "v_mul_f32 v99, s64, v99\n\t"
    "v_mul_f32 v100, s64, v100\n\t"
    "v_mul_f32 v101, s64, v101\n\t"
    "v_mul_f32 v102, s64, v102\n\t"
    "v_mul_f32 v103, s64, v103\n\t"
    "v_cvt_pk_bf16_f32 v8, v96, v97\n\t"
    "v_cvt_pk_bf16_f32 v9, v98, v99\n\t"
    "v_cvt_pk_bf16_f32 v10, v100, v101\n\t"
    "v_cvt_pk_bf16_f32 v11, v102, v103\n\t"
    "v_mul_f32 v104, s64, v104\n\t"
    "v_mul_f32 v105, s64, v105\n\t"
    "v_mul_f32 v106, s64, v106\n\t"
    "v_mul_f32 v107, s64, v107\n\t"
    "v_mul_f32 v108, s64, v108\n\t"
    "v_mul_f32 v109, s64, v109\n\t"
    "v_mul_f32 v110, s64, v110\n\t"
    "v_mul_f32 v111, s64, v111\n\t"
    "v_cvt_pk_bf16_f32 v12, v104, v105\n\t"
    "v_cvt_pk_bf16_f32 v13, v106, v107\n\t"
    "v_cvt_pk_bf16_f32 v14, v108, v109\n\t"
    "v_cvt_pk_bf16_f32 v15, v110, v111\n\t"
    "v_permlane16_swap_b32 v0, v2\n\t"
    "v_permlane16_swap_b32 v1, v3\n\t"
    "v_permlane16_swap_b32 v4, v6\n\t"
    "v_permlane16_swap_b32 v5, v7\n\t"
    "v_permlane16_swap_b32 v8, v10\n\t"
    "v_permlane16_swap_b32 v9, v11\n\t"
    "v_permlane16_swap_b32 v12, v14\n\t"
    "v_permlane16_swap_b32 v13, v15\n\t"
    "v_mul_f32 v144, s64, v144\n\t"
    "v_mul_f32 v145, s64, v145\n\t"
    "v_mul_f32 v146, s64, v146\n\t"
    "v_mul_f32 v147, s64, v147\n\t"
    "v_mul_f32 v148, s64, v148\n\t"
    "v_mul_f32 v149, s64, v149\n\t"
    "v_mul_f32 v150, s64, v150\n\t"
    "v_mul_f32 v151, s64, v151\n\t"
    "v_cvt_pk_bf16_f32 v32, v144, v145\n\t"
    "v_cvt_pk_bf16_f32 v33, v146, v147\n\t"
    "v_cvt_pk_bf16_f32 v34, v148, v149\n\t"
    "v_cvt_pk_bf16_f32 v35, v150, v151\n\t"
    "v_mul_f32 v152, s64, v152\n\t"
    "v_mul_f32 v153, s64, v153\n\t"
    "v_mul_f32 v154, s64, v154\n\t"
    "v_mul_f32 v155, s64, v155\n\t"
    "v_mul_f32 v156, s64, v156\n\t"
    "v_mul_f32 v157, s64, v157\n\t"
    "v_mul_f32 v158, s64, v158\n\t"
    "v_mul_f32 v159, s64, v159\n\t"
    "v_cvt_pk_bf16_f32 v36, v152, v153\n\t"
    "v_cvt_pk_bf16_f32 v37, v154, v155\n\t"
    "v_cvt_pk_bf16_f32 v38, v156, v157\n\t"
    "v_cvt_pk_bf16_f32 v39, v158, v159\n\t"
    "v_mul_f32 v160, s64, v160\n\t"
    "v_mul_f32 v161, s64, v161\n\t"
    "v_mul_f32 v162, s64, v162\n\t"
    "v_mul_f32 v163, s64, v163\n\t"
    "v_mul_f32 v164, s64, v164\n\t"
    "v_mul_f32 v165, s64, v165\n\t"
    "v_mul_f32 v166, s64, v166\n\t"
    "v_mul_f32 v167, s64, v167\n\t"
    "v_cvt_pk_bf16_f32 v40, v160, v161\n\t"
    "v_cvt_pk_bf16_f32 v41, v162, v163\n\t"
    "v_cvt_pk_bf16_f32 v42, v164, v165\n\t"
    "v_cvt_pk_bf16_f32 v43, v166, v167\n\t"
    "v_mul_f32 v168, s64, v168\n\t"
    "v_mul_f32 v169, s64, v169\n\t"
    "v_mul_f32 v170, s64, v170\n\t"
    "v_mul_f32 v171, s64, v171\n\t"
    "v_mul_f32 v172, s64, v172\n\t"
    "v_mul_f32 v173, s64, v173\n\t"
    "v_mul_f32 v174, s64, v174\n\t"
    "v_mul_f32 v175, s64, v175\n\t"
    "v_cvt_pk_bf16_f32 v44, v168, v169\n\t"
    "v_cvt_pk_bf16_f32 v45, v170, v171\n\t"
    "v_cvt_pk_bf16_f32 v46, v172, v173\n\t"
    "v_cvt_pk_bf16_f32 v47, v174, v175\n\t"
    "v_permlane16_swap_b32 v32, v34\n\t"
    "v_permlane16_swap_b32 v33, v35\n\t"
    "v_permlane16_swap_b32 v36, v38\n\t"
    "v_permlane16_swap_b32 v37, v39\n\t"
    "v_permlane16_swap_b32 v40, v42\n\t"
    "v_permlane16_swap_b32 v41, v43\n\t"
    "v_permlane16_swap_b32 v44, v46\n\t"
    "v_permlane16_swap_b32 v45, v47\n\t"
    "v_mul_f32 v112, s64, v112\n\t"
    "v_mul_f32 v113, s64, v113\n\t"
    "v_mul_f32 v114, s64, v114\n\t"
    "v_mul_f32 v115, s64, v115\n\t"
    "v_mul_f32 v116, s64, v116\n\t"
    "v_mul_f32 v117, s64, v117\n\t"
    "v_mul_f32 v118, s64, v118\n\t"
    "v_mul_f32 v119, s64, v119\n\t"
    "v_cvt_pk_bf16_f32 v16, v112, v113\n\t"
    "v_cvt_pk_bf16_f32 v17, v114, v115\n\t"
    "v_cvt_pk_bf16_f32 v18, v116, v117\n\t"
    "v_cvt_pk_bf16_f32 v19, v118, v119\n\t"
    "v_mul_f32 v120, s64, v120\n\t"
    "v_mul_f32 v121, s64, v121\n\t"
    "v_mul_f32 v122, s64, v122\n\t"
    "v_mul_f32 v123, s64, v123\n\t"
    "v_mul_f32 v124, s64, v124\n\t"
    "v_mul_f32 v125, s64, v125\n\t"
    "v_mul_f32 v126, s64, v126\n\t"
    "v_mul_f32 v127, s64, v127\n\t"
    "v_cvt_pk_bf16_f32 v20, v120, v121\n\t"
    "v_cvt_pk_bf16_f32 v21, v122, v123\n\t"
    "v_cvt_pk_bf16_f32 v22, v124, v125\n\t"
    "v_cvt_pk_bf16_f32 v23, v126, v127\n\t"
    "v_mul_f32 v128, s64, v128\n\t"
    "v_mul_f32 v129, s64, v129\n\t"
    "v_mul_f32 v130, s64, v130\n\t"
    "v_mul_f32 v131, s64, v131\n\t"
    "v_mul_f32 v132, s64, v132\n\t"
    "v_mul_f32 v133, s64, v133\n\t"
    "v_mul_f32 v134, s64, v134\n\t"
    "v_mul_f32 v135, s64, v135\n\t"
    "v_cvt_pk_bf16_f32 v24, v128, v129\n\t"
    "v_cvt_pk_bf16_f32 v25, v130, v131\n\t"
    "v_cvt_pk_bf16_f32 v26, v132, v133\n\t"
    "v_cvt_pk_bf16_f32 v27, v134, v135\n\t"
    "v_mul_f32 v136, s64, v136\n\t"
    "v_mul_f32 v137, s64, v137\n\t"
    "v_mul_f32 v138, s64, v138\n\t"
    "v_mul_f32 v139, s64, v139\n\t"
    "v_mul_f32 v140, s64, v140\n\t"
    "v_mul_f32 v141, s64, v141\n\t"
    "v_mul_f32 v142, s64, v142\n\t"
    "v_mul_f32 v143, s64, v143\n\t"
    "v_cvt_pk_bf16_f32 v28, v136, v137\n\t"
    "v_cvt_pk_bf16_f32 v29, v138, v139\n\t"
    "v_cvt_pk_bf16_f32 v30, v140, v141\n\t"
    "v_cvt_pk_bf16_f32 v31, v142, v143\n\t"
    "v_permlane16_swap_b32 v16, v18\n\t"
    "v_permlane16_swap_b32 v17, v19\n\t"
    "v_permlane16_swap_b32 v20, v22\n\t"
    "v_permlane16_swap_b32 v21, v23\n\t"
    "v_permlane16_swap_b32 v24, v26\n\t"
    "v_permlane16_swap_b32 v25, v27\n\t"
    "v_permlane16_swap_b32 v28, v30\n\t"
    "v_permlane16_swap_b32 v29, v31\n\t"
    "s_lshl_b32 s52, 0x4000, s65\n\t"
    "s_mov_b32 m0, s53\n\t"
    "s_add_u32 s41, s53, s52\n\t"
    "v_add_u32 v210, s41, v213\n\t"
    "v_add_u32 v208, s41, v211\n\t"
    "v_add_u32 v209, s41, v212\n\t"
    "s_cmp_eq_u32 s65, 0\n\t"
    "s_movk_i32 s98, 1245\n\t"
    "s_cmovk_i32 s98, 1445\n\t"
    "s_add_u32 s98, s98, s34\n\t"
    "s_cmp_eq_u32 s65, 0\n\t"
    "s_cbranch_scc1 PEEL_O_A_%=\n\t"
    "s_branch PEEL_O_B_%=\n\t"
    "PEEL_E_A_%=:\n\t"
    "s_waitcnt lgkmcnt(0)\n\t"
    "v_mfma_f32_16x16x32_bf16 v[80:83], a[180:183], v[0:3], 0\n\t"
    "v_mfma_f32_16x16x32_bf16 v[84:87], a[196:199], v[0:3], 0\n\t"
    "v_mfma_f32_16x16x32_bf16 v[80:83], a[184:187], v[4:7], v[80:83]\n\t"
    "v_mfma_f32_16x16x32_bf16 v[84:87], a[200:203], v[4:7], v[84:87]\n\t"
    "v_mfma_f32_16x16x32_bf16 v[80:83], a[188:191], v[8:11], v[80:83]\n\t"
    "v_mfma_f32_16x16x32_bf16 v[84:87], a[204:207], v[8:11], v[84:87]\n\t"
    "v_mfma_f32_16x16x32_bf16 v[80:83], a[192:195], v[12:15], v[80:83]\n\t"
    "v_mfma_f32_16x16x32_bf16 v[84:87], a[208:211], v[12:15], v[84:87]\n\t"
    "v_mfma_f32_16x16x32_bf16 v[88:91], a[180:183], v[16:19], 0\n\t"
    "v_mfma_f32_16x16x32_bf16 v[92:95], a[196:199], v[16:19], 0\n\t"
    "v_mfma_f32_16x16x32_bf16 v[88:91], a[184:187], v[20:23], v[88:91]\n\t"
    "v_max3_f32 v224, v80, v81, v82\n\t"
    "v_max3_f32 v225, v83, v84, v85\n\t"
    "v_max3_f32 v224, v224, v86, v87\n\t"
    "v_mfma_f32_16x16x32_bf16 v[92:95], a[200:203], v[20:23], v[92:95]\n\t"
    "v_max_f32 v224, v224, v225\n\t"
    "v_mov_b32 v225, v224\n\t"
    "s_nop 1\n\t"
    "v_permlane16_swap_b32 v224, v225\n\t"
    "v_mfma_f32_16x16x32_bf16 v[88:91], a[188:191], v[24:27], v[88:91]\n\t"
    "v_max_f32 v224, v224, v225\n\t"
    "v_mov_b32 v225, v224\n\t"
    "s_nop 1\n\t"
    "v_permlane32_swap_b32 v224, v225\n\t"
    "v_mfma_f32_16x16x32_bf16 v[92:95], a[204:207], v[24:27], v[92:95]\n\t"
    "v_max_f32 v224, v224, v225\n\t"
    "v_sub_f32 v188, 0, v224\n\t"
    "v_sub_f32 v189, 0, v224\n\t"
    "v_mfma_f32_16x16x32_bf16 v[88:91], a[192:195], v[28:31], v[88:91]\n\t"
    "v_sub_f32 v190, 0, v224\n\t"
    "v_sub_f32 v191, 0, v224\n\t"
    "v_sub_f32 v80, v80, v224\n\t"
    "v_mfma_f32_16x16x32_bf16 v[92:95], a[208:211], v[28:31], v[92:95]\n\t"
    "v_sub_f32 v81, v81, v224\n\t"
    "v_sub_f32 v82, v82, v224\n\t"
    "v_sub_f32 v83, v83, v224\n\t"
    "v_mfma_f32_16x16x32_bf16 v[96:99], a[180:183], v[32:35], 0\n\t"
    "v_mfma_f32_16x16x32_bf16 v[100:103], a[196:199], v[32:35], 0\n\t"
    "v_mfma_f32_16x16x32_bf16 v[96:99], a[184:187], v[36:39], v[96:99]\n\t"
    "v_sub_f32 v84, v84, v224\n\t"
    "v_sub_f32 v85, v85, v224\n\t"
    "v_sub_f32 v86, v86, v224\n\t"
    "v_mfma_f32_16x16x32_bf16 v[100:103], a[200:203], v[36:39], v[100:103]\n\t"
    "v_sub_f32 v87, v87, v224\n\t"
    "v_max3_f32 v226, v88, v89, v90\n\t"
    "v_max3_f32 v227, v91, v92, v93\n\t"
    "v_mfma_f32_16x16x32_bf16 v[96:99], a[188:191], v[40:43], v[96:99]\n\t"
    "v_max3_f32 v226, v226, v94, v95\n\t"
    "v_max_f32 v226, v226, v227\n\t"
    "v_mov_b32 v227, v226\n\t"
    "v_mfma_f32_16x16x32_bf16 v[100:103], a[204:207], v[40:43], v[100:103]\n\t"
    "s_nop 0\n\t"
    "v_permlane16_swap_b32 v226, v227\n\t"
    "v_max_f32 v226, v226, v227\n\t"
    "v_mov_b32 v227, v226\n\t"
    "v_mfma_f32_16x16x32_bf16 v[96:99], a[192:195], v[44:47], v[96:99]\n\t"
    "s_nop 0\n\t"
    "v_permlane32_swap_b32 v226, v227\n\t"
    "v_max_f32 v226, v226, v227\n\t"
    "v_sub_f32 v192, 0, v226\n\t"
    "v_mfma_f32_16x16x32_bf16 v[100:103], a[208:211], v[44:47], v[100:103]\n\t"
    "v_sub_f32 v193, 0, v226\n\t"
    "v_sub_f32 v194, 0, v226\n\t"
    "v_sub_f32 v195, 0, v226\n\t"
    "v_mfma_f32_16x16x32_bf16 v[104:107], a[180:183], v[48:51], 0\n\t"
    "v_mfma_f32_16x16x32_bf16 v[108:111], a[196:199], v[48:51], 0\n\t"
    "v_mfma_f32_16x16x32_bf16 v[104:107], a[184:187], v[52:55], v[104:107]\n\t"
    "v_sub_f32 v88, v88, v226\n\t"
    "v_sub_f32 v89, v89, v226\n\t"
    "v_sub_f32 v90, v90, v226\n\t"
    "v_mfma_f32_16x16x32_bf16 v[108:111], a[200:203], v[52:55], v[108:111]\n\t"
    "v_sub_f32 v91, v91, v226\n\t"
    "v_sub_f32 v92, v92, v226\n\t"
    "v_sub_f32 v93, v93, v226\n\t"
    "v_mfma_f32_16x16x32_bf16 v[104:107], a[188:191], v[56:59], v[104:107]\n\t"
    "v_sub_f32 v94, v94, v226\n\t"
    "v_sub_f32 v95, v95, v226\n\t"
    "v_max3_f32 v224, v96, v97, v98\n\t"
    "v_mfma_f32_16x16x32_bf16 v[108:111], a[204:207], v[56:59], v[108:111]\n\t"
    "v_max3_f32 v225, v99, v100, v101\n\t"
    "v_max3_f32 v224, v224, v102, v103\n\t"
    "v_max_f32 v224, v224, v225\n\t"
    "v_mfma_f32_16x16x32_bf16 v[104:107], a[192:195], v[60:63], v[104:107]\n\t"
    "v_mov_b32 v225, v224\n\t"
    "s_nop 1\n\t"
    "v_permlane16_swap_b32 v224, v225\n\t"
    "v_max_f32 v224, v224, v225\n\t"
    "v_mfma_f32_16x16x32_bf16 v[108:111], a[208:211], v[60:63], v[108:111]\n\t"
    "v_mov_b32 v225, v224\n\t"
    "s_nop 1\n\t"
    "v_permlane32_swap_b32 v224, v225\n\t"
    "v_max_f32 v224, v224, v225\n\t"
    "v_mfma_f32_16x16x32_bf16 v[112:115], a[180:183], v[64:67], 0\n\t"
    "v_mfma_f32_16x16x32_bf16 v[116:119], a[196:199], v[64:67], 0\n\t"
    "v_mfma_f32_16x16x32_bf16 v[112:115], a[184:187], v[68:71], v[112:115]\n\t"
    "v_sub_f32 v196, 0, v224\n\t"
    "v_sub_f32 v197, 0, v224\n\t"
    "v_sub_f32 v198, 0, v224\n\t"
    "v_mfma_f32_16x16x32_bf16 v[116:119], a[200:203], v[68:71], v[116:119]\n\t"
    "v_sub_f32 v199, 0, v224\n\t"
    "v_sub_f32 v96, v96, v224\n\t"
    "v_sub_f32 v97, v97, v224\n\t"
    "v_mfma_f32_16x16x32_bf16 v[112:115], a[188:191], v[72:75], v[112:115]\n\t"
    "v_sub_f32 v98, v98, v224\n\t"
    "v_sub_f32 v99, v99, v224\n\t"
    "v_sub_f32 v100, v100, v224\n\t"
    "v_mfma_f32_16x16x32_bf16 v[116:119], a[204:207], v[72:75], v[116:119]\n\t"
    "v_sub_f32 v101, v101, v224\n\t"
    "v_sub_f32 v102, v102, v224\n\t"
    "v_sub_f32 v103, v103, v224\n\t"
    "v_mfma_f32_16x16x32_bf16 v[112:115], a[192:195], v[76:79], v[112:115]\n\t"
    "v_max3_f32 v226, v104, v105, v106\n\t"
    "v_max3_f32 v227, v107, v108, v109\n\t"
    "v_max3_f32 v226, v226, v110, v111\n\t"
    "v_mfma_f32_16x16x32_bf16 v[116:119], a[208:211], v[76:79], v[116:119]\n\t"
    "v_max_f32 v226, v226, v227\n\t"
    "v_mov_b32 v227, v226\n\t"
    "s_nop 1\n\t"
    "v_permlane16_swap_b32 v226, v227\n\t"
    "v_max_f32 v226, v226, v227\n\t"
    "v_mov_b32 v227, v226\n\t"
    "s_nop 1\n\t"
    "v_permlane32_swap_b32 v226, v227\n\t"
    "v_max_f32 v226, v226, v227\n\t"
    "v_sub_f32 v200, 0, v226\n\t"
    "v_sub_f32 v201, 0, v226\n\t"
    "v_sub_f32 v202, 0, v226\n\t"
    "v_sub_f32 v203, 0, v226\n\t"
    "v_sub_f32 v104, v104, v226\n\t"
    "v_sub_f32 v105, v105, v226\n\t"
    "v_sub_f32 v106, v106, v226\n\t"
    "v_sub_f32 v107, v107, v226\n\t"
    "v_sub_f32 v108, v108, v226\n\t"
    "v_sub_f32 v109, v109, v226\n\t"
    "v_sub_f32 v110, v110, v226\n\t"
    "v_sub_f32 v111, v111, v226\n\t"
    "v_max3_f32 v224, v112, v113, v114\n\t"
    "v_max3_f32 v225, v115, v116, v117\n\t"
    "v_max3_f32 v224, v224, v118, v119\n\t"
    "v_max_f32 v224, v224, v225\n\t"
    "v_mov_b32 v225, v224\n\t"
    "s_nop 1\n\t"
    "v_permlane16_swap_b32 v224, v225\n\t"
    "v_max_f32 v224, v224, v225\n\t"
    "v_mov_b32 v225, v224\n\t"
    "s_nop 1\n\t"
    "v_permlane32_swap_b32 v224, v225\n\t"
    "v_max_f32 v224, v224, v225\n\t"
    "v_sub_f32 v204, 0, v224\n\t"
    "v_sub_f32 v205, 0, v224\n\t"
    "v_sub_f32 v206, 0, v224\n\t"
    "v_sub_f32 v207, 0, v224\n\t"
    "v_sub_f32 v112, v112, v224\n\t"
    "v_sub_f32 v113, v113, v224\n\t"
    "v_sub_f32 v114, v114, v224\n\t"
    "v_sub_f32 v115, v115, v224\n\t"
    "v_sub_f32 v116, v116, v224\n\t"
    "v_sub_f32 v117, v117, v224\n\t"
    "v_sub_f32 v118, v118, v224\n\t"
    "v_sub_f32 v119, v119, v224\n\t"
    "s_waitcnt vmcnt(32) lgkmcnt(0)\n\t"
    "s_barrier\n\t"
    "ds_read_b128 a[180:183], v208 offset:0\n\t"
    "ds_read_b128 a[184:187], v209 offset:0\n\t"
    "ds_read_b128 a[188:191], v208 offset:1024\n\t"
    "ds_read_b128 a[192:195], v209 offset:1024\n\t"
    "ds_read_b128 a[196:199], v208 offset:4096\n\t"
    "ds_read_b128 a[200:203], v209 offset:4096\n\t"
    "ds_read_b128 a[204:207], v208 offset:5120\n\t"
    "ds_read_b128 a[208:211], v209 offset:5120\n\t"
    "buffer_load_dwordx4 v214, s[36:39], s40 offen offset:0 nt lds\n\t"
    "v_exp_f32 v80, v80\n\t"
    "v_exp_f32 v81, v81\n\t"
    "v_exp_f32 v82, v82\n\t"
    "v_exp_f32 v83, v83\n\t"
    "v_exp_f32 v84, v84\n\t"
    "v_exp_f32 v85, v85\n\t"
    "v_exp_f32 v86, v86\n\t"
    "v_exp_f32 v87, v87\n\t"
    "v_exp_f32 v88, v88\n\t"
    "v_exp_f32 v89, v89\n\t"
    "v_exp_f32 v90, v90\n\t"
    "v_exp_f32 v91, v91\n\t"
    "v_exp_f32 v92, v92\n\t"
    "v_exp_f32 v93, v93\n\t"
    "buffer_load_dwordx4 v215, s[36:39], s40 offen offset:1024 nt lds\n\t"
    "v_exp_f32 v94, v94\n\t"
    "v_exp_f32 v95, v95\n\t"
    "v_exp_f32 v96, v96\n\t"
    "v_exp_f32 v97, v97\n\t"
    "v_exp_f32 v98, v98\n\t"
    "v_exp_f32 v99, v99\n\t"
    "v_exp_f32 v100, v100\n\t"
    "v_exp_f32 v101, v101\n\t"
    "v_exp_f32 v102, v102\n\t"
    "v_exp_f32 v103, v103\n\t"
    "ds_read_b64_tr_b16 a[212:213], v210 offset:0\n\t"
    "ds_read_b64_tr_b16 a[214:215], v210 offset:4096\n\t"
    "v_exp_f32 v104, v104\n\t"
    "ds_read_b64_tr_b16 a[216:217], v210 offset:256\n\t"
    "ds_read_b64_tr_b16 a[218:219], v210 offset:4352\n\t"
    "v_exp_f32 v105, v105\n\t"
    "ds_read_b64_tr_b16 a[220:221], v210 offset:512\n\t"
    "ds_read_b64_tr_b16 a[222:223], v210 offset:4608\n\t"
    "buffer_load_dwordx4 v216, s[36:39], s40 offen offset:2048 nt lds\n\t"
    "s_branch LOOP_O_A_%=\n\t"
    "PEEL_O_A_%=:\n\t"
    "s_waitcnt lgkmcnt(0)\n\t"
    "v_mfma_f32_16x16x32_bf16 v[120:123], a[180:183], v[0:3], 0\n\t"
    "v_mfma_f32_16x16x32_bf16 v[124:127], a[196:199], v[0:3], 0\n\t"
    "v_mfma_f32_16x16x32_bf16 v[120:123], a[184:187], v[4:7], v[120:123]\n\t"
    "v_mfma_f32_16x16x32_bf16 v[124:127], a[200:203], v[4:7], v[124:127]\n\t"
    "v_mfma_f32_16x16x32_bf16 v[120:123], a[188:191], v[8:11], v[120:123]\n\t"
    "v_mfma_f32_16x16x32_bf16 v[124:127], a[204:207], v[8:11], v[124:127]\n\t"
    "v_mfma_f32_16x16x32_bf16 v[120:123], a[192:195], v[12:15], v[120:123]\n\t"
    "v_mfma_f32_16x16x32_bf16 v[124:127], a[208:211], v[12:15], v[124:127]\n\t"
    "v_mfma_f32_16x16x32_bf16 v[128:131], a[180:183], v[16:19], 0\n\t"
    "v_mfma_f32_16x16x32_bf16 v[132:135], a[196:199], v[16:19], 0\n\t"
    "v_mfma_f32_16x16x32_bf16 v[128:131], a[184:187], v[20:23], v[128:131]\n\t"
    "v_max3_f32 v224, v120, v121, v122\n\t"
    "v_max3_f32 v225, v123, v124, v125\n\t"
    "v_max3_f32 v224, v224, v126, v127\n\t"
    "v_mfma_f32_16x16x32_bf16 v[132:135], a[200:203], v[20:23], v[132:135]\n\t"
    "v_max_f32 v224, v224, v225\n\t"
    "v_mov_b32 v225, v224\n\t"
    "s_nop 1\n\t"
    "v_permlane16_swap_b32 v224, v225\n\t"
    "v_mfma_f32_16x16x32_bf16 v[128:131], a[188:191], v[24:27], v[128:131]\n\t"
    "v_max_f32 v224, v224, v225\n\t"
    "v_mov_b32 v225, v224\n\t"
    "s_nop 1\n\t"
    "v_permlane32_swap_b32 v224, v225\n\t"
    "v_mfma_f32_16x16x32_bf16 v[132:135], a[204:207], v[24:27], v[132:135]\n\t"
    "v_max_f32 v224, v224, v225\n\t"
    "v_sub_f32 v188, 0, v224\n\t"
    "v_sub_f32 v189, 0, v224\n\t"
    "v_mfma_f32_16x16x32_bf16 v[128:131], a[192:195], v[28:31], v[128:131]\n\t"
    "v_sub_f32 v190, 0, v224\n\t"
    "v_sub_f32 v191, 0, v224\n\t"
    "v_sub_f32 v120, v120, v224\n\t"
    "v_mfma_f32_16x16x32_bf16 v[132:135], a[208:211], v[28:31], v[132:135]\n\t"
    "v_sub_f32 v121, v121, v224\n\t"
    "v_sub_f32 v122, v122, v224\n\t"
    "v_sub_f32 v123, v123, v224\n\t"
    "v_mfma_f32_16x16x32_bf16 v[136:139], a[180:183], v[32:35], 0\n\t"
    "v_mfma_f32_16x16x32_bf16 v[140:143], a[196:199], v[32:35], 0\n\t"
    "v_mfma_f32_16x16x32_bf16 v[136:139], a[184:187], v[36:39], v[136:139]\n\t"
    "v_sub_f32 v124, v124, v224\n\t"
    "v_sub_f32 v125, v125, v224\n\t"
    "v_sub_f32 v126, v126, v224\n\t"
    "v_mfma_f32_16x16x32_bf16 v[140:143], a[200:203], v[36:39], v[140:143]\n\t"
    "v_sub_f32 v127, v127, v224\n\t"
    "v_max3_f32 v226, v128, v129, v130\n\t"
    "v_max3_f32 v227, v131, v132, v133\n\t"
    "v_mfma_f32_16x16x32_bf16 v[136:139], a[188:191], v[40:43], v[136:139]\n\t"
    "v_max3_f32 v226, v226, v134, v135\n\t"
    "v_max_f32 v226, v226, v227\n\t"
    "v_mov_b32 v227, v226\n\t"
    "v_mfma_f32_16x16x32_bf16 v[140:143], a[204:207], v[40:43], v[140:143]\n\t"
    "s_nop 0\n\t"
    "v_permlane16_swap_b32 v226, v227\n\t"
    "v_max_f32 v226, v226, v227\n\t"
    "v_mov_b32 v227, v226\n\t"
    "v_mfma_f32_16x16x32_bf16 v[136:139], a[192:195], v[44:47], v[136:139]\n\t"
    "s_nop 0\n\t"
    "v_permlane32_swap_b32 v226, v227\n\t"
    "v_max_f32 v226, v226, v227\n\t"
    "v_sub_f32 v192, 0, v226\n\t"
    "v_mfma_f32_16x16x32_bf16 v[140:143], a[208:211], v[44:47], v[140:143]\n\t"
    "v_sub_f32 v193, 0, v226\n\t"
    "v_sub_f32 v194, 0, v226\n\t"
    "v_sub_f32 v195, 0, v226\n\t"
    "v_mfma_f32_16x16x32_bf16 v[144:147], a[180:183], v[48:51], 0\n\t"
    "v_mfma_f32_16x16x32_bf16 v[148:151], a[196:199], v[48:51], 0\n\t"
    "v_mfma_f32_16x16x32_bf16 v[144:147], a[184:187], v[52:55], v[144:147]\n\t"
    "v_sub_f32 v128, v128, v226\n\t"
    "v_sub_f32 v129, v129, v226\n\t"
    "v_sub_f32 v130, v130, v226\n\t"
    "v_mfma_f32_16x16x32_bf16 v[148:151], a[200:203], v[52:55], v[148:151]\n\t"
    "v_sub_f32 v131, v131, v226\n\t"
    "v_sub_f32 v132, v132, v226\n\t"
    "v_sub_f32 v133, v133, v226\n\t"
    "v_mfma_f32_16x16x32_bf16 v[144:147], a[188:191], v[56:59], v[144:147]\n\t"
    "v_sub_f32 v134, v134, v226\n\t"
    "v_sub_f32 v135, v135, v226\n\t"
    "v_max3_f32 v224, v136, v137, v138\n\t"
    "v_mfma_f32_16x16x32_bf16 v[148:151], a[204:207], v[56:59], v[148:151]\n\t"
    "v_max3_f32 v225, v139, v140, v141\n\t"
    "v_max3_f32 v224, v224, v142, v143\n\t"
    "v_max_f32 v224, v224, v225\n\t"
    "v_mfma_f32_16x16x32_bf16 v[144:147], a[192:195], v[60:63], v[144:147]\n\t"
    "v_mov_b32 v225, v224\n\t"
    "s_nop 1\n\t"
    "v_permlane16_swap_b32 v224, v225\n\t"
    "v_max_f32 v224, v224, v225\n\t"
    "v_mfma_f32_16x16x32_bf16 v[148:151], a[208:211], v[60:63], v[148:151]\n\t"
    "v_mov_b32 v225, v224\n\t"
    "s_nop 1\n\t"
    "v_permlane32_swap_b32 v224, v225\n\t"
    "v_max_f32 v224, v224, v225\n\t"
    "v_mfma_f32_16x16x32_bf16 v[112:115], a[180:183], v[64:67], 0\n\t"
    "v_mfma_f32_16x16x32_bf16 v[116:119], a[196:199], v[64:67], 0\n\t"
    "v_mfma_f32_16x16x32_bf16 v[112:115], a[184:187], v[68:71], v[112:115]\n\t"
    "v_sub_f32 v196, 0, v224\n\t"
    "v_sub_f32 v197, 0, v224\n\t"
    "v_sub_f32 v198, 0, v224\n\t"
    "v_mfma_f32_16x16x32_bf16 v[116:119], a[200:203], v[68:71], v[116:119]\n\t"
    "v_sub_f32 v199, 0, v224\n\t"
    "v_sub_f32 v136, v136, v224\n\t"
    "v_sub_f32 v137, v137, v224\n\t"
    "v_mfma_f32_16x16x32_bf16 v[112:115], a[188:191], v[72:75], v[112:115]\n\t"
    "v_sub_f32 v138, v138, v224\n\t"
    "v_sub_f32 v139, v139, v224\n\t"
    "v_sub_f32 v140, v140, v224\n\t"
    "v_mfma_f32_16x16x32_bf16 v[116:119], a[204:207], v[72:75], v[116:119]\n\t"
    "v_sub_f32 v141, v141, v224\n\t"
    "v_sub_f32 v142, v142, v224\n\t"
    "v_sub_f32 v143, v143, v224\n\t"
    "v_mfma_f32_16x16x32_bf16 v[112:115], a[192:195], v[76:79], v[112:115]\n\t"
    "v_max3_f32 v226, v144, v145, v146\n\t"
    "v_max3_f32 v227, v147, v148, v149\n\t"
    "v_max3_f32 v226, v226, v150, v151\n\t"
    "v_mfma_f32_16x16x32_bf16 v[116:119], a[208:211], v[76:79], v[116:119]\n\t"
    "v_max_f32 v226, v226, v227\n\t"
    "v_mov_b32 v227, v226\n\t"
    "s_nop 1\n\t"
    "v_permlane16_swap_b32 v226, v227\n\t"
    "v_max_f32 v226, v226, v227\n\t"
    "v_mov_b32 v227, v226\n\t"
    "s_nop 1\n\t"
    "v_permlane32_swap_b32 v226, v227\n\t"
    "v_max_f32 v226, v226, v227\n\t"
    "v_sub_f32 v200, 0, v226\n\t"
    "v_sub_f32 v201, 0, v226\n\t"
    "v_sub_f32 v202, 0, v226\n\t"
    "v_sub_f32 v203, 0, v226\n\t"
    "v_sub_f32 v144, v144, v226\n\t"
    "v_sub_f32 v145, v145, v226\n\t"
    "v_sub_f32 v146, v146, v226\n\t"
    "v_sub_f32 v147, v147, v226\n\t"
    "v_sub_f32 v148, v148, v226\n\t"
    "v_sub_f32 v149, v149, v226\n\t"
    "v_sub_f32 v150, v150, v226\n\t"
    "v_sub_f32 v151, v151, v226\n\t"
    "v_max3_f32 v224, v112, v113, v114\n\t"
    "v_max3_f32 v225, v115, v116, v117\n\t"
    "v_max3_f32 v224, v224, v118, v119\n\t"
    "v_max_f32 v224, v224, v225\n\t"
    "v_mov_b32 v225, v224\n\t"
    "s_nop 1\n\t"
    "v_permlane16_swap_b32 v224, v225\n\t"
    "v_max_f32 v224, v224, v225\n\t"
    "v_mov_b32 v225, v224\n\t"
    "s_nop 1\n\t"
    "v_permlane32_swap_b32 v224, v225\n\t"
    "v_max_f32 v224, v224, v225\n\t"
    "v_sub_f32 v204, 0, v224\n\t"
    "v_sub_f32 v205, 0, v224\n\t"
    "v_sub_f32 v206, 0, v224\n\t"
    "v_sub_f32 v207, 0, v224\n\t"
    "s_memrealtime s[56:57]\n\t"
    "s_waitcnt vmcnt(32) lgkmcnt(0)\n\t"
    "s_sub_u32 s54, s34, s56\n\t"
    "s_cmp_gt_i32 s54, 0\n\t"
    "s_cbranch_scc1 TR_PApeel_%=\n\t"
    "BK_PApeel_%=:\n\t"
    "s_barrier\n\t"
    "ds_read_b128 a[180:183], v208 offset:0\n\t"
    "ds_read_b128 a[184:187], v209 offset:0\n\t"
    "ds_read_b128 a[188:191], v208 offset:1024\n\t"
    "ds_read_b128 a[192:195], v209 offset:1024\n\t"
    "ds_read_b128 a[196:199], v208 offset:4096\n\t"
    "s_sub_u32 s57, s56, s98\n\t"
    "buffer_load_dwordx4 v214, s[36:39], s40 offen offset:0 nt lds\n\t"
    "s_ashr_i32 s57, s57, 3\n\t"
    "ds_read_b128 a[200:203], v209 offset:4096\n\t"
    "s_min_i32 s57, s57, 8\n\t"
    "s_max_i32 s57, s57, -20\n\t"
    "s_add_u32 s98, s98, s35\n\t"
    "s_sub_u32 s56, s56, s57\n\t"
    "ds_read_b128 a[204:207], v208 offset:5120\n\t"
    "s_add_u32 s34, s56, s35\n\t"
    "s_sub_u32 s35, s35, 6\n\t"
    "s_max_u32 s35, s35, 180\n\t"
    "s_cmp_lt_u32 s44, 2048\n\t"
    "s_cselect_b32 s35, 0, s35\n\t"
    "ds_read_b128 a[208:211], v209 offset:5120\n\t"
    "v_exp_f32 v120, v120\n\t"
    "v_exp_f32 v121, v121\n\t"
    "v_exp_f32 v122, v122\n\t"
    "v_exp_f32 v123, v123\n\t"
    "v_exp_f32 v124, v124\n\t"
    "v_exp_f32 v125, v125\n\t"
    "buffer_load_dwordx4 v215, s[36:39], s40 offen offset:1024 nt lds\n\t"
    "v_exp_f32 v126, v126\n\t"
    "v_exp_f32 v127, v127\n\t"
    "v_exp_f32 v128, v128\n\t"
    "v_exp_f32 v129, v129\n\t"
    "v_exp_f32 v130, v130\n\t"
    "v_exp_f32 v131, v131\n\t"
    "v_exp_f32 v132, v132\n\t"
    "v_exp_f32 v133, v133\n\t"
    "v_exp_f32 v134, v134\n\t"
    "v_exp_f32 v135, v135\n\t"
    "v_exp_f32 v136, v136\n\t"
    "v_exp_f32 v137, v137\n\t"
    "v_exp_f32 v138, v138\n\t"
    "ds_read_b64_tr_b16 a[212:213], v210 offset:0\n\t"
    "ds_read_b64_tr_b16 a[214:215], v210 offset:4096\n\t"
    "v_exp_f32 v139, v139\n\t"
    "ds_read_b64_tr_b16 a[216:217], v210 offset:256\n\t"
    "ds_read_b64_tr_b16 a[218:219], v210 offset:4352\n\t"
    "buffer_load_dwordx4 v216, s[36:39], s40 offen offset:2048 nt lds\n\t"
    "ds_read_b64_tr_b16 a[220:221], v210 offset:512\n\t"
    "ds_read_b64_tr_b16 a[222:223], v210 offset:4608\n\t"
    "LOOP_E_A_%=:\n\t"
    "s_waitcnt lgkmcnt(6)\n\t"
    "v_mfma_f32_16x16x32_bf16 v[80:83], a[180:183], v[0:3], v[188:191]\n\t"
    "ds_read_b64_tr_b16 a[224:225], v210 offset:768\n\t"
    "ds_read_b64_tr_b16 a[226:227], v210 offset:4864\n\t"
    "v_mfma_f32_16x16x32_bf16 v[88:91], a[180:183], v[16:19], v[192:195]\n\t"
    "v_exp_f32 v140, v140\n\t"
    "v_mfma_f32_16x16x32_bf16 v[96:99], a[180:183], v[32:35], v[196:199]\n\t"
    "ds_read_b64_tr_b16 a[228:229], v210 offset:1024\n\t"
    "ds_read_b64_tr_b16 a[230:231], v210 offset:5120\n\t"
    "v_mfma_f32_16x16x32_bf16 v[104:107], a[180:183], v[48:51], v[200:203]\n\t"
    "v_exp_f32 v141, v141\n\t"
    "v_mfma_f32_16x16x32_bf16 v[112:115], a[180:183], v[64:67], v[204:207]\n\t"
    "ds_read_b64_tr_b16 a[232:233], v210 offset:1280\n\t"
    "ds_read_b64_tr_b16 a[234:235], v210 offset:5376\n\t"
    "v_mfma_f32_16x16x32_bf16 v[80:83], a[184:187], v[4:7], v[80:83]\n\t"
    "v_exp_f32 v142, v142\n\t"
    "v_mfma_f32_16x16x32_bf16 v[88:91], a[184:187], v[20:23], v[88:91]\n\t"
    "ds_read_b64_tr_b16 a[236:237], v210 offset:1536\n\t"
    "ds_read_b64_tr_b16 a[238:239], v210 offset:5632\n\t"
    "v_mfma_f32_16x16x32_bf16 v[96:99], a[184:187], v[36:39], v[96:99]\n\t"
    "v_exp_f32 v143, v143\n\t"
    "v_mfma_f32_16x16x32_bf16 v[104:107], a[184:187], v[52:55], v[104:107]\n\t"
    "ds_read_b64_tr_b16 a[240:241], v210 offset:1792\n\t"
    "ds_read_b64_tr_b16 a[242:243], v210 offset:5888\n\t"
    "v_mfma_f32_16x16x32_bf16 v[112:115], a[184:187], v[68:71], v[112:115]\n\t"
    "v_exp_f32 v144, v144\n\t"
    "v_mfma_f32_16x16x32_bf16 v[80:83], a[188:191], v[8:11], v[80:83]\n\t"
    "v_exp_f32 v145, v145\n\t"
    "v_mfma_f32_16x16x32_bf16 v[88:91], a[188:191], v[24:27], v[88:91]\n\t"
    "v_cvt_pk_bf16_f32 v172, v120, v121\n\t"
    "v_cvt_pk_bf16_f32 v173, v122, v123\n\t"
    "v_mfma_f32_16x16x32_bf16 v[96:99], a[188:191], v[40:43], v[96:99]\n\t"
    "v_exp_f32 v146, v146\n\t"
    "v_mfma_f32_16x16x32_bf16 v[104:107], a[188:191], v[56:59], v[104:107]\n\t"
    "v_cvt_pk_bf16_f32 v174, v124, v125\n\t"
    "v_cvt_pk_bf16_f32 v175, v126, v127\n\t"
    "v_mfma_f32_16x16x32_bf16 v[112:115], a[188:191], v[72:75], v[112:115]\n\t"
    "buffer_load_dwordx4 v217, s[36:39], s40 offen offset:3072 nt lds\n\t"
    "v_mfma_f32_16x16x32_bf16 v[80:83], a[192:195], v[12:15], v[80:83]\n\t"
    "v_exp_f32 v147, v147\n\t"
    "v_mfma_f32_16x16x32_bf16 v[88:91], a[192:195], v[28:31], v[88:91]\n\t"
    "v_cvt_pk_bf16_f32 v176, v128, v129\n\t"
    "v_cvt_pk_bf16_f32 v177, v130, v131\n\t"
    "v_mfma_f32_16x16x32_bf16 v[96:99], a[192:195], v[44:47], v[96:99]\n\t"
    "v_exp_f32 v148, v148\n\t"
    "v_mfma_f32_16x16x32_bf16 v[104:107], a[192:195], v[60:63], v[104:107]\n\t"
    "v_cvt_pk_bf16_f32 v178, v132, v133\n\t"
    "v_cvt_pk_bf16_f32 v179, v134, v135\n\t"
    "v_mfma_f32_16x16x32_bf16 v[112:115], a[192:195], v[76:79], v[112:115]\n\t"
    "v_exp_f32 v149, v149\n\t"
    "v_mfma_f32_16x16x32_bf16 v[84:87], a[196:199], v[0:3], v[188:191]\n\t"
    "v_cvt_pk_bf16_f32 v180, v136, v137\n\t"
    "v_cvt_pk_bf16_f32 v181, v138, v139\n\t"
    "v_mfma_f32_16x16x32_bf16 v[92:95], a[196:199], v[16:19], v[192:195]\n\t"
    "v_exp_f32 v150, v150\n\t"
    "v_mfma_f32_16x16x32_bf16 v[100:103], a[196:199], v[32:35], v[196:199]\n\t"
    "v_cvt_pk_bf16_f32 v182, v140, v141\n\t"
    "v_cvt_pk_bf16_f32 v183, v142, v143\n\t"
    "v_mfma_f32_16x16x32_bf16 v[108:111], a[196:199], v[48:51], v[200:203]\n\t"
    "v_exp_f32 v151, v151\n\t"
    "v_mfma_f32_16x16x32_bf16 v[116:119], a[196:199], v[64:67], v[204:207]\n\t"
    "v_cvt_pk_bf16_f32 v184, v144, v145\n\t"
    "v_cvt_pk_bf16_f32 v185, v146, v147\n\t"
    "v_mfma_f32_16x16x32_bf16 v[84:87], a[200:203], v[4:7], v[84:87]\n\t"
    "v_cvt_pk_bf16_f32 v186, v148, v149\n\t"
    "v_cvt_pk_bf16_f32 v187, v150, v151\n\t"
    "v_mfma_f32_16x16x32_bf16 v[92:95], a[200:203], v[20:23], v[92:95]\n\t"
    "v_mfma_f32_16x16x32_bf16 v[100:103], a[200:203], v[36:39], v[100:103]\n\t"
    "v_mfma_f32_16x16x32_bf16 v[108:111], a[200:203], v[52:55], v[108:111]\n\t"
    "s_add_u32 s40, s40, 8192\n\t"
    "s_mov_b32 m0, s41\n\t"
    "v_mfma_f32_16x16x32_bf16 v[116:119], a[200:203], v[68:71], v[116:119]\n\t"
    "s_add_u32 s41, s41, 16384\n\t"
    "v_mfma_f32_16x16x32_bf16 v[84:87], a[204:207], v[8:11], v[84:87]\n\t"
    "s_cmp_eq_u32 s41, s43\n\t"
    "s_cselect_b32 s41, s42, s41\n\t"
    "v_mfma_f32_16x16x32_bf16 v[92:95], a[204:207], v[24:27], v[92:95]\n\t"
    "v_add_u32 v208, s41, v211\n\t"
    "v_add_u32 v209, s41, v212\n\t"
    "v_mfma_f32_16x16x32_bf16 v[100:103], a[204:207], v[40:43], v[100:103]\n\t"
    "v_add_u32 v210, s41, v213\n\t"
    "v_mfma_f32_16x16x32_bf16 v[108:111], a[204:207], v[56:59], v[108:111]\n\t"
    "v_mfma_f32_16x16x32_bf16 v[116:119], a[204:207], v[72:75], v[116:119]\n\t"
    "v_mfma_f32_16x16x32_bf16 v[84:87], a[208:211], v[12:15], v[84:87]\n\t"
    "v_mfma_f32_16x16x32_bf16 v[92:95], a[208:211], v[28:31], v[92:95]\n\t"
    "s_waitcnt vmcnt(32) lgkmcnt(0)\n\t"
    "s_barrier\n\t"
    "v_mfma_f32_16x16x32_bf16 v[100:103], a[208:211], v[44:47], v[100:103]\n\t"
    "v_mfma_f32_16x16x32_bf16 v[108:111], a[208:211], v[60:63], v[108:111]\n\t"
    "v_mfma_f32_16x16x32_bf16 v[116:119], a[208:211], v[76:79], v[116:119]\n\t"
    "s_cbranch_vccnz TR_AloopE_%=\n\t"
    "BK_AloopE_%=:\n\t"
    "v_mfma_f32_16x16x32_bf16 a[0:3], a[212:215], v[172:175], a[0:3]\n\t"
    "ds_read_b128 a[180:183], v208 offset:0\n\t"
    "ds_read_b128 a[184:187], v209 offset:0\n\t"
    "v_mfma_f32_16x16x32_bf16 a[32:35], a[212:215], v[176:179], a[32:35]\n\t"
    "ds_read_b128 a[188:191], v208 offset:1024\n\t"
    "ds_read_b128 a[192:195], v209 offset:1024\n\t"
    "v_mfma_f32_16x16x32_bf16 a[64:67], a[212:215], v[180:183], a[64:67]\n\t"
    "ds_read_b128 a[196:199], v208 offset:4096\n\t"
    "ds_read_b128 a[200:203], v209 offset:4096\n\t"
    "v_mfma_f32_16x16x32_bf16 a[96:99], a[212:215], v[184:187], a[96:99]\n\t"
    "ds_read_b128 a[204:207], v208 offset:5120\n\t"
    "ds_read_b128 a[208:211], v209 offset:5120\n\t"
    "v_mfma_f32_16x16x32_bf16 a[4:7], a[216:219], v[172:175], a[4:7]\n\t"
    "buffer_load_dwordx4 v214, s[36:39], s40 offen offset:0 nt lds\n\t"
    "v_mfma_f32_16x16x32_bf16 a[36:39], a[216:219], v[176:179], a[36:39]\n\t"
    "v_exp_f32 v80, v80\n\t"
    "v_mfma_f32_16x16x32_bf16 a[68:71], a[216:219], v[180:183], a[68:71]\n\t"
    "v_exp_f32 v81, v81\n\t"
    "v_mfma_f32_16x16x32_bf16 a[100:103], a[216:219], v[184:187], a[100:103]\n\t"
    "v_exp_f32 v82, v82\n\t"
    "v_mfma_f32_16x16x32_bf16 a[160:163], a[244:247], v[172:175], a[160:163]\n\t"
    "v_exp_f32 v83, v83\n\t"
    "v_mfma_f32_16x16x32_bf16 a[8:11], a[220:223], v[172:175], a[8:11]\n\t"
    "v_exp_f32 v84, v84\n\t"
    "v_mfma_f32_16x16x32_bf16 a[40:43], a[220:223], v[176:179], a[40:43]\n\t"
    "v_exp_f32 v85, v85\n\t"
    "v_mfma_f32_16x16x32_bf16 a[72:75], a[220:223], v[180:183], a[72:75]\n\t"
    "v_exp_f32 v86, v86\n\t"
    "v_mfma_f32_16x16x32_bf16 a[104:107], a[220:223], v[184:187], a[104:107]\n\t"
    "v_exp_f32 v87, v87\n\t"
    "v_mfma_f32_16x16x32_bf16 a[12:15], a[224:227], v[172:175], a[12:15]\n\t"
    "v_exp_f32 v88, v88\n\t"
    "v_mfma_f32_16x16x32_bf16 a[44:47], a[224:227], v[176:179], a[44:47]\n\t"
    "v_exp_f32 v89, v89\n\t"
    "v_mfma_f32_16x16x32_bf16 a[76:79], a[224:227], v[180:183], a[76:79]\n\t"
    "v_exp_f32 v90, v90\n\t"
    "v_mfma_f32_16x16x32_bf16 a[108:111], a[224:227], v[184:187], a[108:111]\n\t"
    "v_exp_f32 v91, v91\n\t"
    "v_mfma_f32_16x16x32_bf16 a[164:167], a[244:247], v[176:179], a[164:167]\n\t"
    "v_exp_f32 v92, v92\n\t"
    "v_mfma_f32_16x16x32_bf16 a[16:19], a[228:231], v[172:175], a[16:19]\n\t"
    "v_exp_f32 v93, v93\n\t"
    "v_mfma_f32_16x16x32_bf16 a[48:51], a[228:231], v[176:179], a[48:51]\n\t"
    "buffer_load_dwordx4 v215, s[36:39], s40 offen offset:1024 nt lds\n\t"
    "v_mfma_f32_16x16x32_bf16 a[80:83], a[228:231], v[180:183], a[80:83]\n\t"
    "v_exp_f32 v94, v94\n\t"
    "v_mfma_f32_16x16x32_bf16 a[112:115], a[228:231], v[184:187], a[112:115]\n\t"
    "v_exp_f32 v95, v95\n\t"
    "v_mfma_f32_16x16x32_bf16 a[20:23], a[232:235], v[172:175], a[20:23]\n\t"
    "v_exp_f32 v96, v96\n\t"
    "v_mfma_f32_16x16x32_bf16 a[52:55], a[232:235], v[176:179], a[52:55]\n\t"
    "v_exp_f32 v97, v97\n\t"
    "v_mfma_f32_16x16x32_bf16 a[84:87], a[232:235], v[180:183], a[84:87]\n\t"
    "v_exp_f32 v98, v98\n\t"
    "v_mfma_f32_16x16x32_bf16 a[116:119], a[232:235], v[184:187], a[116:119]\n\t"
    "v_exp_f32 v99, v99\n\t"
    "v_mfma_f32_16x16x32_bf16 a[168:171], a[244:247], v[180:183], a[168:171]\n\t"
    "v_exp_f32 v100, v100\n\t"
    "v_mfma_f32_16x16x32_bf16 a[24:27], a[236:239], v[172:175], a[24:27]\n\t"
    "v_exp_f32 v101, v101\n\t"
    "v_mfma_f32_16x16x32_bf16 a[56:59], a[236:239], v[176:179], a[56:59]\n\t"
    "v_exp_f32 v102, v102\n\t"
    "v_mfma_f32_16x16x32_bf16 a[88:91], a[236:239], v[180:183], a[88:91]\n\t"
    "v_exp_f32 v103, v103\n\t"
    "v_mfma_f32_16x16x32_bf16 a[120:123], a[236:239], v[184:187], a[120:123]\n\t"
    "ds_read_b64_tr_b16 a[212:213], v210 offset:0\n\t"
    "ds_read_b64_tr_b16 a[214:215], v210 offset:4096\n\t"
    "v_mfma_f32_16x16x32_bf16 a[28:31], a[240:243], v[172:175], a[28:31]\n\t"
    "v_exp_f32 v104, v104\n\t"
    "v_mfma_f32_16x16x32_bf16 a[60:63], a[240:243], v[176:179], a[60:63]\n\t"
    "ds_read_b64_tr_b16 a[216:217], v210 offset:256\n\t"
    "ds_read_b64_tr_b16 a[218:219], v210 offset:4352\n\t"
    "v_mfma_f32_16x16x32_bf16 a[92:95], a[240:243], v[180:183], a[92:95]\n\t"
    "v_exp_f32 v105, v105\n\t"
    "v_mfma_f32_16x16x32_bf16 a[124:127], a[240:243], v[184:187], a[124:127]\n\t"
    "ds_read_b64_tr_b16 a[220:221], v210 offset:512\n\t"
    "ds_read_b64_tr_b16 a[222:223], v210 offset:4608\n\t"
    "v_mfma_f32_16x16x32_bf16 a[172:175], a[244:247], v[184:187], a[172:175]\n\t"
    "buffer_load_dwordx4 v216, s[36:39], s40 offen offset:2048 nt lds\n\t"
    "s_lshr_b32 s44, s44, 1\n\t"
    "s_cbranch_scc0 DRAIN_O_A_%=\n\t"
    "LOOP_O_A_%=:\n\t"
    "s_waitcnt lgkmcnt(6)\n\t"
    "v_mfma_f32_16x16x32_bf16 v[120:123], a[180:183], v[0:3], v[188:191]\n\t"
    "ds_read_b64_tr_b16 a[224:225], v210 offset:768\n\t"
    "ds_read_b64_tr_b16 a[226:227], v210 offset:4864\n\t"
    "v_mfma_f32_16x16x32_bf16 v[128:131], a[180:183], v[16:19], v[192:195]\n\t"
    "v_cvt_pk_bf16_f32 v152, v80, v81\n\t"
    "v_cvt_pk_bf16_f32 v153, v82, v83\n\t"
    "v_mfma_f32_16x16x32_bf16 v[136:139], a[180:183], v[32:35], v[196:199]\n\t"
    "ds_read_b64_tr_b16 a[228:229], v210 offset:1024\n\t"
    "ds_read_b64_tr_b16 a[230:231], v210 offset:5120\n\t"
    "v_mfma_f32_16x16x32_bf16 v[144:147], a[180:183], v[48:51], v[200:203]\n\t"
    "v_exp_f32 v106, v106\n\t"
    "v_mfma_f32_16x16x32_bf16 v[120:123], a[184:187], v[4:7], v[120:123]\n\t"
    "ds_read_b64_tr_b16 a[232:233], v210 offset:1280\n\t"
    "ds_read_b64_tr_b16 a[234:235], v210 offset:5376\n\t"
    "v_mfma_f32_16x16x32_bf16 v[128:131], a[184:187], v[20:23], v[128:131]\n\t"
    "v_cvt_pk_bf16_f32 v154, v84, v85\n\t"
    "v_cvt_pk_bf16_f32 v155, v86, v87\n\t"
    "v_mfma_f32_16x16x32_bf16 v[136:139], a[184:187], v[36:39], v[136:139]\n\t"
    "ds_read_b64_tr_b16 a[236:237], v210 offset:1536\n\t"
    "ds_read_b64_tr_b16 a[238:239], v210 offset:5632\n\t"
    "v_mfma_f32_16x16x32_bf16 v[144:147], a[184:187], v[52:55], v[144:147]\n\t"
    "v_exp_f32 v107, v107\n\t"
    "v_mfma_f32_16x16x32_bf16 v[120:123], a[188:191], v[8:11], v[120:123]\n\t"
    "ds_read_b64_tr_b16 a[240:241], v210 offset:1792\n\t"
    "ds_read_b64_tr_b16 a[242:243], v210 offset:5888\n\t"
    "v_mfma_f32_16x16x32_bf16 v[128:131], a[188:191], v[24:27], v[128:131]\n\t"
    "v_cvt_pk_bf16_f32 v156, v88, v89\n\t"
    "v_cvt_pk_bf16_f32 v157, v90, v91\n\t"
    "v_mfma_f32_16x16x32_bf16 v[136:139], a[188:191], v[40:43], v[136:139]\n\t"
    "v_exp_f32 v108, v108\n\t"
    "v_mfma_f32_16x16x32_bf16 v[144:147], a[188:191], v[56:59], v[144:147]\n\t"
    "v_cvt_pk_bf16_f32 v158, v92, v93\n\t"
    "v_cvt_pk_bf16_f32 v159, v94, v95\n\t"
    "v_mfma_f32_16x16x32_bf16 v[120:123], a[192:195], v[12:15], v[120:123]\n\t"
    "v_exp_f32 v109, v109\n\t"
    "v_mfma_f32_16x16x32_bf16 v[128:131], a[192:195], v[28:31], v[128:131]\n\t"
    "v_cvt_pk_bf16_f32 v160, v96, v97\n\t"
    "v_cvt_pk_bf16_f32 v161, v98, v99\n\t"
    "v_mfma_f32_16x16x32_bf16 v[136:139], a[192:195], v[44:47], v[136:139]\n\t"
    "v_exp_f32 v110, v110\n\t"
    "v_mfma_f32_16x16x32_bf16 v[144:147], a[192:195], v[60:63], v[144:147]\n\t"
    "buffer_load_dwordx4 v217, s[36:39], s40 offen offset:3072 nt lds\n\t"
    "v_mfma_f32_16x16x32_bf16 v[124:127], a[196:199], v[0:3], v[188:191]\n\t"
    "v_cvt_pk_bf16_f32 v162, v100, v101\n\t"
    "v_cvt_pk_bf16_f32 v163, v102, v103\n\t"
    "v_mfma_f32_16x16x32_bf16 v[132:135], a[196:199], v[16:19], v[192:195]\n\t"
    "v_exp_f32 v111, v111\n\t"
    "v_mfma_f32_16x16x32_bf16 v[140:143], a[196:199], v[32:35], v[196:199]\n\t"
    "v_cvt_pk_bf16_f32 v164, v104, v105\n\t"
    "v_cvt_pk_bf16_f32 v165, v106, v107\n\t"
    "v_mfma_f32_16x16x32_bf16 v[148:151], a[196:199], v[48:51], v[200:203]\n\t"
    "v_cvt_pk_bf16_f32 v166, v108, v109\n\t"
    "v_cvt_pk_bf16_f32 v167, v110, v111\n\t"
    "v_mfma_f32_16x16x32_bf16 v[124:127], a[200:203], v[4:7], v[124:127]\n\t"
    "v_mfma_f32_16x16x32_bf16 v[132:135], a[200:203], v[20:23], v[132:135]\n\t"
    "v_mfma_f32_16x16x32_bf16 v[140:143], a[200:203], v[36:39], v[140:143]\n\t"
    "v_mfma_f32_16x16x32_bf16 v[148:151], a[200:203], v[52:55], v[148:151]\n\t"
    "v_mfma_f32_16x16x32_bf16 v[124:127], a[204:207], v[8:11], v[124:127]\n\t"
    "v_exp_f32 v112, v112\n\t"
    "v_mfma_f32_16x16x32_bf16 v[132:135], a[204:207], v[24:27], v[132:135]\n\t"
    "v_exp_f32 v113, v113\n\t"
    "v_mfma_f32_16x16x32_bf16 v[140:143], a[204:207], v[40:43], v[140:143]\n\t"
    "v_exp_f32 v114, v114\n\t"
    "v_mfma_f32_16x16x32_bf16 v[148:151], a[204:207], v[56:59], v[148:151]\n\t"
    "v_exp_f32 v115, v115\n\t"
    "v_mfma_f32_16x16x32_bf16 v[124:127], a[208:211], v[12:15], v[124:127]\n\t"
    "s_add_u32 s40, s40, 8192\n\t"
    "s_mov_b32 m0, s41\n\t"
    "v_mfma_f32_16x16x32_bf16 v[132:135], a[208:211], v[28:31], v[132:135]\n\t"
    "s_add_u32 s41, s41, 16384\n\t"
    "v_cvt_pk_bf16_f32 v168, v112, v113\n\t"
    "v_mfma_f32_16x16x32_bf16 v[140:143], a[208:211], v[44:47], v[140:143]\n\t"
    "s_cmp_eq_u32 s41, s43\n\t"
    "s_cselect_b32 s41, s42, s41\n\t"
    "v_mfma_f32_16x16x32_bf16 v[148:151], a[208:211], v[60:63], v[148:151]\n\t"
    "s_cbranch_vccnz TR_AloopO_%=\n\t"
    "BK_AloopO_%=:\n\t"
    "s_waitcnt lgkmcnt(0)\n\t"
    "v_mfma_f32_16x16x32_bf16 a[0:3], a[212:215], v[152:155], a[0:3]\n\t"
    "s_memrealtime s[56:57]\n\t"
    "v_add_u32 v208, s41, v211\n\t"
    "v_mfma_f32_16x16x32_bf16 a[32:35], a[212:215], v[156:159], a[32:35]\n\t"
    "v_add_u32 v209, s41, v212\n\t"
    "v_add_u32 v210, s41, v213\n\t"
    "v_mfma_f32_16x16x32_bf16 a[64:67], a[212:215], v[160:163], a[64:67]\n\t"
    "v_exp_f32 v116, v116\n\t"
    "v_mfma_f32_16x16x32_bf16 a[96:99], a[212:215], v[164:167], a[96:99]\n\t"
    "v_exp_f32 v117, v117\n\t"
    "v_mfma_f32_16x16x32_bf16 a[4:7], a[216:219], v[152:155], a[4:7]\n\t"
    "s_waitcnt vmcnt(32) lgkmcnt(0)\n\t"
    "s_sub_u32 s54, s34, s56\n\t"
    "s_cmp_gt_i32 s54, 0\n\t"
    "s_cbranch_scc1 TR_PAloop_%=\n\t"
    "BK_PAloop_%=:\n\t"
    "s_barrier\n\t"
    "v_mfma_f32_16x16x32_bf16 a[36:39], a[216:219], v[156:159], a[36:39]\n\t"
    "ds_read_b128 a[180:183], v208 offset:0\n\t"
    "ds_read_b128 a[184:187], v209 offset:0\n\t"
    "v_mfma_f32_16x16x32_bf16 a[68:71], a[216:219], v[160:163], a[68:71]\n\t"
    "ds_read_b128 a[188:191], v208 offset:1024\n\t"
    "ds_read_b128 a[192:195], v209 offset:1024\n\t"
    "v_mfma_f32_16x16x32_bf16 a[100:103], a[216:219], v[164:167], a[100:103]\n\t"
    "ds_read_b128 a[196:199], v208 offset:4096\n\t"
    "s_sub_u32 s57, s56, s98\n\t"
    "v_mfma_f32_16x16x32_bf16 a[160:163], a[244:247], v[152:155], a[160:163]\n\t"
    "buffer_load_dwordx4 v214, s[36:39], s40 offen offset:0 nt lds\n\t"
    "v_mfma_f32_16x16x32_bf16 a[8:11], a[220:223], v[152:155], a[8:11]\n\t"
    "s_ashr_i32 s57, s57, 3\n\t"
    "ds_read_b128 a[200:203], v209 offset:4096\n\t"
    "v_mfma_f32_16x16x32_bf16 a[40:43], a[220:223], v[156:159], a[40:43]\n\t"
    "s_min_i32 s57, s57, 8\n\t"
    "s_max_i32 s57, s57, -20\n\t"
    "v_mfma_f32_16x16x32_bf16 a[72:75], a[220:223], v[160:163], a[72:75]\n\t"
    "s_add_u32 s98, s98, s35\n\t"
    "s_sub_u32 s56, s56, s57\n\t"
    "v_mfma_f32_16x16x32_bf16 a[104:107], a[220:223], v[164:167], a[104:107]\n\t"
    "ds_read_b128 a[204:207], v208 offset:5120\n\t"
    "s_add_u32 s34, s56, s35\n\t"
    "v_mfma_f32_16x16x32_bf16 a[12:15], a[224:227], v[152:155], a[12:15]\n\t"
    "s_sub_u32 s35, s35, 6\n\t"
    "s_max_u32 s35, s35, 180\n\t"
    "v_mfma_f32_16x16x32_bf16 a[44:47], a[224:227], v[156:159], a[44:47]\n\t"
    "s_cmp_lt_u32 s44, 2048\n\t"
    "s_cselect_b32 s35, 0, s35\n\t"
    "v_mfma_f32_16x16x32_bf16 a[76:79], a[224:227], v[160:163], a[76:79]\n\t"
    "ds_read_b128 a[208:211], v209 offset:5120\n\t"
    "v_cvt_pk_bf16_f32 v169, v114, v115\n\t"
    "v_mfma_f32_16x16x32_bf16 a[108:111], a[224:227], v[164:167], a[108:111]\n\t"
    "v_exp_f32 v118, v118\n\t"
    "v_mfma_f32_16x16x32_bf16 a[164:167], a[244:247], v[156:159], a[164:167]\n\t"
    "v_exp_f32 v119, v119\n\t"
    "v_mfma_f32_16x16x32_bf16 a[16:19], a[228:231], v[152:155], a[16:19]\n\t"
    "v_cvt_pk_bf16_f32 v170, v116, v117\n\t"
    "v_cvt_pk_bf16_f32 v171, v118, v119\n\t"
    "v_mfma_f32_16x16x32_bf16 a[48:51], a[228:231], v[156:159], a[48:51]\n\t"
    "v_exp_f32 v120, v120\n\t"
    "v_mfma_f32_16x16x32_bf16 a[80:83], a[228:231], v[160:163], a[80:83]\n\t"
    "v_exp_f32 v121, v121\n\t"
    "v_mfma_f32_16x16x32_bf16 a[112:115], a[228:231], v[164:167], a[112:115]\n\t"
    "v_exp_f32 v122, v122\n\t"
    "v_mfma_f32_16x16x32_bf16 a[20:23], a[232:235], v[152:155], a[20:23]\n\t"
    "v_exp_f32 v123, v123\n\t"
    "v_mfma_f32_16x16x32_bf16 a[52:55], a[232:235], v[156:159], a[52:55]\n\t"
    "v_exp_f32 v124, v124\n\t"
    "v_mfma_f32_16x16x32_bf16 a[84:87], a[232:235], v[160:163], a[84:87]\n\t"
    "v_exp_f32 v125, v125\n\t"
    "v_mfma_f32_16x16x32_bf16 a[116:119], a[232:235], v[164:167], a[116:119]\n\t"
    "buffer_load_dwordx4 v215, s[36:39], s40 offen offset:1024 nt lds\n\t"
    "v_mfma_f32_16x16x32_bf16 a[168:171], a[244:247], v[160:163], a[168:171]\n\t"
    "v_exp_f32 v126, v126\n\t"
    "v_mfma_f32_16x16x32_bf16 a[24:27], a[236:239], v[152:155], a[24:27]\n\t"
    "v_exp_f32 v127, v127\n\t"
    "v_mfma_f32_16x16x32_bf16 a[56:59], a[236:239], v[156:159], a[56:59]\n\t"
    "v_exp_f32 v128, v128\n\t"
    "v_mfma_f32_16x16x32_bf16 a[88:91], a[236:239], v[160:163], a[88:91]\n\t"
    "v_exp_f32 v129, v129\n\t"
    "v_mfma_f32_16x16x32_bf16 a[120:123], a[236:239], v[164:167], a[120:123]\n\t"
    "v_exp_f32 v130, v130\n\t"
    "v_mfma_f32_16x16x32_bf16 a[28:31], a[240:243], v[152:155], a[28:31]\n\t"
    "v_exp_f32 v131, v131\n\t"
    "v_mfma_f32_16x16x32_bf16 a[60:63], a[240:243], v[156:159], a[60:63]\n\t"
    "v_exp_f32 v132, v132\n\t"
    "v_mfma_f32_16x16x32_bf16 a[92:95], a[240:243], v[160:163], a[92:95]\n\t"
    "v_exp_f32 v133, v133\n\t"
    "v_mfma_f32_16x16x32_bf16 a[124:127], a[240:243], v[164:167], a[124:127]\n\t"
    "v_exp_f32 v134, v134\n\t"
    "v_mfma_f32_16x16x32_bf16 a[172:175], a[244:247], v[164:167], a[172:175]\n\t"
    "v_exp_f32 v135, v135\n\t"
    "v_mfma_f32_16x16x32_bf16 a[128:131], a[212:215], v[168:171], a[128:131]\n\t"
    "v_exp_f32 v136, v136\n\t"
    "v_mfma_f32_16x16x32_bf16 a[132:135], a[216:219], v[168:171], a[132:135]\n\t"
    "v_exp_f32 v137, v137\n\t"
    "v_mfma_f32_16x16x32_bf16 a[136:139], a[220:223], v[168:171], a[136:139]\n\t"
    "v_exp_f32 v138, v138\n\t"
    "v_mfma_f32_16x16x32_bf16 a[140:143], a[224:227], v[168:171], a[140:143]\n\t"
    "ds_read_b64_tr_b16 a[212:213], v210 offset:0\n\t"
    "ds_read_b64_tr_b16 a[214:215], v210 offset:4096\n\t"
    "v_mfma_f32_16x16x32_bf16 a[144:147], a[228:231], v[168:171], a[144:147]\n\t"
    "v_exp_f32 v139, v139\n\t"
    "v_mfma_f32_16x16x32_bf16 a[148:151], a[232:235], v[168:171], a[148:151]\n\t"
    "ds_read_b64_tr_b16 a[216:217], v210 offset:256\n\t"
    "ds_read_b64_tr_b16 a[218:219], v210 offset:4352\n\t"
    "v_mfma_f32_16x16x32_bf16 a[152:155], a[236:239], v[168:171], a[152:155]\n\t"
    "buffer_load_dwordx4 v216, s[36:39], s40 offen offset:2048 nt lds\n\t"
    "v_mfma_f32_16x16x32_bf16 a[156:159], a[240:243], v[168:171], a[156:159]\n\t"
    "ds_read_b64_tr_b16 a[220:221], v210 offset:512\n\t"
    "ds_read_b64_tr_b16 a[222:223], v210 offset:4608\n\t"
    "v_mfma_f32_16x16x32_bf16 a[176:179], a[244:247], v[168:171], a[176:179]\n\t"
    "s_and_b32 s52, s44, 126\n\t"
    "s_cbranch_scc1 TCH_A_%=\n\t"
    "TCB_A_%=:\n\t"
    "s_lshr_b32 s44, s44, 1\n\t"
    "s_cbranch_scc1 LOOP_E_A_%=\n\t"
    "ds_read_b64_tr_b16 a[224:225], v210 offset:768\n\t"
    "ds_read_b64_tr_b16 a[226:227], v210 offset:4864\n\t"
    "v_exp_f32 v140, v140\n\t"
    "ds_read_b64_tr_b16 a[228:229], v210 offset:1024\n\t"
    "ds_read_b64_tr_b16 a[230:231], v210 offset:5120\n\t"
    "v_exp_f32 v141, v141\n\t"
    "ds_read_b64_tr_b16 a[232:233], v210 offset:1280\n\t"
    "ds_read_b64_tr_b16 a[234:235], v210 offset:5376\n\t"
    "v_exp_f32 v142, v142\n\t"
    "ds_read_b64_tr_b16 a[236:237], v210 offset:1536\n\t"
    "ds_read_b64_tr_b16 a[238:239], v210 offset:5632\n\t"
    "v_exp_f32 v143, v143\n\t"
    "ds_read_b64_tr_b16 a[240:241], v210 offset:1792\n\t"
    "ds_read_b64_tr_b16 a[242:243], v210 offset:5888\n\t"
    "v_exp_f32 v144, v144\n\t"
    "v_exp_f32 v145, v145\n\t"
    "v_cvt_pk_bf16_f32 v172, v120, v121\n\t"
    "v_cvt_pk_bf16_f32 v173, v122, v123\n\t"
    "v_exp_f32 v146, v146\n\t"
    "v_cvt_pk_bf16_f32 v174, v124, v125\n\t"
    "v_cvt_pk_bf16_f32 v175, v126, v127\n\t"
    "v_exp_f32 v147, v147\n\t"
    "v_cvt_pk_bf16_f32 v176, v128, v129\n\t"
    "v_cvt_pk_bf16_f32 v177, v130, v131\n\t"
    "v_exp_f32 v148, v148\n\t"
    "v_cvt_pk_bf16_f32 v178, v132, v133\n\t"
    "v_cvt_pk_bf16_f32 v179, v134, v135\n\t"
    "v_exp_f32 v149, v149\n\t"
    "v_cvt_pk_bf16_f32 v180, v136, v137\n\t"
    "v_cvt_pk_bf16_f32 v181, v138, v139\n\t"
    "v_exp_f32 v150, v150\n\t"
    "v_cvt_pk_bf16_f32 v182, v140, v141\n\t"
    "v_cvt_pk_bf16_f32 v183, v142, v143\n\t"
    "v_exp_f32 v151, v151\n\t"
    "v_cvt_pk_bf16_f32 v184, v144, v145\n\t"
    "v_cvt_pk_bf16_f32 v185, v146, v147\n\t"
    "v_cvt_pk_bf16_f32 v186, v148, v149\n\t"
    "v_cvt_pk_bf16_f32 v187, v150, v151\n\t"
    "s_cbranch_vccnz TR_AdrainE_%=\n\t"
    "BK_AdrainE_%=:\n\t"
    "v_lshlrev_b32 v226, 4, v222\n\t"
    "s_lshl_b32 s52, s62, 13\n\t"
    "s_xor_b32 s53, s52, 0x2000\n\t"
    "v_add_u32 v224, s52, v226\n\t"
    "v_add_u32 v225, s53, v226\n\t"
    "s_waitcnt vmcnt(0) lgkmcnt(0)\n\t"
    "s_barrier\n\t"
    "v_mfma_f32_16x16x32_bf16 a[0:3], a[212:215], v[172:175], a[0:3]\n\t"
    "s_add_u32 m0, s70, 81920\n\t"
    "s_xor_b32 s66, s26, 256\n\t"
    "v_mfma_f32_16x16x32_bf16 a[32:35], a[212:215], v[176:179], a[32:35]\n\t"
    "buffer_load_dwordx4 v247, s[4:7], s66 offen offset:0 lds\n\t"
    "v_mfma_f32_16x16x32_bf16 a[64:67], a[212:215], v[180:183], a[64:67]\n\t"
    "buffer_load_dwordx4 v248, s[4:7], s66 offen offset:1024 lds\n\t"
    "v_mfma_f32_16x16x32_bf16 a[96:99], a[212:215], v[184:187], a[96:99]\n\t"
    "buffer_load_dwordx4 v249, s[4:7], s66 offen offset:2048 lds\n\t"
    "v_mfma_f32_16x16x32_bf16 a[4:7], a[216:219], v[172:175], a[4:7]\n\t"
    "buffer_load_dwordx4 v250, s[4:7], s66 offen offset:3072 lds\n\t"
    "v_mfma_f32_16x16x32_bf16 a[36:39], a[216:219], v[176:179], a[36:39]\n\t"
    "s_add_u32 m0, s70, 32768\n\t"
    "s_xor_b32 s66, s26, 512\n\t"
    "v_mfma_f32_16x16x32_bf16 a[68:71], a[216:219], v[180:183], a[68:71]\n\t"
    "buffer_load_dwordx4 v247, s[4:7], s66 offen offset:0 lds\n\t"
    "v_mfma_f32_16x16x32_bf16 a[100:103], a[216:219], v[184:187], a[100:103]\n\t"
    "buffer_load_dwordx4 v248, s[4:7], s66 offen offset:1024 lds\n\t"
    "v_mfma_f32_16x16x32_bf16 a[160:163], a[244:247], v[172:175], a[160:163]\n\t"
    "buffer_load_dwordx4 v249, s[4:7], s66 offen offset:2048 lds\n\t"
    "v_mfma_f32_16x16x32_bf16 a[8:11], a[220:223], v[172:175], a[8:11]\n\t"
    "buffer_load_dwordx4 v250, s[4:7], s66 offen offset:3072 lds\n\t"
    "v_mfma_f32_16x16x32_bf16 a[40:43], a[220:223], v[176:179], a[40:43]\n\t"
    "s_add_u32 m0, s70, 49152\n\t"
    "s_xor_b32 s66, s26, 768\n\t"
    "v_mfma_f32_16x16x32_bf16 a[72:75], a[220:223], v[180:183], a[72:75]\n\t"
    "buffer_load_dwordx4 v247, s[4:7], s66 offen offset:0 lds\n\t"
    "v_mfma_f32_16x16x32_bf16 a[104:107], a[220:223], v[184:187], a[104:107]\n\t"
    "buffer_load_dwordx4 v248, s[4:7], s66 offen offset:1024 lds\n\t"
    "v_mfma_f32_16x16x32_bf16 a[12:15], a[224:227], v[172:175], a[12:15]\n\t"
    "buffer_load_dwordx4 v249, s[4:7], s66 offen offset:2048 lds\n\t"
    "v_mfma_f32_16x16x32_bf16 a[44:47], a[224:227], v[176:179], a[44:47]\n\t"
    "buffer_load_dwordx4 v250, s[4:7], s66 offen offset:3072 lds\n\t"
    "v_mfma_f32_16x16x32_bf16 a[76:79], a[224:227], v[180:183], a[76:79]\n\t"
    "s_add_u32 m0, s70, 98304\n\t"
    "s_xor_b32 s66, s26, 0\n\t"
    "v_mfma_f32_16x16x32_bf16 a[108:111], a[224:227], v[184:187], a[108:111]\n\t"
    "buffer_load_dwordx4 v247, s[4:7], s66 offen offset:0 lds\n\t"
    "v_mfma_f32_16x16x32_bf16 a[164:167], a[244:247], v[176:179], a[164:167]\n\t"
    "buffer_load_dwordx4 v248, s[4:7], s66 offen offset:1024 lds\n\t"
    "v_mfma_f32_16x16x32_bf16 a[16:19], a[228:231], v[172:175], a[16:19]\n\t"
    "buffer_load_dwordx4 v249, s[4:7], s66 offen offset:2048 lds\n\t"
    "v_mfma_f32_16x16x32_bf16 a[48:51], a[228:231], v[176:179], a[48:51]\n\t"
    "buffer_load_dwordx4 v250, s[4:7], s66 offen offset:3072 lds\n\t"
    "v_mfma_f32_16x16x32_bf16 a[80:83], a[228:231], v[180:183], a[80:83]\n\t"
    "s_add_u32 m0, s16, 65536\n\t"
    "v_mfma_f32_16x16x32_bf16 a[112:115], a[228:231], v[184:187], a[112:115]\n\t"
    "buffer_load_dwordx4 v251, s[4:7], s26 offen lds\n\t"
    "v_mfma_f32_16x16x32_bf16 a[20:23], a[232:235], v[172:175], a[20:23]\n\t"
    "s_add_u32 m0, s16, 66560\n\t"
    "s_add_u32 s66, s26, 36864\n\t"
    "v_mfma_f32_16x16x32_bf16 a[52:55], a[232:235], v[176:179], a[52:55]\n\t"
    "buffer_load_dwordx4 v251, s[4:7], s66 offen lds\n\t"
    "v_mfma_f32_16x16x32_bf16 a[84:87], a[232:235], v[180:183], a[84:87]\n\t"
    "s_add_u32 m0, s16, 73728\n\t"
    "v_mfma_f32_16x16x32_bf16 a[116:119], a[232:235], v[184:187], a[116:119]\n\t"
    "buffer_load_dwordx4 v251, s[4:7], s27 offen lds\n\t"
    "v_mfma_f32_16x16x32_bf16 a[168:171], a[244:247], v[180:183], a[168:171]\n\t"
    "s_add_u32 m0, s16, 74752\n\t"
    "s_add_u32 s66, s27, 36864\n\t"
    "v_mfma_f32_16x16x32_bf16 a[24:27], a[236:239], v[172:175], a[24:27]\n\t"
    "buffer_load_dwordx4 v251, s[4:7], s66 offen lds\n\t"
    "v_mfma_f32_16x16x32_bf16 a[56:59], a[236:239], v[176:179], a[56:59]\n\t"
    "global_load_dword v254, v252, s[60:61] sc1\n\t"
    "v_mfma_f32_16x16x32_bf16 a[88:91], a[236:239], v[180:183], a[88:91]\n\t"
    "v_add_u32 v227, 4096, v226\n\t"
    "v_add_u32 v228, 8192, v226\n\t"
    "v_mfma_f32_16x16x32_bf16 a[120:123], a[236:239], v[184:187], a[120:123]\n\t"
    "v_add_u32 v229, 12288, v226\n\t"
    "v_add_u32 v230, 16384, v226\n\t"
    "v_mfma_f32_16x16x32_bf16 a[28:31], a[240:243], v[172:175], a[28:31]\n\t"
    "s_mov_b32 s56, 0x5d800000\n\t"
    "s_lshl_b32 s54, s62, 2\n\t"
    "v_mfma_f32_16x16x32_bf16 a[60:63], a[240:243], v[176:179], a[60:63]\n\t"
    "v_mov_b32 v231, s54\n\t"
    "v_mfma_f32_16x16x32_bf16 a[92:95], a[240:243], v[180:183], a[92:95]\n\t"
    "v_mfma_f32_16x16x32_bf16 a[124:127], a[240:243], v[184:187], a[124:127]\n\t"
    "v_mfma_f32_16x16x32_bf16 a[172:175], a[244:247], v[184:187], a[172:175]\n\t"
    "s_branch EPI0_%=\n\t"
    "DRAIN_O_A_%=:\n\t"
    "ds_read_b64_tr_b16 a[224:225], v210 offset:768\n\t"
    "ds_read_b64_tr_b16 a[226:227], v210 offset:4864\n\t"
    "v_cvt_pk_bf16_f32 v152, v80, v81\n\t"
    "v_cvt_pk_bf16_f32 v153, v82, v83\n\t"
    "ds_read_b64_tr_b16 a[228:229], v210 offset:1024\n\t"
    "ds_read_b64_tr_b16 a[230:231], v210 offset:5120\n\t"
    "v_exp_f32 v106, v106\n\t"
    "ds_read_b64_tr_b16 a[232:233], v210 offset:1280\n\t"
    "ds_read_b64_tr_b16 a[234:235], v210 offset:5376\n\t"
    "v_cvt_pk_bf16_f32 v154, v84, v85\n\t"
    "v_cvt_pk_bf16_f32 v155, v86, v87\n\t"
    "ds_read_b64_tr_b16 a[236:237], v210 offset:1536\n\t"
    "ds_read_b64_tr_b16 a[238:239], v210 offset:5632\n\t"
    "v_exp_f32 v107, v107\n\t"
    "ds_read_b64_tr_b16 a[240:241], v210 offset:1792\n\t"
    "ds_read_b64_tr_b16 a[242:243], v210 offset:5888\n\t"
    "v_cvt_pk_bf16_f32 v156, v88, v89\n\t"
    "v_cvt_pk_bf16_f32 v157, v90, v91\n\t"
    "v_exp_f32 v108, v108\n\t"
    "v_cvt_pk_bf16_f32 v158, v92, v93\n\t"
    "v_cvt_pk_bf16_f32 v159, v94, v95\n\t"
    "v_exp_f32 v109, v109\n\t"
    "v_cvt_pk_bf16_f32 v160, v96, v97\n\t"
    "v_cvt_pk_bf16_f32 v161, v98, v99\n\t"
    "v_exp_f32 v110, v110\n\t"
    "v_cvt_pk_bf16_f32 v162, v100, v101\n\t"
    "v_cvt_pk_bf16_f32 v163, v102, v103\n\t"
    "v_exp_f32 v111, v111\n\t"
    "v_cvt_pk_bf16_f32 v164, v104, v105\n\t"
    "v_cvt_pk_bf16_f32 v165, v106, v107\n\t"
    "v_cvt_pk_bf16_f32 v166, v108, v109\n\t"
    "v_cvt_pk_bf16_f32 v167, v110, v111\n\t"
    "v_exp_f32 v112, v112\n\t"
    "v_exp_f32 v113, v113\n\t"
    "v_exp_f32 v114, v114\n\t"
    "v_exp_f32 v115, v115\n\t"
    "v_cvt_pk_bf16_f32 v168, v112, v113\n\t"
    "s_cbranch_vccnz TR_AdrainO_%=\n\t"
    "BK_AdrainO_%=:\n\t"
    "v_lshlrev_b32 v226, 4, v222\n\t"
    "s_lshl_b32 s52, s62, 13\n\t"
    "s_xor_b32 s53, s52, 0x2000\n\t"
    "v_add_u32 v224, s52, v226\n\t"
    "v_add_u32 v225, s53, v226\n\t"
    "s_waitcnt vmcnt(0) lgkmcnt(0)\n\t"
    "s_barrier\n\t"
    "v_mfma_f32_16x16x32_bf16 a[0:3], a[212:215], v[152:155], a[0:3]\n\t"
    "s_add_u32 m0, s70, 81920\n\t"
    "s_xor_b32 s66, s26, 256\n\t"
    "v_mfma_f32_16x16x32_bf16 a[32:35], a[212:215], v[156:159], a[32:35]\n\t"
    "buffer_load_dwordx4 v247, s[4:7], s66 offen offset:0 lds\n\t"
    "v_mfma_f32_16x16x32_bf16 a[64:67], a[212:215], v[160:163], a[64:67]\n\t"
    "buffer_load_dwordx4 v248, s[4:7], s66 offen offset:1024 lds\n\t"
    "v_exp_f32 v116, v116\n\t"
    "v_mfma_f32_16x16x32_bf16 a[96:99], a[212:215], v[164:167], a[96:99]\n\t"
    "buffer_load_dwordx4 v249, s[4:7], s66 offen offset:2048 lds\n\t"
    "v_exp_f32 v117, v117\n\t"
    "v_mfma_f32_16x16x32_bf16 a[4:7], a[216:219], v[152:155], a[4:7]\n\t"
    "buffer_load_dwordx4 v250, s[4:7], s66 offen offset:3072 lds\n\t"
    "v_mfma_f32_16x16x32_bf16 a[36:39], a[216:219], v[156:159], a[36:39]\n\t"
    "s_add_u32 m0, s70, 32768\n\t"
    "s_xor_b32 s66, s26, 512\n\t"
    "v_mfma_f32_16x16x32_bf16 a[68:71], a[216:219], v[160:163], a[68:71]\n\t"
    "buffer_load_dwordx4 v247, s[4:7], s66 offen offset:0 lds\n\t"
    "v_mfma_f32_16x16x32_bf16 a[100:103], a[216:219], v[164:167], a[100:103]\n\t"
    "buffer_load_dwordx4 v248, s[4:7], s66 offen offset:1024 lds\n\t"
    "v_mfma_f32_16x16x32_bf16 a[160:163], a[244:247], v[152:155], a[160:163]\n\t"
    "buffer_load_dwordx4 v249, s[4:7], s66 offen offset:2048 lds\n\t"
    "v_mfma_f32_16x16x32_bf16 a[8:11], a[220:223], v[152:155], a[8:11]\n\t"
    "buffer_load_dwordx4 v250, s[4:7], s66 offen offset:3072 lds\n\t"
    "v_mfma_f32_16x16x32_bf16 a[40:43], a[220:223], v[156:159], a[40:43]\n\t"
    "s_add_u32 m0, s70, 49152\n\t"
    "s_xor_b32 s66, s26, 768\n\t"
    "v_mfma_f32_16x16x32_bf16 a[72:75], a[220:223], v[160:163], a[72:75]\n\t"
    "buffer_load_dwordx4 v247, s[4:7], s66 offen offset:0 lds\n\t"
    "v_mfma_f32_16x16x32_bf16 a[104:107], a[220:223], v[164:167], a[104:107]\n\t"
    "buffer_load_dwordx4 v248, s[4:7], s66 offen offset:1024 lds\n\t"
    "v_mfma_f32_16x16x32_bf16 a[12:15], a[224:227], v[152:155], a[12:15]\n\t"
    "buffer_load_dwordx4 v249, s[4:7], s66 offen offset:2048 lds\n\t"
    "v_mfma_f32_16x16x32_bf16 a[44:47], a[224:227], v[156:159], a[44:47]\n\t"
    "buffer_load_dwordx4 v250, s[4:7], s66 offen offset:3072 lds\n\t"
    "v_mfma_f32_16x16x32_bf16 a[76:79], a[224:227], v[160:163], a[76:79]\n\t"
    "s_add_u32 m0, s70, 98304\n\t"
    "s_xor_b32 s66, s26, 0\n\t"
    "v_cvt_pk_bf16_f32 v169, v114, v115\n\t"
    "v_mfma_f32_16x16x32_bf16 a[108:111], a[224:227], v[164:167], a[108:111]\n\t"
    "buffer_load_dwordx4 v247, s[4:7], s66 offen offset:0 lds\n\t"
    "v_exp_f32 v118, v118\n\t"
    "v_mfma_f32_16x16x32_bf16 a[164:167], a[244:247], v[156:159], a[164:167]\n\t"
    "buffer_load_dwordx4 v248, s[4:7], s66 offen offset:1024 lds\n\t"
    "v_exp_f32 v119, v119\n\t"
    "v_mfma_f32_16x16x32_bf16 a[16:19], a[228:231], v[152:155], a[16:19]\n\t"
    "buffer_load_dwordx4 v249, s[4:7], s66 offen offset:2048 lds\n\t"
    "v_cvt_pk_bf16_f32 v170, v116, v117\n\t"
    "v_cvt_pk_bf16_f32 v171, v118, v119\n\t"
    "v_mfma_f32_16x16x32_bf16 a[48:51], a[228:231], v[156:159], a[48:51]\n\t"
    "buffer_load_dwordx4 v250, s[4:7], s66 offen offset:3072 lds\n\t"
    "v_mfma_f32_16x16x32_bf16 a[80:83], a[228:231], v[160:163], a[80:83]\n\t"
    "s_add_u32 m0, s16, 65536\n\t"
    "v_mfma_f32_16x16x32_bf16 a[112:115], a[228:231], v[164:167], a[112:115]\n\t"
    "buffer_load_dwordx4 v251, s[4:7], s26 offen lds\n\t"
    "v_mfma_f32_16x16x32_bf16 a[20:23], a[232:235], v[152:155], a[20:23]\n\t"
    "s_add_u32 m0, s16, 66560\n\t"
    "s_add_u32 s66, s26, 36864\n\t"
    "v_mfma_f32_16x16x32_bf16 a[52:55], a[232:235], v[156:159], a[52:55]\n\t"
    "buffer_load_dwordx4 v251, s[4:7], s66 offen lds\n\t"
    "v_mfma_f32_16x16x32_bf16 a[84:87], a[232:235], v[160:163], a[84:87]\n\t"
    "s_add_u32 m0, s16, 73728\n\t"
    "v_mfma_f32_16x16x32_bf16 a[116:119], a[232:235], v[164:167], a[116:119]\n\t"
    "buffer_load_dwordx4 v251, s[4:7], s27 offen lds\n\t"
    "v_mfma_f32_16x16x32_bf16 a[168:171], a[244:247], v[160:163], a[168:171]\n\t"
    "s_add_u32 m0, s16, 74752\n\t"
    "s_add_u32 s66, s27, 36864\n\t"
    "v_mfma_f32_16x16x32_bf16 a[24:27], a[236:239], v[152:155], a[24:27]\n\t"
    "buffer_load_dwordx4 v251, s[4:7], s66 offen lds\n\t"
    "v_mfma_f32_16x16x32_bf16 a[56:59], a[236:239], v[156:159], a[56:59]\n\t"
    "global_load_dword v254, v252, s[60:61] sc1\n\t"
    "v_mfma_f32_16x16x32_bf16 a[88:91], a[236:239], v[160:163], a[88:91]\n\t"
    "v_add_u32 v227, 4096, v226\n\t"
    "v_add_u32 v228, 8192, v226\n\t"
    "v_mfma_f32_16x16x32_bf16 a[120:123], a[236:239], v[164:167], a[120:123]\n\t"
    "v_add_u32 v229, 12288, v226\n\t"
    "v_add_u32 v230, 16384, v226\n\t"
    "v_mfma_f32_16x16x32_bf16 a[28:31], a[240:243], v[152:155], a[28:31]\n\t"
    "s_mov_b32 s56, 0x5d800000\n\t"
    "s_lshl_b32 s54, s62, 2\n\t"
    "v_mfma_f32_16x16x32_bf16 a[60:63], a[240:243], v[156:159], a[60:63]\n\t"
    "v_mov_b32 v231, s54\n\t"
    "v_mfma_f32_16x16x32_bf16 a[92:95], a[240:243], v[160:163], a[92:95]\n\t"
    "v_mfma_f32_16x16x32_bf16 a[124:127], a[240:243], v[164:167], a[124:127]\n\t"
    "v_mfma_f32_16x16x32_bf16 a[172:175], a[244:247], v[164:167], a[172:175]\n\t"
    "v_mfma_f32_16x16x32_bf16 a[128:131], a[212:215], v[168:171], a[128:131]\n\t"
    "v_mfma_f32_16x16x32_bf16 a[132:135], a[216:219], v[168:171], a[132:135]\n\t"
    "v_mfma_f32_16x16x32_bf16 a[136:139], a[220:223], v[168:171], a[136:139]\n\t"
    "v_mfma_f32_16x16x32_bf16 a[140:143], a[224:227], v[168:171], a[140:143]\n\t"
    "v_mfma_f32_16x16x32_bf16 a[144:147], a[228:231], v[168:171], a[144:147]\n\t"
    "v_mfma_f32_16x16x32_bf16 a[148:151], a[232:235], v[168:171], a[148:151]\n\t"
    "v_mfma_f32_16x16x32_bf16 a[152:155], a[236:239], v[168:171], a[152:155]\n\t"
    "v_mfma_f32_16x16x32_bf16 a[156:159], a[240:243], v[168:171], a[156:159]\n\t"
    "v_mfma_f32_16x16x32_bf16 a[176:179], a[244:247], v[168:171], a[176:179]\n\t"
    "s_branch EPI1_%=\n\t"
    "PEEL_E_B_%=:\n\t"
    "s_waitcnt lgkmcnt(0)\n\t"
    "v_mfma_f32_16x16x32_bf16 v[80:83], a[180:183], v[0:3], 0\n\t"
    "v_mfma_f32_16x16x32_bf16 v[84:87], a[196:199], v[0:3], 0\n\t"
    "v_mfma_f32_16x16x32_bf16 v[80:83], a[184:187], v[4:7], v[80:83]\n\t"
    "v_mfma_f32_16x16x32_bf16 v[84:87], a[200:203], v[4:7], v[84:87]\n\t"
    "v_mfma_f32_16x16x32_bf16 v[80:83], a[188:191], v[8:11], v[80:83]\n\t"
    "v_mfma_f32_16x16x32_bf16 v[84:87], a[204:207], v[8:11], v[84:87]\n\t"
    "v_mfma_f32_16x16x32_bf16 v[80:83], a[192:195], v[12:15], v[80:83]\n\t"
    "v_mfma_f32_16x16x32_bf16 v[84:87], a[208:211], v[12:15], v[84:87]\n\t"
    "v_mfma_f32_16x16x32_bf16 v[88:91], a[180:183], v[16:19], 0\n\t"
    "v_mfma_f32_16x16x32_bf16 v[92:95], a[196:199], v[16:19], 0\n\t"
    "v_mfma_f32_16x16x32_bf16 v[88:91], a[184:187], v[20:23], v[88:91]\n\t"
    "v_max3_f32 v224, v80, v81, v82\n\t"
    "v_max3_f32 v225, v83, v84, v85\n\t"
    "v_max3_f32 v224, v224, v86, v87\n\t"
    "v_mfma_f32_16x16x32_bf16 v[92:95], a[200:203], v[20:23], v[92:95]\n\t"
    "v_max_f32 v224, v224, v225\n\t"
    "v_mov_b32 v225, v224\n\t"
    "s_nop 1\n\t"
    "v_permlane16_swap_b32 v224, v225\n\t"
    "v_mfma_f32_16x16x32_bf16 v[88:91], a[188:191], v[24:27], v[88:91]\n\t"
    "v_max_f32 v224, v224, v225\n\t"
    "v_mov_b32 v225, v224\n\t"
    "s_nop 1\n\t"
    "v_permlane32_swap_b32 v224, v225\n\t"
    "v_mfma_f32_16x16x32_bf16 v[92:95], a[204:207], v[24:27], v[92:95]\n\t"
    "v_max_f32 v224, v224, v225\n\t"
    "v_sub_f32 v188, 0, v224\n\t"
    "v_sub_f32 v189, 0, v224\n\t"
    "v_mfma_f32_16x16x32_bf16 v[88:91], a[192:195], v[28:31], v[88:91]\n\t"
    "v_sub_f32 v190, 0, v224\n\t"
    "v_sub_f32 v191, 0, v224\n\t"
    "v_sub_f32 v80, v80, v224\n\t"
    "v_mfma_f32_16x16x32_bf16 v[92:95], a[208:211], v[28:31], v[92:95]\n\t"
    "v_sub_f32 v81, v81, v224\n\t"
    "v_sub_f32 v82, v82, v224\n\t"
    "v_sub_f32 v83, v83, v224\n\t"
    "v_mfma_f32_16x16x32_bf16 v[96:99], a[180:183], v[32:35], 0\n\t"
    "v_mfma_f32_16x16x32_bf16 v[100:103], a[196:199], v[32:35], 0\n\t"
    "v_mfma_f32_16x16x32_bf16 v[96:99], a[184:187], v[36:39], v[96:99]\n\t"
    "v_sub_f32 v84, v84, v224\n\t"
    "v_sub_f32 v85, v85, v224\n\t"
    "v_sub_f32 v86, v86, v224\n\t"
    "v_mfma_f32_16x16x32_bf16 v[100:103], a[200:203], v[36:39], v[100:103]\n\t"
    "v_sub_f32 v87, v87, v224\n\t"
    "v_max3_f32 v226, v88, v89, v90\n\t"
    "v_max3_f32 v227, v91, v92, v93\n\t"
    "v_mfma_f32_16x16x32_bf16 v[96:99], a[188:191], v[40:43], v[96:99]\n\t"
    "v_max3_f32 v226, v226, v94, v95\n\t"
    "v_max_f32 v226, v226, v227\n\t"
    "v_mov_b32 v227, v226\n\t"
    "v_mfma_f32_16x16x32_bf16 v[100:103], a[204:207], v[40:43], v[100:103]\n\t"
    "s_nop 0\n\t"
    "v_permlane16_swap_b32 v226, v227\n\t"
    "v_max_f32 v226, v226, v227\n\t"
    "v_mov_b32 v227, v226\n\t"
    "v_mfma_f32_16x16x32_bf16 v[96:99], a[192:195], v[44:47], v[96:99]\n\t"
    "s_nop 0\n\t"
    "v_permlane32_swap_b32 v226, v227\n\t"
    "v_max_f32 v226, v226, v227\n\t"
    "v_sub_f32 v192, 0, v226\n\t"
    "v_mfma_f32_16x16x32_bf16 v[100:103], a[208:211], v[44:47], v[100:103]\n\t"
    "v_sub_f32 v193, 0, v226\n\t"
    "v_sub_f32 v194, 0, v226\n\t"
    "v_sub_f32 v195, 0, v226\n\t"
    "v_mfma_f32_16x16x32_bf16 v[104:107], a[180:183], v[48:51], 0\n\t"
    "v_mfma_f32_16x16x32_bf16 v[108:111], a[196:199], v[48:51], 0\n\t"
    "v_mfma_f32_16x16x32_bf16 v[104:107], a[184:187], v[52:55], v[104:107]\n\t"
    "v_sub_f32 v88, v88, v226\n\t"
    "v_sub_f32 v89, v89, v226\n\t"
    "v_sub_f32 v90, v90, v226\n\t"
    "v_mfma_f32_16x16x32_bf16 v[108:111], a[200:203], v[52:55], v[108:111]\n\t"
    "v_sub_f32 v91, v91, v226\n\t"
    "v_sub_f32 v92, v92, v226\n\t"
    "v_sub_f32 v93, v93, v226\n\t"
    "v_mfma_f32_16x16x32_bf16 v[104:107], a[188:191], v[56:59], v[104:107]\n\t"
    "v_sub_f32 v94, v94, v226\n\t"
    "v_sub_f32 v95, v95, v226\n\t"
    "v_max3_f32 v224, v96, v97, v98\n\t"
    "v_mfma_f32_16x16x32_bf16 v[108:111], a[204:207], v[56:59], v[108:111]\n\t"
    "v_max3_f32 v225, v99, v100, v101\n\t"
    "v_max3_f32 v224, v224, v102, v103\n\t"
    "v_max_f32 v224, v224, v225\n\t"
    "v_mfma_f32_16x16x32_bf16 v[104:107], a[192:195], v[60:63], v[104:107]\n\t"
    "v_mov_b32 v225, v224\n\t"
    "s_nop 1\n\t"
    "v_permlane16_swap_b32 v224, v225\n\t"
    "v_max_f32 v224, v224, v225\n\t"
    "v_mfma_f32_16x16x32_bf16 v[108:111], a[208:211], v[60:63], v[108:111]\n\t"
    "v_mov_b32 v225, v224\n\t"
    "s_nop 1\n\t"
    "v_permlane32_swap_b32 v224, v225\n\t"
    "v_max_f32 v224, v224, v225\n\t"
    "v_mfma_f32_16x16x32_bf16 v[112:115], a[180:183], v[64:67], 0\n\t"
    "v_mfma_f32_16x16x32_bf16 v[116:119], a[196:199], v[64:67], 0\n\t"
    "v_mfma_f32_16x16x32_bf16 v[112:115], a[184:187], v[68:71], v[112:115]\n\t"
    "v_sub_f32 v196, 0, v224\n\t"
    "v_sub_f32 v197, 0, v224\n\t"
    "v_sub_f32 v198, 0, v224\n\t"
    "v_mfma_f32_16x16x32_bf16 v[116:119], a[200:203], v[68:71], v[116:119]\n\t"
    "v_sub_f32 v199, 0, v224\n\t"
    "v_sub_f32 v96, v96, v224\n\t"
    "v_sub_f32 v97, v97, v224\n\t"
    "v_mfma_f32_16x16x32_bf16 v[112:115], a[188:191], v[72:75], v[112:115]\n\t"
    "v_sub_f32 v98, v98, v224\n\t"
    "v_sub_f32 v99, v99, v224\n\t"
    "v_sub_f32 v100, v100, v224\n\t"
    "v_mfma_f32_16x16x32_bf16 v[116:119], a[204:207], v[72:75], v[116:119]\n\t"
    "v_sub_f32 v101, v101, v224\n\t"
    "v_sub_f32 v102, v102, v224\n\t"
    "v_sub_f32 v103, v103, v224\n\t"
    "v_mfma_f32_16x16x32_bf16 v[112:115], a[192:195], v[76:79], v[112:115]\n\t"
    "v_max3_f32 v226, v104, v105, v106\n\t"
    "v_max3_f32 v227, v107, v108, v109\n\t"
    "v_max3_f32 v226, v226, v110, v111\n\t"
    "v_mfma_f32_16x16x32_bf16 v[116:119], a[208:211], v[76:79], v[116:119]\n\t"
    "v_max_f32 v226, v226, v227\n\t"
    "v_mov_b32 v227, v226\n\t"
    "s_nop 1\n\t"
    "v_permlane16_swap_b32 v226, v227\n\t"
    "v_max_f32 v226, v226, v227\n\t"
    "v_mov_b32 v227, v226\n\t"
    "s_nop 1\n\t"
    "v_permlane32_swap_b32 v226, v227\n\t"
    "v_max_f32 v226, v226, v227\n\t"
    "v_sub_f32 v200, 0, v226\n\t"
    "v_sub_f32 v201, 0, v226\n\t"
    "v_sub_f32 v202, 0, v226\n\t"
    "v_sub_f32 v203, 0, v226\n\t"
    "v_sub_f32 v104, v104, v226\n\t"
    "v_sub_f32 v105, v105, v226\n\t"
    "v_sub_f32 v106, v106, v226\n\t"
    "v_sub_f32 v107, v107, v226\n\t"
    "v_sub_f32 v108, v108, v226\n\t"
    "v_sub_f32 v109, v109, v226\n\t"
    "v_sub_f32 v110, v110, v226\n\t"
    "v_sub_f32 v111, v111, v226\n\t"
    "v_max3_f32 v224, v112, v113, v114\n\t"
    "v_max3_f32 v225, v115, v116, v117\n\t"
    "v_max3_f32 v224, v224, v118, v119\n\t"
    "v_max_f32 v224, v224, v225\n\t"
    "v_mov_b32 v225, v224\n\t"
    "s_nop 1\n\t"
    "v_permlane16_swap_b32 v224, v225\n\t"
    "v_max_f32 v224, v224, v225\n\t"
    "v_mov_b32 v225, v224\n\t"
    "s_nop 1\n\t"
    "v_permlane32_swap_b32 v224, v225\n\t"
    "v_max_f32 v224, v224, v225\n\t"
    "v_sub_f32 v204, 0, v224\n\t"
    "v_sub_f32 v205, 0, v224\n\t"
    "v_sub_f32 v206, 0, v224\n\t"
    "v_sub_f32 v207, 0, v224\n\t"
    "v_sub_f32 v112, v112, v224\n\t"
    "v_sub_f32 v113, v113, v224\n\t"
    "v_sub_f32 v114, v114, v224\n\t"
    "v_sub_f32 v115, v115, v224\n\t"
    "v_sub_f32 v116, v116, v224\n\t"
    "v_sub_f32 v117, v117, v224\n\t"
    "v_sub_f32 v118, v118, v224\n\t"
    "v_sub_f32 v119, v119, v224\n\t"
    "s_waitcnt vmcnt(24) lgkmcnt(0)\n\t"
    "s_barrier\n\t"
    "ds_read_b128 a[180:183], v208 offset:0\n\t"
    "ds_read_b128 a[184:187], v209 offset:0\n\t"
    "ds_read_b128 a[188:191], v208 offset:1024\n\t"
    "ds_read_b128 a[192:195], v209 offset:1024\n\t"
    "ds_read_b128 a[196:199], v208 offset:4096\n\t"
    "ds_read_b128 a[200:203], v209 offset:4096\n\t"
    "ds_read_b128 a[204:207], v208 offset:5120\n\t"
    "ds_read_b128 a[208:211], v209 offset:5120\n\t"
    "buffer_load_dwordx4 v214, s[36:39], s40 offen offset:0 nt lds\n\t"
    "v_exp_f32 v80, v80\n\t"
    "v_exp_f32 v81, v81\n\t"
    "v_exp_f32 v82, v82\n\t"
    "v_exp_f32 v83, v83\n\t"
    "v_exp_f32 v84, v84\n\t"
    "v_exp_f32 v85, v85\n\t"
    "buffer_load_dwordx4 v215, s[36:39], s40 offen offset:1024 nt lds\n\t"
    "v_exp_f32 v86, v86\n\t"
    "v_exp_f32 v87, v87\n\t"
    "v_exp_f32 v88, v88\n\t"
    "v_exp_f32 v89, v89\n\t"
    "v_exp_f32 v90, v90\n\t"
    "v_exp_f32 v91, v91\n\t"
    "v_exp_f32 v92, v92\n\t"
    "buffer_load_dwordx4 v216, s[36:39], s40 offen offset:2048 nt lds\n\t"
    "v_exp_f32 v93, v93\n\t"
    "v_exp_f32 v94, v94\n\t"
    "v_exp_f32 v95, v95\n\t"
    "v_exp_f32 v96, v96\n\t"
    "v_exp_f32 v97, v97\n\t"
    "v_exp_f32 v98, v98\n\t"
    "v_exp_f32 v99, v99\n\t"
    "buffer_load_dwordx4 v217, s[36:39], s40 offen offset:3072 nt lds\n\t"
    "v_exp_f32 v100, v100\n\t"
    "v_exp_f32 v101, v101\n\t"
    "s_add_u32 m0, m0, 4096\n\t"
    "ds_read_b64_tr_b16 a[212:213], v210 offset:0\n\t"
    "ds_read_b64_tr_b16 a[214:215], v210 offset:4096\n\t"
    "v_cvt_pk_bf16_f32 v152, v80, v81\n\t"
    "ds_read_b64_tr_b16 a[216:217], v210 offset:256\n\t"
    "ds_read_b64_tr_b16 a[218:219], v210 offset:4352\n\t"
    "v_exp_f32 v102, v102\n\t"
    "ds_read_b64_tr_b16 a[220:221], v210 offset:512\n\t"
    "ds_read_b64_tr_b16 a[222:223], v210 offset:4608\n\t"
    "buffer_load_dwordx4 v218, s[36:39], s40 offen offset:0 nt lds\n\t"
    "s_branch LOOP_O_B_%=\n\t"
    "PEEL_O_B_%=:\n\t"
    "s_waitcnt lgkmcnt(0)\n\t"
    "v_mfma_f32_16x16x32_bf16 v[120:123], a[180:183], v[0:3], 0\n\t"
    "v_mfma_f32_16x16x32_bf16 v[124:127], a[196:199], v[0:3], 0\n\t"
    "v_mfma_f32_16x16x32_bf16 v[120:123], a[184:187], v[4:7], v[120:123]\n\t"
    "v_mfma_f32_16x16x32_bf16 v[124:127], a[200:203], v[4:7], v[124:127]\n\t"
    "v_mfma_f32_16x16x32_bf16 v[120:123], a[188:191], v[8:11], v[120:123]\n\t"
    "v_mfma_f32_16x16x32_bf16 v[124:127], a[204:207], v[8:11], v[124:127]\n\t"
    "v_mfma_f32_16x16x32_bf16 v[120:123], a[192:195], v[12:15], v[120:123]\n\t"
    "v_mfma_f32_16x16x32_bf16 v[124:127], a[208:211], v[12:15], v[124:127]\n\t"
    "v_mfma_f32_16x16x32_bf16 v[128:131], a[180:183], v[16:19], 0\n\t"
    "v_mfma_f32_16x16x32_bf16 v[132:135], a[196:199], v[16:19], 0\n\t"
    "v_mfma_f32_16x16x32_bf16 v[128:131], a[184:187], v[20:23], v[128:131]\n\t"
    "v_max3_f32 v224, v120, v121, v122\n\t"
    "v_max3_f32 v225, v123, v124, v125\n\t"
    "v_max3_f32 v224, v224, v126, v127\n\t"
    "v_mfma_f32_16x16x32_bf16 v[132:135], a[200:203], v[20:23], v[132:135]\n\t"
    "v_max_f32 v224, v224, v225\n\t"
    "v_mov_b32 v225, v224\n\t"
    "s_nop 1\n\t"
    "v_permlane16_swap_b32 v224, v225\n\t"
    "v_mfma_f32_16x16x32_bf16 v[128:131], a[188:191], v[24:27], v[128:131]\n\t"
    "v_max_f32 v224, v224, v225\n\t"
    "v_mov_b32 v225, v224\n\t"
    "s_nop 1\n\t"
    "v_permlane32_swap_b32 v224, v225\n\t"
    "v_mfma_f32_16x16x32_bf16 v[132:135], a[204:207], v[24:27], v[132:135]\n\t"
    "v_max_f32 v224, v224, v225\n\t"
    "v_sub_f32 v188, 0, v224\n\t"
    "v_sub_f32 v189, 0, v224\n\t"
    "v_mfma_f32_16x16x32_bf16 v[128:131], a[192:195], v[28:31], v[128:131]\n\t"
    "v_sub_f32 v190, 0, v224\n\t"
    "v_sub_f32 v191, 0, v224\n\t"
    "v_sub_f32 v120, v120, v224\n\t"
    "v_mfma_f32_16x16x32_bf16 v[132:135], a[208:211], v[28:31], v[132:135]\n\t"
    "v_sub_f32 v121, v121, v224\n\t"
    "v_sub_f32 v122, v122, v224\n\t"
    "v_sub_f32 v123, v123, v224\n\t"
    "v_mfma_f32_16x16x32_bf16 v[136:139], a[180:183], v[32:35], 0\n\t"
    "v_mfma_f32_16x16x32_bf16 v[140:143], a[196:199], v[32:35], 0\n\t"
    "v_mfma_f32_16x16x32_bf16 v[136:139], a[184:187], v[36:39], v[136:139]\n\t"
    "v_sub_f32 v124, v124, v224\n\t"
    "v_sub_f32 v125, v125, v224\n\t"
    "v_sub_f32 v126, v126, v224\n\t"
    "v_mfma_f32_16x16x32_bf16 v[140:143], a[200:203], v[36:39], v[140:143]\n\t"
    "v_sub_f32 v127, v127, v224\n\t"
    "v_max3_f32 v226, v128, v129, v130\n\t"
    "v_max3_f32 v227, v131, v132, v133\n\t"
    "v_mfma_f32_16x16x32_bf16 v[136:139], a[188:191], v[40:43], v[136:139]\n\t"
    "v_max3_f32 v226, v226, v134, v135\n\t"
    "v_max_f32 v226, v226, v227\n\t"
    "v_mov_b32 v227, v226\n\t"
    "v_mfma_f32_16x16x32_bf16 v[140:143], a[204:207], v[40:43], v[140:143]\n\t"
    "s_nop 0\n\t"
    "v_permlane16_swap_b32 v226, v227\n\t"
    "v_max_f32 v226, v226, v227\n\t"
    "v_mov_b32 v227, v226\n\t"
    "v_mfma_f32_16x16x32_bf16 v[136:139], a[192:195], v[44:47], v[136:139]\n\t"
    "s_nop 0\n\t"
    "v_permlane32_swap_b32 v226, v227\n\t"
    "v_max_f32 v226, v226, v227\n\t"
    "v_sub_f32 v192, 0, v226\n\t"
    "v_mfma_f32_16x16x32_bf16 v[140:143], a[208:211], v[44:47], v[140:143]\n\t"
    "v_sub_f32 v193, 0, v226\n\t"
    "v_sub_f32 v194, 0, v226\n\t"
    "v_sub_f32 v195, 0, v226\n\t"
    "v_mfma_f32_16x16x32_bf16 v[144:147], a[180:183], v[48:51], 0\n\t"
    "v_mfma_f32_16x16x32_bf16 v[148:151], a[196:199], v[48:51], 0\n\t"
    "v_mfma_f32_16x16x32_bf16 v[144:147], a[184:187], v[52:55], v[144:147]\n\t"
    "v_sub_f32 v128, v128, v226\n\t"
    "v_sub_f32 v129, v129, v226\n\t"
    "v_sub_f32 v130, v130, v226\n\t"
    "v_mfma_f32_16x16x32_bf16 v[148:151], a[200:203], v[52:55], v[148:151]\n\t"
    "v_sub_f32 v131, v131, v226\n\t"
    "v_sub_f32 v132, v132, v226\n\t"
    "v_sub_f32 v133, v133, v226\n\t"
    "v_mfma_f32_16x16x32_bf16 v[144:147], a[188:191], v[56:59], v[144:147]\n\t"
    "v_sub_f32 v134, v134, v226\n\t"
    "v_sub_f32 v135, v135, v226\n\t"
    "v_max3_f32 v224, v136, v137, v138\n\t"
    "v_mfma_f32_16x16x32_bf16 v[148:151], a[204:207], v[56:59], v[148:151]\n\t"
    "v_max3_f32 v225, v139, v140, v141\n\t"
    "v_max3_f32 v224, v224, v142, v143\n\t"
    "v_max_f32 v224, v224, v225\n\t"
    "v_mfma_f32_16x16x32_bf16 v[144:147], a[192:195], v[60:63], v[144:147]\n\t"
    "v_mov_b32 v225, v224\n\t"
    "s_nop 1\n\t"
    "v_permlane16_swap_b32 v224, v225\n\t"
    "v_max_f32 v224, v224, v225\n\t"
    "v_mfma_f32_16x16x32_bf16 v[148:151], a[208:211], v[60:63], v[148:151]\n\t"
    "v_mov_b32 v225, v224\n\t"
    "s_nop 1\n\t"
    "v_permlane32_swap_b32 v224, v225\n\t"
    "v_max_f32 v224, v224, v225\n\t"
    "v_mfma_f32_16x16x32_bf16 v[112:115], a[180:183], v[64:67], 0\n\t"
    "v_mfma_f32_16x16x32_bf16 v[116:119], a[196:199], v[64:67], 0\n\t"
    "v_mfma_f32_16x16x32_bf16 v[112:115], a[184:187], v[68:71], v[112:115]\n\t"
    "v_sub_f32 v196, 0, v224\n\t"
    "v_sub_f32 v197, 0, v224\n\t"
    "v_sub_f32 v198, 0, v224\n\t"
    "v_mfma_f32_16x16x32_bf16 v[116:119], a[200:203], v[68:71], v[116:119]\n\t"
    "v_sub_f32 v199, 0, v224\n\t"
    "v_sub_f32 v136, v136, v224\n\t"
    "v_sub_f32 v137, v137, v224\n\t"
    "v_mfma_f32_16x16x32_bf16 v[112:115], a[188:191], v[72:75], v[112:115]\n\t"
    "v_sub_f32 v138, v138, v224\n\t"
    "v_sub_f32 v139, v139, v224\n\t"
    "v_sub_f32 v140, v140, v224\n\t"
    "v_mfma_f32_16x16x32_bf16 v[116:119], a[204:207], v[72:75], v[116:119]\n\t"
    "v_sub_f32 v141, v141, v224\n\t"
    "v_sub_f32 v142, v142, v224\n\t"
    "v_sub_f32 v143, v143, v224\n\t"
    "v_mfma_f32_16x16x32_bf16 v[112:115], a[192:195], v[76:79], v[112:115]\n\t"
    "v_max3_f32 v226, v144, v145, v146\n\t"
    "v_max3_f32 v227, v147, v148, v149\n\t"
    "v_max3_f32 v226, v226, v150, v151\n\t"
    "v_mfma_f32_16x16x32_bf16 v[116:119], a[208:211], v[76:79], v[116:119]\n\t"
    "v_max_f32 v226, v226, v227\n\t"
    "v_mov_b32 v227, v226\n\t"
    "s_nop 1\n\t"
    "v_permlane16_swap_b32 v226, v227\n\t"
    "v_max_f32 v226, v226, v227\n\t"
    "v_mov_b32 v227, v226\n\t"
    "s_nop 1\n\t"
    "v_permlane32_swap_b32 v226, v227\n\t"
    "v_max_f32 v226, v226, v227\n\t"
    "v_sub_f32 v200, 0, v226\n\t"
    "v_sub_f32 v201, 0, v226\n\t"
    "v_sub_f32 v202, 0, v226\n\t"
    "v_sub_f32 v203, 0, v226\n\t"
    "v_sub_f32 v144, v144, v226\n\t"
    "v_sub_f32 v145, v145, v226\n\t"
    "v_sub_f32 v146, v146, v226\n\t"
    "v_sub_f32 v147, v147, v226\n\t"
    "v_sub_f32 v148, v148, v226\n\t"
    "v_sub_f32 v149, v149, v226\n\t"
    "v_sub_f32 v150, v150, v226\n\t"
    "v_sub_f32 v151, v151, v226\n\t"
    "v_max3_f32 v224, v112, v113, v114\n\t"
    "v_max3_f32 v225, v115, v116, v117\n\t"
    "v_max3_f32 v224, v224, v118, v119\n\t"
    "v_max_f32 v224, v224, v225\n\t"
    "v_mov_b32 v225, v224\n\t"
    "s_nop 1\n\t"
    "v_permlane16_swap_b32 v224, v225\n\t"
    "v_max_f32 v224, v224, v225\n\t"
    "v_mov_b32 v225, v224\n\t"
    "s_nop 1\n\t"
    "v_permlane32_swap_b32 v224, v225\n\t"
    "v_max_f32 v224, v224, v225\n\t"
    "v_sub_f32 v204, 0, v224\n\t"
    "v_sub_f32 v205, 0, v224\n\t"
    "v_sub_f32 v206, 0, v224\n\t"
    "v_sub_f32 v207, 0, v224\n\t"
    "s_memrealtime s[56:57]\n\t"
    "s_waitcnt vmcnt(24) lgkmcnt(0)\n\t"
    "s_sub_u32 s54, s34, s56\n\t"
    "s_cmp_gt_i32 s54, 0\n\t"
    "s_cbranch_scc1 TR_PBpeel_%=\n\t"
    "BK_PBpeel_%=:\n\t"
    "s_barrier\n\t"
    "ds_read_b128 a[180:183], v208 offset:0\n\t"
    "ds_read_b128 a[184:187], v209 offset:0\n\t"
    "ds_read_b128 a[188:191], v208 offset:1024\n\t"
    "ds_read_b128 a[192:195], v209 offset:1024\n\t"
    "ds_read_b128 a[196:199], v208 offset:4096\n\t"
    "s_sub_u32 s57, s56, s98\n\t"
    "buffer_load_dwordx4 v214, s[36:39], s40 offen offset:0 nt lds\n\t"
    "s_ashr_i32 s57, s57, 3\n\t"
    "ds_read_b128 a[200:203], v209 offset:4096\n\t"
    "s_min_i32 s57, s57, 8\n\t"
    "s_max_i32 s57, s57, -20\n\t"
    "s_add_u32 s98, s98, s35\n\t"
    "s_sub_u32 s56, s56, s57\n\t"
    "ds_read_b128 a[204:207], v208 offset:5120\n\t"
    "s_add_u32 s34, s56, s35\n\t"
    "s_sub_u32 s35, s35, 6\n\t"
    "s_max_u32 s35, s35, 180\n\t"
    "s_cmp_lt_u32 s44, 2048\n\t"
    "s_cselect_b32 s35, 0, s35\n\t"
    "ds_read_b128 a[208:211], v209 offset:5120\n\t"
    "buffer_load_dwordx4 v215, s[36:39], s40 offen offset:1024 nt lds\n\t"
    "v_exp_f32 v120, v120\n\t"
    "v_exp_f32 v121, v121\n\t"
    "v_exp_f32 v122, v122\n\t"
    "v_exp_f32 v123, v123\n\t"
    "buffer_load_dwordx4 v216, s[36:39], s40 offen offset:2048 nt lds\n\t"
    "v_exp_f32 v124, v124\n\t"
    "v_exp_f32 v125, v125\n\t"
    "v_exp_f32 v126, v126\n\t"
    "v_exp_f32 v127, v127\n\t"
    "v_exp_f32 v128, v128\n\t"
    "v_exp_f32 v129, v129\n\t"
    "v_exp_f32 v130, v130\n\t"
    "buffer_load_dwordx4 v217, s[36:39], s40 offen offset:3072 nt lds\n\t"
    "v_exp_f32 v131, v131\n\t"
    "v_exp_f32 v132, v132\n\t"
    "v_exp_f32 v133, v133\n\t"
    "s_add_u32 m0, m0, 4096\n\t"
    "v_cvt_pk_bf16_f32 v172, v120, v121\n\t"
    "v_exp_f32 v134, v134\n\t"
    "ds_read_b64_tr_b16 a[212:213], v210 offset:0\n\t"
    "ds_read_b64_tr_b16 a[214:215], v210 offset:4096\n\t"
    "v_exp_f32 v135, v135\n\t"
    "ds_read_b64_tr_b16 a[216:217], v210 offset:256\n\t"
    "ds_read_b64_tr_b16 a[218:219], v210 offset:4352\n\t"
    "buffer_load_dwordx4 v218, s[36:39], s40 offen offset:0 nt lds\n\t"
    "ds_read_b64_tr_b16 a[220:221], v210 offset:512\n\t"
    "ds_read_b64_tr_b16 a[222:223], v210 offset:4608\n\t"
    "LOOP_E_B_%=:\n\t"
    "s_waitcnt lgkmcnt(6)\n\t"
    "v_mfma_f32_16x16x32_bf16 v[80:83], a[180:183], v[0:3], v[188:191]\n\t"
    "ds_read_b64_tr_b16 a[224:225], v210 offset:768\n\t"
    "ds_read_b64_tr_b16 a[226:227], v210 offset:4864\n\t"
    "v_mfma_f32_16x16x32_bf16 v[88:91], a[180:183], v[16:19], v[192:195]\n\t"
    "v_exp_f32 v136, v136\n\t"
    "v_mfma_f32_16x16x32_bf16 v[96:99], a[180:183], v[32:35], v[196:199]\n\t"
    "ds_read_b64_tr_b16 a[228:229], v210 offset:1024\n\t"
    "ds_read_b64_tr_b16 a[230:231], v210 offset:5120\n\t"
    "v_mfma_f32_16x16x32_bf16 v[104:107], a[180:183], v[48:51], v[200:203]\n\t"
    "v_exp_f32 v137, v137\n\t"
    "v_mfma_f32_16x16x32_bf16 v[112:115], a[180:183], v[64:67], v[204:207]\n\t"
    "ds_read_b64_tr_b16 a[232:233], v210 offset:1280\n\t"
    "ds_read_b64_tr_b16 a[234:235], v210 offset:5376\n\t"
    "v_mfma_f32_16x16x32_bf16 v[80:83], a[184:187], v[4:7], v[80:83]\n\t"
    "buffer_load_dwordx4 v219, s[36:39], s40 offen offset:1024 nt lds\n\t"
    "v_mfma_f32_16x16x32_bf16 v[88:91], a[184:187], v[20:23], v[88:91]\n\t"
    "v_exp_f32 v138, v138\n\t"
    "v_mfma_f32_16x16x32_bf16 v[96:99], a[184:187], v[36:39], v[96:99]\n\t"
    "v_exp_f32 v139, v139\n\t"
    "v_mfma_f32_16x16x32_bf16 v[104:107], a[184:187], v[52:55], v[104:107]\n\t"
    "ds_read_b64_tr_b16 a[236:237], v210 offset:1536\n\t"
    "ds_read_b64_tr_b16 a[238:239], v210 offset:5632\n\t"
    "v_mfma_f32_16x16x32_bf16 v[112:115], a[184:187], v[68:71], v[112:115]\n\t"
    "v_exp_f32 v140, v140\n\t"
    "v_mfma_f32_16x16x32_bf16 v[80:83], a[188:191], v[8:11], v[80:83]\n\t"
    "v_exp_f32 v141, v141\n\t"
    "v_mfma_f32_16x16x32_bf16 v[88:91], a[188:191], v[24:27], v[88:91]\n\t"
    "v_exp_f32 v142, v142\n\t"
    "v_mfma_f32_16x16x32_bf16 v[96:99], a[188:191], v[40:43], v[96:99]\n\t"
    "ds_read_b64_tr_b16 a[240:241], v210 offset:1792\n\t"
    "ds_read_b64_tr_b16 a[242:243], v210 offset:5888\n\t"
    "v_mfma_f32_16x16x32_bf16 v[104:107], a[188:191], v[56:59], v[104:107]\n\t"
    "v_exp_f32 v143, v143\n\t"
    "v_mfma_f32_16x16x32_bf16 v[112:115], a[188:191], v[72:75], v[112:115]\n\t"
    "buffer_load_dwordx4 v220, s[36:39], s40 offen offset:2048 nt lds\n\t"
    "v_mfma_f32_16x16x32_bf16 v[80:83], a[192:195], v[12:15], v[80:83]\n\t"
    "v_exp_f32 v144, v144\n\t"
    "v_mfma_f32_16x16x32_bf16 v[88:91], a[192:195], v[28:31], v[88:91]\n\t"
    "v_exp_f32 v145, v145\n\t"
    "v_mfma_f32_16x16x32_bf16 v[96:99], a[192:195], v[44:47], v[96:99]\n\t"
    "v_exp_f32 v146, v146\n\t"
    "v_mfma_f32_16x16x32_bf16 v[104:107], a[192:195], v[60:63], v[104:107]\n\t"
    "v_cvt_pk_bf16_f32 v173, v122, v123\n\t"
    "v_cvt_pk_bf16_f32 v174, v124, v125\n\t"
    "v_mfma_f32_16x16x32_bf16 v[112:115], a[192:195], v[76:79], v[112:115]\n\t"
    "v_exp_f32 v147, v147\n\t"
    "v_mfma_f32_16x16x32_bf16 v[84:87], a[196:199], v[0:3], v[188:191]\n\t"
    "v_cvt_pk_bf16_f32 v175, v126, v127\n\t"
    "v_cvt_pk_bf16_f32 v176, v128, v129\n\t"
    "v_mfma_f32_16x16x32_bf16 v[92:95], a[196:199], v[16:19], v[192:195]\n\t"
    "v_exp_f32 v148, v148\n\t"
    "v_mfma_f32_16x16x32_bf16 v[100:103], a[196:199], v[32:35], v[196:199]\n\t"
    "buffer_load_dwordx4 v221, s[36:39], s40 offen offset:3072 nt lds\n\t"
    "v_mfma_f32_16x16x32_bf16 v[108:111], a[196:199], v[48:51], v[200:203]\n\t"
    "v_cvt_pk_bf16_f32 v177, v130, v131\n\t"
    "v_cvt_pk_bf16_f32 v178, v132, v133\n\t"
    "v_mfma_f32_16x16x32_bf16 v[116:119], a[196:199], v[64:67], v[204:207]\n\t"
    "v_exp_f32 v149, v149\n\t"
    "v_mfma_f32_16x16x32_bf16 v[84:87], a[200:203], v[4:7], v[84:87]\n\t"
    "v_cvt_pk_bf16_f32 v179, v134, v135\n\t"
    "v_cvt_pk_bf16_f32 v180, v136, v137\n\t"
    "v_mfma_f32_16x16x32_bf16 v[92:95], a[200:203], v[20:23], v[92:95]\n\t"
    "v_exp_f32 v150, v150\n\t"
    "v_mfma_f32_16x16x32_bf16 v[100:103], a[200:203], v[36:39], v[100:103]\n\t"
    "v_cvt_pk_bf16_f32 v181, v138, v139\n\t"
    "v_cvt_pk_bf16_f32 v182, v140, v141\n\t"
    "v_mfma_f32_16x16x32_bf16 v[108:111], a[200:203], v[52:55], v[108:111]\n\t"
    "s_add_u32 s40, s40, 8192\n\t"
    "s_mov_b32 m0, s41\n\t"
    "v_mfma_f32_16x16x32_bf16 v[116:119], a[200:203], v[68:71], v[116:119]\n\t"
    "s_add_u32 s41, s41, 32768\n\t"
    "v_cvt_pk_bf16_f32 v183, v142, v143\n\t"
    "v_mfma_f32_16x16x32_bf16 v[84:87], a[204:207], v[8:11], v[84:87]\n\t"
    "s_cmp_eq_u32 s41, s43\n\t"
    "s_cselect_b32 s41, s42, s41\n\t"
    "v_mfma_f32_16x16x32_bf16 v[92:95], a[204:207], v[24:27], v[92:95]\n\t"
    "v_add_u32 v208, s41, v211\n\t"
    "v_add_u32 v209, s41, v212\n\t"
    "v_mfma_f32_16x16x32_bf16 v[100:103], a[204:207], v[40:43], v[100:103]\n\t"
    "v_add_u32 v210, s41, v213\n\t"
    "v_cvt_pk_bf16_f32 v184, v144, v145\n\t"
    "v_mfma_f32_16x16x32_bf16 v[108:111], a[204:207], v[56:59], v[108:111]\n\t"
    "v_exp_f32 v151, v151\n\t"
    "v_mfma_f32_16x16x32_bf16 v[116:119], a[204:207], v[72:75], v[116:119]\n\t"
    "v_cvt_pk_bf16_f32 v185, v146, v147\n\t"
    "v_cvt_pk_bf16_f32 v186, v148, v149\n\t"
    "v_mfma_f32_16x16x32_bf16 v[84:87], a[208:211], v[12:15], v[84:87]\n\t"
    "v_cvt_pk_bf16_f32 v187, v150, v151\n\t"
    "v_mfma_f32_16x16x32_bf16 v[92:95], a[208:211], v[28:31], v[92:95]\n\t"
    "s_waitcnt vmcnt(24) lgkmcnt(0)\n\t"
    "s_barrier\n\t"
    "v_mfma_f32_16x16x32_bf16 v[100:103], a[208:211], v[44:47], v[100:103]\n\t"
    "v_mfma_f32_16x16x32_bf16 v[108:111], a[208:211], v[60:63], v[108:111]\n\t"
    "v_mfma_f32_16x16x32_bf16 v[116:119], a[208:211], v[76:79], v[116:119]\n\t"
    "s_cbranch_vccnz TR_BloopE_%=\n\t"
    "BK_BloopE_%=:\n\t"
    "v_mfma_f32_16x16x32_bf16 a[0:3], a[212:215], v[172:175], a[0:3]\n\t"
    "ds_read_b128 a[180:183], v208 offset:0\n\t"
    "ds_read_b128 a[184:187], v209 offset:0\n\t"
    "v_mfma_f32_16x16x32_bf16 a[32:35], a[212:215], v[176:179], a[32:35]\n\t"
    "ds_read_b128 a[188:191], v208 offset:1024\n\t"
    "ds_read_b128 a[192:195], v209 offset:1024\n\t"
    "v_mfma_f32_16x16x32_bf16 a[64:67], a[212:215], v[180:183], a[64:67]\n\t"
    "ds_read_b128 a[196:199], v208 offset:4096\n\t"
    "ds_read_b128 a[200:203], v209 offset:4096\n\t"
    "v_mfma_f32_16x16x32_bf16 a[96:99], a[212:215], v[184:187], a[96:99]\n\t"
    "ds_read_b128 a[204:207], v208 offset:5120\n\t"
    "ds_read_b128 a[208:211], v209 offset:5120\n\t"
    "v_mfma_f32_16x16x32_bf16 a[4:7], a[216:219], v[172:175], a[4:7]\n\t"
    "buffer_load_dwordx4 v214, s[36:39], s40 offen offset:0 nt lds\n\t"
    "v_mfma_f32_16x16x32_bf16 a[36:39], a[216:219], v[176:179], a[36:39]\n\t"
    "v_exp_f32 v80, v80\n\t"
    "v_mfma_f32_16x16x32_bf16 a[68:71], a[216:219], v[180:183], a[68:71]\n\t"
    "v_exp_f32 v81, v81\n\t"
    "v_mfma_f32_16x16x32_bf16 a[100:103], a[216:219], v[184:187], a[100:103]\n\t"
    "v_exp_f32 v82, v82\n\t"
    "v_mfma_f32_16x16x32_bf16 a[160:163], a[244:247], v[172:175], a[160:163]\n\t"
    "v_exp_f32 v83, v83\n\t"
    "v_mfma_f32_16x16x32_bf16 a[8:11], a[220:223], v[172:175], a[8:11]\n\t"
    "v_exp_f32 v84, v84\n\t"
    "v_mfma_f32_16x16x32_bf16 a[40:43], a[220:223], v[176:179], a[40:43]\n\t"
    "v_exp_f32 v85, v85\n\t"
    "v_mfma_f32_16x16x32_bf16 a[72:75], a[220:223], v[180:183], a[72:75]\n\t"
    "buffer_load_dwordx4 v215, s[36:39], s40 offen offset:1024 nt lds\n\t"
    "v_mfma_f32_16x16x32_bf16 a[104:107], a[220:223], v[184:187], a[104:107]\n\t"
    "v_exp_f32 v86, v86\n\t"
    "v_mfma_f32_16x16x32_bf16 a[12:15], a[224:227], v[172:175], a[12:15]\n\t"
    "v_exp_f32 v87, v87\n\t"
    "v_mfma_f32_16x16x32_bf16 a[44:47], a[224:227], v[176:179], a[44:47]\n\t"
    "v_exp_f32 v88, v88\n\t"
    "v_mfma_f32_16x16x32_bf16 a[76:79], a[224:227], v[180:183], a[76:79]\n\t"
    "v_exp_f32 v89, v89\n\t"
    "v_mfma_f32_16x16x32_bf16 a[108:111], a[224:227], v[184:187], a[108:111]\n\t"
    "v_exp_f32 v90, v90\n\t"
    "v_mfma_f32_16x16x32_bf16 a[164:167], a[244:247], v[176:179], a[164:167]\n\t"
    "v_exp_f32 v91, v91\n\t"
    "v_mfma_f32_16x16x32_bf16 a[16:19], a[228:231], v[172:175], a[16:19]\n\t"
    "v_exp_f32 v92, v92\n\t"
    "v_mfma_f32_16x16x32_bf16 a[48:51], a[228:231], v[176:179], a[48:51]\n\t"
    "buffer_load_dwordx4 v216, s[36:39], s40 offen offset:2048 nt lds\n\t"
    "v_mfma_f32_16x16x32_bf16 a[80:83], a[228:231], v[180:183], a[80:83]\n\t"
    "v_exp_f32 v93, v93\n\t"
    "v_mfma_f32_16x16x32_bf16 a[112:115], a[228:231], v[184:187], a[112:115]\n\t"
    "v_exp_f32 v94, v94\n\t"
    "v_mfma_f32_16x16x32_bf16 a[20:23], a[232:235], v[172:175], a[20:23]\n\t"
    "v_exp_f32 v95, v95\n\t"
    "v_mfma_f32_16x16x32_bf16 a[52:55], a[232:235], v[176:179], a[52:55]\n\t"
    "v_exp_f32 v96, v96\n\t"
    "v_mfma_f32_16x16x32_bf16 a[84:87], a[232:235], v[180:183], a[84:87]\n\t"
    "v_exp_f32 v97, v97\n\t"
    "v_mfma_f32_16x16x32_bf16 a[116:119], a[232:235], v[184:187], a[116:119]\n\t"
    "v_exp_f32 v98, v98\n\t"
    "v_mfma_f32_16x16x32_bf16 a[168:171], a[244:247], v[180:183], a[168:171]\n\t"
    "v_exp_f32 v99, v99\n\t"
    "v_mfma_f32_16x16x32_bf16 a[24:27], a[236:239], v[172:175], a[24:27]\n\t"
    "buffer_load_dwordx4 v217, s[36:39], s40 offen offset:3072 nt lds\n\t"
    "v_mfma_f32_16x16x32_bf16 a[56:59], a[236:239], v[176:179], a[56:59]\n\t"
    "v_exp_f32 v100, v100\n\t"
    "v_mfma_f32_16x16x32_bf16 a[88:91], a[236:239], v[180:183], a[88:91]\n\t"
    "v_exp_f32 v101, v101\n\t"
    "v_mfma_f32_16x16x32_bf16 a[120:123], a[236:239], v[184:187], a[120:123]\n\t"
    "s_add_u32 m0, m0, 4096\n\t"
    "ds_read_b64_tr_b16 a[212:213], v210 offset:0\n\t"
    "v_mfma_f32_16x16x32_bf16 a[28:31], a[240:243], v[172:175], a[28:31]\n\t"
    "ds_read_b64_tr_b16 a[214:215], v210 offset:4096\n\t"
    "v_cvt_pk_bf16_f32 v152, v80, v81\n\t"
    "v_mfma_f32_16x16x32_bf16 a[60:63], a[240:243], v[176:179], a[60:63]\n\t"
    "ds_read_b64_tr_b16 a[216:217], v210 offset:256\n\t"
    "ds_read_b64_tr_b16 a[218:219], v210 offset:4352\n\t"
    "v_mfma_f32_16x16x32_bf16 a[92:95], a[240:243], v[180:183], a[92:95]\n\t"
    "v_exp_f32 v102, v102\n\t"
    "v_mfma_f32_16x16x32_bf16 a[124:127], a[240:243], v[184:187], a[124:127]\n\t"
    "ds_read_b64_tr_b16 a[220:221], v210 offset:512\n\t"
    "ds_read_b64_tr_b16 a[222:223], v210 offset:4608\n\t"
    "v_mfma_f32_16x16x32_bf16 a[172:175], a[244:247], v[184:187], a[172:175]\n\t"
    "buffer_load_dwordx4 v218, s[36:39], s40 offen offset:0 nt lds\n\t"
    "s_lshr_b32 s44, s44, 1\n\t"
    "s_cbranch_scc0 DRAIN_O_B_%=\n\t"
    "LOOP_O_B_%=:\n\t"
    "s_waitcnt lgkmcnt(6)\n\t"
    "v_mfma_f32_16x16x32_bf16 v[120:123], a[180:183], v[0:3], v[188:191]\n\t"
    "ds_read_b64_tr_b16 a[224:225], v210 offset:768\n\t"
    "ds_read_b64_tr_b16 a[226:227], v210 offset:4864\n\t"
    "v_mfma_f32_16x16x32_bf16 v[128:131], a[180:183], v[16:19], v[192:195]\n\t"
    "v_exp_f32 v103, v103\n\t"
    "v_mfma_f32_16x16x32_bf16 v[136:139], a[180:183], v[32:35], v[196:199]\n\t"
    "ds_read_b64_tr_b16 a[228:229], v210 offset:1024\n\t"
    "ds_read_b64_tr_b16 a[230:231], v210 offset:5120\n\t"
    "v_mfma_f32_16x16x32_bf16 v[144:147], a[180:183], v[48:51], v[200:203]\n\t"
    "v_exp_f32 v104, v104\n\t"
    "v_mfma_f32_16x16x32_bf16 v[120:123], a[184:187], v[4:7], v[120:123]\n\t"
    "ds_read_b64_tr_b16 a[232:233], v210 offset:1280\n\t"
    "ds_read_b64_tr_b16 a[234:235], v210 offset:5376\n\t"
    "v_mfma_f32_16x16x32_bf16 v[128:131], a[184:187], v[20:23], v[128:131]\n\t"
    "v_exp_f32 v105, v105\n\t"
    "v_mfma_f32_16x16x32_bf16 v[136:139], a[184:187], v[36:39], v[136:139]\n\t"
    "ds_read_b64_tr_b16 a[236:237], v210 offset:1536\n\t"
    "ds_read_b64_tr_b16 a[238:239], v210 offset:5632\n\t"
    "v_mfma_f32_16x16x32_bf16 v[144:147], a[184:187], v[52:55], v[144:147]\n\t"
    "buffer_load_dwordx4 v219, s[36:39], s40 offen offset:1024 nt lds\n\t"
    "v_mfma_f32_16x16x32_bf16 v[120:123], a[188:191], v[8:11], v[120:123]\n\t"
    "ds_read_b64_tr_b16 a[240:241], v210 offset:1792\n\t"
    "v_cvt_pk_bf16_f32 v153, v82, v83\n\t"
    "v_mfma_f32_16x16x32_bf16 v[128:131], a[188:191], v[24:27], v[128:131]\n\t"
    "v_exp_f32 v106, v106\n\t"
    "v_mfma_f32_16x16x32_bf16 v[136:139], a[188:191], v[40:43], v[136:139]\n\t"
    "ds_read_b64_tr_b16 a[242:243], v210 offset:5888\n\t"
    "v_cvt_pk_bf16_f32 v154, v84, v85\n\t"
    "v_mfma_f32_16x16x32_bf16 v[144:147], a[188:191], v[56:59], v[144:147]\n\t"
    "v_exp_f32 v107, v107\n\t"
    "v_mfma_f32_16x16x32_bf16 v[120:123], a[192:195], v[12:15], v[120:123]\n\t"
    "v_cvt_pk_bf16_f32 v155, v86, v87\n\t"
    "v_cvt_pk_bf16_f32 v156, v88, v89\n\t"
    "v_mfma_f32_16x16x32_bf16 v[128:131], a[192:195], v[28:31], v[128:131]\n\t"
    "v_exp_f32 v108, v108\n\t"
    "v_mfma_f32_16x16x32_bf16 v[136:139], a[192:195], v[44:47], v[136:139]\n\t"
    "v_cvt_pk_bf16_f32 v157, v90, v91\n\t"
    "v_cvt_pk_bf16_f32 v158, v92, v93\n\t"
    "v_mfma_f32_16x16x32_bf16 v[144:147], a[192:195], v[60:63], v[144:147]\n\t"
    "buffer_load_dwordx4 v220, s[36:39], s40 offen offset:2048 nt lds\n\t"
    "v_mfma_f32_16x16x32_bf16 v[124:127], a[196:199], v[0:3], v[188:191]\n\t"
    "v_exp_f32 v109, v109\n\t"
    "v_mfma_f32_16x16x32_bf16 v[132:135], a[196:199], v[16:19], v[192:195]\n\t"
    "v_cvt_pk_bf16_f32 v159, v94, v95\n\t"
    "v_cvt_pk_bf16_f32 v160, v96, v97\n\t"
    "v_mfma_f32_16x16x32_bf16 v[140:143], a[196:199], v[32:35], v[196:199]\n\t"
    "v_exp_f32 v110, v110\n\t"
    "v_mfma_f32_16x16x32_bf16 v[148:151], a[196:199], v[48:51], v[200:203]\n\t"
    "v_cvt_pk_bf16_f32 v161, v98, v99\n\t"
    "v_cvt_pk_bf16_f32 v162, v100, v101\n\t"
    "v_mfma_f32_16x16x32_bf16 v[124:127], a[200:203], v[4:7], v[124:127]\n\t"
    "v_exp_f32 v111, v111\n\t"
    "v_mfma_f32_16x16x32_bf16 v[132:135], a[200:203], v[20:23], v[132:135]\n\t"
    "v_cvt_pk_bf16_f32 v163, v102, v103\n\t"
    "v_cvt_pk_bf16_f32 v164, v104, v105\n\t"
    "v_mfma_f32_16x16x32_bf16 v[140:143], a[200:203], v[36:39], v[140:143]\n\t"
    "v_cvt_pk_bf16_f32 v165, v106, v107\n\t"
    "v_cvt_pk_bf16_f32 v166, v108, v109\n\t"
    "v_mfma_f32_16x16x32_bf16 v[148:151], a[200:203], v[52:55], v[148:151]\n\t"
    "buffer_load_dwordx4 v221, s[36:39], s40 offen offset:3072 nt lds\n\t"
    "v_mfma_f32_16x16x32_bf16 v[124:127], a[204:207], v[8:11], v[124:127]\n\t"
    "v_cvt_pk_bf16_f32 v167, v110, v111\n\t"
    "v_mfma_f32_16x16x32_bf16 v[132:135], a[204:207], v[24:27], v[132:135]\n\t"
    "v_exp_f32 v112, v112\n\t"
    "v_mfma_f32_16x16x32_bf16 v[140:143], a[204:207], v[40:43], v[140:143]\n\t"
    "v_exp_f32 v113, v113\n\t"
    "v_mfma_f32_16x16x32_bf16 v[148:151], a[204:207], v[56:59], v[148:151]\n\t"
    "v_exp_f32 v114, v114\n\t"
    "v_mfma_f32_16x16x32_bf16 v[124:127], a[208:211], v[12:15], v[124:127]\n\t"
    "s_add_u32 s40, s40, 8192\n\t"
    "s_mov_b32 m0, s41\n\t"
    "v_mfma_f32_16x16x32_bf16 v[132:135], a[208:211], v[28:31], v[132:135]\n\t"
    "s_add_u32 s41, s41, 32768\n\t"
    "v_cvt_pk_bf16_f32 v168, v112, v113\n\t"
    "v_mfma_f32_16x16x32_bf16 v[140:143], a[208:211], v[44:47], v[140:143]\n\t"
    "s_cmp_eq_u32 s41, s43\n\t"
    "s_cselect_b32 s41, s42, s41\n\t"
    "v_mfma_f32_16x16x32_bf16 v[148:151], a[208:211], v[60:63], v[148:151]\n\t"
    "s_cbranch_vccnz TR_BloopO_%=\n\t"
    "BK_BloopO_%=:\n\t"
    "s_waitcnt lgkmcnt(0)\n\t"
    "v_mfma_f32_16x16x32_bf16 a[0:3], a[212:215], v[152:155], a[0:3]\n\t"
    "s_memrealtime s[56:57]\n\t"
    "v_add_u32 v208, s41, v211\n\t"
    "v_mfma_f32_16x16x32_bf16 a[32:35], a[212:215], v[156:159], a[32:35]\n\t"
    "v_add_u32 v209, s41, v212\n\t"
    "v_add_u32 v210, s41, v213\n\t"
    "v_mfma_f32_16x16x32_bf16 a[64:67], a[212:215], v[160:163], a[64:67]\n\t"
    "v_exp_f32 v115, v115\n\t"
    "v_mfma_f32_16x16x32_bf16 a[96:99], a[212:215], v[164:167], a[96:99]\n\t"
    "v_exp_f32 v116, v116\n\t"
    "v_mfma_f32_16x16x32_bf16 a[4:7], a[216:219], v[152:155], a[4:7]\n\t"
    "s_waitcnt vmcnt(24) lgkmcnt(0)\n\t"
    "s_sub_u32 s54, s34, s56\n\t"
    "s_cmp_gt_i32 s54, 0\n\t"
    "s_cbranch_scc1 TR_PBloop_%=\n\t"
    "BK_PBloop_%=:\n\t"
    "s_barrier\n\t"
    "v_mfma_f32_16x16x32_bf16 a[36:39], a[216:219], v[156:159], a[36:39]\n\t"
    "ds_read_b128 a[180:183], v208 offset:0\n\t"
    "ds_read_b128 a[184:187], v209 offset:0\n\t"
    "v_mfma_f32_16x16x32_bf16 a[68:71], a[216:219], v[160:163], a[68:71]\n\t"
    "ds_read_b128 a[188:191], v208 offset:1024\n\t"
    "ds_read_b128 a[192:195], v209 offset:1024\n\t"
    "v_mfma_f32_16x16x32_bf16 a[100:103], a[216:219], v[164:167], a[100:103]\n\t"
    "ds_read_b128 a[196:199], v208 offset:4096\n\t"
    "s_sub_u32 s57, s56, s98\n\t"
    "v_mfma_f32_16x16x32_bf16 a[160:163], a[244:247], v[152:155], a[160:163]\n\t"
    "buffer_load_dwordx4 v214, s[36:39], s40 offen offset:0 nt lds\n\t"
    "v_mfma_f32_16x16x32_bf16 a[8:11], a[220:223], v[152:155], a[8:11]\n\t"
    "s_ashr_i32 s57, s57, 3\n\t"
    "ds_read_b128 a[200:203], v209 offset:4096\n\t"
    "v_mfma_f32_16x16x32_bf16 a[40:43], a[220:223], v[156:159], a[40:43]\n\t"
    "s_min_i32 s57, s57, 8\n\t"
    "s_max_i32 s57, s57, -20\n\t"
    "v_mfma_f32_16x16x32_bf16 a[72:75], a[220:223], v[160:163], a[72:75]\n\t"
    "s_add_u32 s98, s98, s35\n\t"
    "s_sub_u32 s56, s56, s57\n\t"
    "v_mfma_f32_16x16x32_bf16 a[104:107], a[220:223], v[164:167], a[104:107]\n\t"
    "ds_read_b128 a[204:207], v208 offset:5120\n\t"
    "s_add_u32 s34, s56, s35\n\t"
    "v_mfma_f32_16x16x32_bf16 a[12:15], a[224:227], v[152:155], a[12:15]\n\t"
    "s_sub_u32 s35, s35, 6\n\t"
    "s_max_u32 s35, s35, 180\n\t"
    "v_mfma_f32_16x16x32_bf16 a[44:47], a[224:227], v[156:159], a[44:47]\n\t"
    "s_cmp_lt_u32 s44, 2048\n\t"
    "s_cselect_b32 s35, 0, s35\n\t"
    "v_mfma_f32_16x16x32_bf16 a[76:79], a[224:227], v[160:163], a[76:79]\n\t"
    "ds_read_b128 a[208:211], v209 offset:5120\n\t"
    "v_cvt_pk_bf16_f32 v169, v114, v115\n\t"
    "v_mfma_f32_16x16x32_bf16 a[108:111], a[224:227], v[164:167], a[108:111]\n\t"
    "buffer_load_dwordx4 v215, s[36:39], s40 offen offset:1024 nt lds\n\t"
    "v_mfma_f32_16x16x32_bf16 a[164:167], a[244:247], v[156:159], a[164:167]\n\t"
    "v_exp_f32 v117, v117\n\t"
    "v_mfma_f32_16x16x32_bf16 a[16:19], a[228:231], v[152:155], a[16:19]\n\t"
    "v_exp_f32 v118, v118\n\t"
    "v_mfma_f32_16x16x32_bf16 a[48:51], a[228:231], v[156:159], a[48:51]\n\t"
    "v_exp_f32 v119, v119\n\t"
    "v_mfma_f32_16x16x32_bf16 a[80:83], a[228:231], v[160:163], a[80:83]\n\t"
    "v_cvt_pk_bf16_f32 v170, v116, v117\n\t"
    "v_cvt_pk_bf16_f32 v171, v118, v119\n\t"
    "v_mfma_f32_16x16x32_bf16 a[112:115], a[228:231], v[164:167], a[112:115]\n\t"
    "v_exp_f32 v120, v120\n\t"
    "v_mfma_f32_16x16x32_bf16 a[20:23], a[232:235], v[152:155], a[20:23]\n\t"
    "v_exp_f32 v121, v121\n\t"
    "v_mfma_f32_16x16x32_bf16 a[52:55], a[232:235], v[156:159], a[52:55]\n\t"
    "v_exp_f32 v122, v122\n\t"
    "v_mfma_f32_16x16x32_bf16 a[84:87], a[232:235], v[160:163], a[84:87]\n\t"
    "v_exp_f32 v123, v123\n\t"
    "v_mfma_f32_16x16x32_bf16 a[116:119], a[232:235], v[164:167], a[116:119]\n\t"
    "buffer_load_dwordx4 v216, s[36:39], s40 offen offset:2048 nt lds\n\t"
    "v_mfma_f32_16x16x32_bf16 a[168:171], a[244:247], v[160:163], a[168:171]\n\t"
    "v_exp_f32 v124, v124\n\t"
    "v_mfma_f32_16x16x32_bf16 a[24:27], a[236:239], v[152:155], a[24:27]\n\t"
    "v_exp_f32 v125, v125\n\t"
    "v_mfma_f32_16x16x32_bf16 a[56:59], a[236:239], v[156:159], a[56:59]\n\t"
    "v_exp_f32 v126, v126\n\t"
    "v_mfma_f32_16x16x32_bf16 a[88:91], a[236:239], v[160:163], a[88:91]\n\t"
    "v_exp_f32 v127, v127\n\t"
    "v_mfma_f32_16x16x32_bf16 a[120:123], a[236:239], v[164:167], a[120:123]\n\t"
    "v_exp_f32 v128, v128\n\t"
    "v_mfma_f32_16x16x32_bf16 a[28:31], a[240:243], v[152:155], a[28:31]\n\t"
    "v_exp_f32 v129, v129\n\t"
    "v_mfma_f32_16x16x32_bf16 a[60:63], a[240:243], v[156:159], a[60:63]\n\t"
    "v_exp_f32 v130, v130\n\t"
    "v_mfma_f32_16x16x32_bf16 a[92:95], a[240:243], v[160:163], a[92:95]\n\t"
    "buffer_load_dwordx4 v217, s[36:39], s40 offen offset:3072 nt lds\n\t"
    "v_mfma_f32_16x16x32_bf16 a[124:127], a[240:243], v[164:167], a[124:127]\n\t"
    "v_exp_f32 v131, v131\n\t"
    "v_mfma_f32_16x16x32_bf16 a[172:175], a[244:247], v[164:167], a[172:175]\n\t"
    "v_exp_f32 v132, v132\n\t"
    "v_mfma_f32_16x16x32_bf16 a[128:131], a[212:215], v[168:171], a[128:131]\n\t"
    "v_exp_f32 v133, v133\n\t"
    "v_mfma_f32_16x16x32_bf16 a[132:135], a[216:219], v[168:171], a[132:135]\n\t"
    "s_add_u32 m0, m0, 4096\n\t"
    "v_cvt_pk_bf16_f32 v172, v120, v121\n\t"
    "v_mfma_f32_16x16x32_bf16 a[136:139], a[220:223], v[168:171], a[136:139]\n\t"
    "v_exp_f32 v134, v134\n\t"
    "v_mfma_f32_16x16x32_bf16 a[140:143], a[224:227], v[168:171], a[140:143]\n\t"
    "ds_read_b64_tr_b16 a[212:213], v210 offset:0\n\t"
    "ds_read_b64_tr_b16 a[214:215], v210 offset:4096\n\t"
    "v_mfma_f32_16x16x32_bf16 a[144:147], a[228:231], v[168:171], a[144:147]\n\t"
    "v_exp_f32 v135, v135\n\t"
    "v_mfma_f32_16x16x32_bf16 a[148:151], a[232:235], v[168:171], a[148:151]\n\t"
    "ds_read_b64_tr_b16 a[216:217], v210 offset:256\n\t"
    "ds_read_b64_tr_b16 a[218:219], v210 offset:4352\n\t"
    "v_mfma_f32_16x16x32_bf16 a[152:155], a[236:239], v[168:171], a[152:155]\n\t"
    "buffer_load_dwordx4 v218, s[36:39], s40 offen offset:0 nt lds\n\t"
    "v_mfma_f32_16x16x32_bf16 a[156:159], a[240:243], v[168:171], a[156:159]\n\t"
    "ds_read_b64_tr_b16 a[220:221], v210 offset:512\n\t"
    "ds_read_b64_tr_b16 a[222:223], v210 offset:4608\n\t"
    "v_mfma_f32_16x16x32_bf16 a[176:179], a[244:247], v[168:171], a[176:179]\n\t"
    "TCB_B_%=:\n\t"
    "s_lshr_b32 s44, s44, 1\n\t"
    "s_cbranch_scc1 LOOP_E_B_%=\n\t"
    "ds_read_b64_tr_b16 a[224:225], v210 offset:768\n\t"
    "ds_read_b64_tr_b16 a[226:227], v210 offset:4864\n\t"
    "v_exp_f32 v136, v136\n\t"
    "ds_read_b64_tr_b16 a[228:229], v210 offset:1024\n\t"
    "ds_read_b64_tr_b16 a[230:231], v210 offset:5120\n\t"
    "v_exp_f32 v137, v137\n\t"
    "ds_read_b64_tr_b16 a[232:233], v210 offset:1280\n\t"
    "ds_read_b64_tr_b16 a[234:235], v210 offset:5376\n\t"
    "v_exp_f32 v138, v138\n\t"
    "v_exp_f32 v139, v139\n\t"
    "ds_read_b64_tr_b16 a[236:237], v210 offset:1536\n\t"
    "ds_read_b64_tr_b16 a[238:239], v210 offset:5632\n\t"
    "v_exp_f32 v140, v140\n\t"
    "v_exp_f32 v141, v141\n\t"
    "v_exp_f32 v142, v142\n\t"
    "ds_read_b64_tr_b16 a[240:241], v210 offset:1792\n\t"
    "ds_read_b64_tr_b16 a[242:243], v210 offset:5888\n\t"
    "v_exp_f32 v143, v143\n\t"
    "v_exp_f32 v144, v144\n\t"
    "v_exp_f32 v145, v145\n\t"
    "v_exp_f32 v146, v146\n\t"
    "v_cvt_pk_bf16_f32 v173, v122, v123\n\t"
    "v_cvt_pk_bf16_f32 v174, v124, v125\n\t"
    "v_exp_f32 v147, v147\n\t"
    "v_cvt_pk_bf16_f32 v175, v126, v127\n\t"
    "v_cvt_pk_bf16_f32 v176, v128, v129\n\t"
    "v_exp_f32 v148, v148\n\t"
    "v_cvt_pk_bf16_f32 v177, v130, v131\n\t"
    "v_cvt_pk_bf16_f32 v178, v132, v133\n\t"
    "v_exp_f32 v149, v149\n\t"
    "v_cvt_pk_bf16_f32 v179, v134, v135\n\t"
    "v_cvt_pk_bf16_f32 v180, v136, v137\n\t"
    "v_exp_f32 v150, v150\n\t"
    "v_cvt_pk_bf16_f32 v181, v138, v139\n\t"
    "v_cvt_pk_bf16_f32 v182, v140, v141\n\t"
    "v_cvt_pk_bf16_f32 v183, v142, v143\n\t"
    "v_cvt_pk_bf16_f32 v184, v144, v145\n\t"
    "v_exp_f32 v151, v151\n\t"
    "v_cvt_pk_bf16_f32 v185, v146, v147\n\t"
    "v_cvt_pk_bf16_f32 v186, v148, v149\n\t"
    "v_cvt_pk_bf16_f32 v187, v150, v151\n\t"
    "s_cbranch_vccnz TR_BdrainE_%=\n\t"
    "BK_BdrainE_%=:\n\t"
    "v_lshlrev_b32 v226, 4, v222\n\t"
    "s_lshl_b32 s52, s62, 13\n\t"
    "s_xor_b32 s53, s52, 0x2000\n\t"
    "v_add_u32 v224, s52, v226\n\t"
    "v_add_u32 v225, s53, v226\n\t"
    "s_waitcnt vmcnt(0) lgkmcnt(0)\n\t"
    "s_barrier\n\t"
    "v_mfma_f32_16x16x32_bf16 a[0:3], a[212:215], v[172:175], a[0:3]\n\t"
    "s_add_u32 m0, s70, 81920\n\t"
    "s_xor_b32 s66, s26, 256\n\t"
    "v_mfma_f32_16x16x32_bf16 a[32:35], a[212:215], v[176:179], a[32:35]\n\t"
    "buffer_load_dwordx4 v247, s[4:7], s66 offen offset:0 lds\n\t"
    "v_mfma_f32_16x16x32_bf16 a[64:67], a[212:215], v[180:183], a[64:67]\n\t"
    "buffer_load_dwordx4 v248, s[4:7], s66 offen offset:1024 lds\n\t"
    "v_mfma_f32_16x16x32_bf16 a[96:99], a[212:215], v[184:187], a[96:99]\n\t"
    "buffer_load_dwordx4 v249, s[4:7], s66 offen offset:2048 lds\n\t"
    "v_mfma_f32_16x16x32_bf16 a[4:7], a[216:219], v[172:175], a[4:7]\n\t"
    "buffer_load_dwordx4 v250, s[4:7], s66 offen offset:3072 lds\n\t"
    "v_mfma_f32_16x16x32_bf16 a[36:39], a[216:219], v[176:179], a[36:39]\n\t"
    "s_add_u32 m0, s70, 32768\n\t"
    "s_xor_b32 s66, s26, 512\n\t"
    "v_mfma_f32_16x16x32_bf16 a[68:71], a[216:219], v[180:183], a[68:71]\n\t"
    "buffer_load_dwordx4 v247, s[4:7], s66 offen offset:0 lds\n\t"
    "v_mfma_f32_16x16x32_bf16 a[100:103], a[216:219], v[184:187], a[100:103]\n\t"
    "buffer_load_dwordx4 v248, s[4:7], s66 offen offset:1024 lds\n\t"
    "v_mfma_f32_16x16x32_bf16 a[160:163], a[244:247], v[172:175], a[160:163]\n\t"
    "buffer_load_dwordx4 v249, s[4:7], s66 offen offset:2048 lds\n\t"
    "v_mfma_f32_16x16x32_bf16 a[8:11], a[220:223], v[172:175], a[8:11]\n\t"
    "buffer_load_dwordx4 v250, s[4:7], s66 offen offset:3072 lds\n\t"
    "v_mfma_f32_16x16x32_bf16 a[40:43], a[220:223], v[176:179], a[40:43]\n\t"
    "s_add_u32 m0, s70, 49152\n\t"
    "s_xor_b32 s66, s26, 768\n\t"
    "v_mfma_f32_16x16x32_bf16 a[72:75], a[220:223], v[180:183], a[72:75]\n\t"
    "buffer_load_dwordx4 v247, s[4:7], s66 offen offset:0 lds\n\t"
    "v_mfma_f32_16x16x32_bf16 a[104:107], a[220:223], v[184:187], a[104:107]\n\t"
    "buffer_load_dwordx4 v248, s[4:7], s66 offen offset:1024 lds\n\t"
    "v_mfma_f32_16x16x32_bf16 a[12:15], a[224:227], v[172:175], a[12:15]\n\t"
    "buffer_load_dwordx4 v249, s[4:7], s66 offen offset:2048 lds\n\t"
    "v_mfma_f32_16x16x32_bf16 a[44:47], a[224:227], v[176:179], a[44:47]\n\t"
    "buffer_load_dwordx4 v250, s[4:7], s66 offen offset:3072 lds\n\t"
    "v_mfma_f32_16x16x32_bf16 a[76:79], a[224:227], v[180:183], a[76:79]\n\t"
    "s_add_u32 m0, s70, 98304\n\t"
    "s_xor_b32 s66, s26, 0\n\t"
    "v_mfma_f32_16x16x32_bf16 a[108:111], a[224:227], v[184:187], a[108:111]\n\t"
    "buffer_load_dwordx4 v247, s[4:7], s66 offen offset:0 lds\n\t"
    "v_mfma_f32_16x16x32_bf16 a[164:167], a[244:247], v[176:179], a[164:167]\n\t"
    "buffer_load_dwordx4 v248, s[4:7], s66 offen offset:1024 lds\n\t"
    "v_mfma_f32_16x16x32_bf16 a[16:19], a[228:231], v[172:175], a[16:19]\n\t"
    "buffer_load_dwordx4 v249, s[4:7], s66 offen offset:2048 lds\n\t"
    "v_mfma_f32_16x16x32_bf16 a[48:51], a[228:231], v[176:179], a[48:51]\n\t"
    "buffer_load_dwordx4 v250, s[4:7], s66 offen offset:3072 lds\n\t"
    "v_mfma_f32_16x16x32_bf16 a[80:83], a[228:231], v[180:183], a[80:83]\n\t"
    "s_add_u32 m0, s16, 65536\n\t"
    "v_mfma_f32_16x16x32_bf16 a[112:115], a[228:231], v[184:187], a[112:115]\n\t"
    "buffer_load_dwordx4 v251, s[4:7], s26 offen lds\n\t"
    "v_mfma_f32_16x16x32_bf16 a[20:23], a[232:235], v[172:175], a[20:23]\n\t"
    "s_add_u32 m0, s16, 66560\n\t"
    "s_add_u32 s66, s26, 36864\n\t"
    "v_mfma_f32_16x16x32_bf16 a[52:55], a[232:235], v[176:179], a[52:55]\n\t"
    "buffer_load_dwordx4 v251, s[4:7], s66 offen lds\n\t"
    "v_mfma_f32_16x16x32_bf16 a[84:87], a[232:235], v[180:183], a[84:87]\n\t"
    "s_add_u32 m0, s16, 73728\n\t"
    "v_mfma_f32_16x16x32_bf16 a[116:119], a[232:235], v[184:187], a[116:119]\n\t"
    "buffer_load_dwordx4 v251, s[4:7], s27 offen lds\n\t"
    "v_mfma_f32_16x16x32_bf16 a[168:171], a[244:247], v[180:183], a[168:171]\n\t"
    "s_add_u32 m0, s16, 74752\n\t"
    "s_add_u32 s66, s27, 36864\n\t"
    "v_mfma_f32_16x16x32_bf16 a[24:27], a[236:239], v[172:175], a[24:27]\n\t"
    "buffer_load_dwordx4 v251, s[4:7], s66 offen lds\n\t"
    "v_mfma_f32_16x16x32_bf16 a[56:59], a[236:239], v[176:179], a[56:59]\n\t"
    "global_load_dword v254, v252, s[60:61] sc1\n\t"
    "v_mfma_f32_16x16x32_bf16 a[88:91], a[236:239], v[180:183], a[88:91]\n\t"
    "v_add_u32 v227, 4096, v226\n\t"
    "v_add_u32 v228, 8192, v226\n\t"
    "v_mfma_f32_16x16x32_bf16 a[120:123], a[236:239], v[184:187], a[120:123]\n\t"
    "v_add_u32 v229, 12288, v226\n\t"
    "v_add_u32 v230, 16384, v226\n\t"
    "v_mfma_f32_16x16x32_bf16 a[28:31], a[240:243], v[172:175], a[28:31]\n\t"
    "s_mov_b32 s56, 0x5d800000\n\t"
    "s_lshl_b32 s54, s62, 2\n\t"
    "v_mfma_f32_16x16x32_bf16 a[60:63], a[240:243], v[176:179], a[60:63]\n\t"
    "v_mov_b32 v231, s54\n\t"
    "v_mfma_f32_16x16x32_bf16 a[92:95], a[240:243], v[180:183], a[92:95]\n\t"
    "v_mfma_f32_16x16x32_bf16 a[124:127], a[240:243], v[184:187], a[124:127]\n\t"
    "v_mfma_f32_16x16x32_bf16 a[172:175], a[244:247], v[184:187], a[172:175]\n\t"
    "s_branch EPI0_%=\n\t"
    "DRAIN_O_B_%=:\n\t"
    "ds_read_b64_tr_b16 a[224:225], v210 offset:768\n\t"
    "ds_read_b64_tr_b16 a[226:227], v210 offset:4864\n\t"
    "v_exp_f32 v103, v103\n\t"
    "ds_read_b64_tr_b16 a[228:229], v210 offset:1024\n\t"
    "ds_read_b64_tr_b16 a[230:231], v210 offset:5120\n\t"
    "v_exp_f32 v104, v104\n\t"
    "ds_read_b64_tr_b16 a[232:233], v210 offset:1280\n\t"
    "ds_read_b64_tr_b16 a[234:235], v210 offset:5376\n\t"
    "v_exp_f32 v105, v105\n\t"
    "ds_read_b64_tr_b16 a[236:237], v210 offset:1536\n\t"
    "ds_read_b64_tr_b16 a[238:239], v210 offset:5632\n\t"
    "ds_read_b64_tr_b16 a[240:241], v210 offset:1792\n\t"
    "v_cvt_pk_bf16_f32 v153, v82, v83\n\t"
    "v_exp_f32 v106, v106\n\t"
    "ds_read_b64_tr_b16 a[242:243], v210 offset:5888\n\t"
    "v_cvt_pk_bf16_f32 v154, v84, v85\n\t"
    "v_exp_f32 v107, v107\n\t"
    "v_cvt_pk_bf16_f32 v155, v86, v87\n\t"
    "v_cvt_pk_bf16_f32 v156, v88, v89\n\t"
    "v_exp_f32 v108, v108\n\t"
    "v_cvt_pk_bf16_f32 v157, v90, v91\n\t"
    "v_cvt_pk_bf16_f32 v158, v92, v93\n\t"
    "v_exp_f32 v109, v109\n\t"
    "v_cvt_pk_bf16_f32 v159, v94, v95\n\t"
    "v_cvt_pk_bf16_f32 v160, v96, v97\n\t"
    "v_exp_f32 v110, v110\n\t"
    "v_cvt_pk_bf16_f32 v161, v98, v99\n\t"
    "v_cvt_pk_bf16_f32 v162, v100, v101\n\t"
    "v_exp_f32 v111, v111\n\t"
    "v_cvt_pk_bf16_f32 v163, v102, v103\n\t"
    "v_cvt_pk_bf16_f32 v164, v104, v105\n\t"
    "v_cvt_pk_bf16_f32 v165, v106, v107\n\t"
    "v_cvt_pk_bf16_f32 v166, v108, v109\n\t"
    "v_cvt_pk_bf16_f32 v167, v110, v111\n\t"
    "v_exp_f32 v112, v112\n\t"
    "v_exp_f32 v113, v113\n\t"
    "v_exp_f32 v114, v114\n\t"
    "v_cvt_pk_bf16_f32 v168, v112, v113\n\t"
    "s_cbranch_vccnz TR_BdrainO_%=\n\t"
    "BK_BdrainO_%=:\n\t"
    "v_lshlrev_b32 v226, 4, v222\n\t"
    "s_lshl_b32 s52, s62, 13\n\t"
    "s_xor_b32 s53, s52, 0x2000\n\t"
    "v_add_u32 v224, s52, v226\n\t"
    "v_add_u32 v225, s53, v226\n\t"
    "s_waitcnt vmcnt(0) lgkmcnt(0)\n\t"
    "s_barrier\n\t"
    "v_mfma_f32_16x16x32_bf16 a[0:3], a[212:215], v[152:155], a[0:3]\n\t"
    "s_add_u32 m0, s70, 81920\n\t"
    "s_xor_b32 s66, s26, 256\n\t"
    "v_mfma_f32_16x16x32_bf16 a[32:35], a[212:215], v[156:159], a[32:35]\n\t"
    "buffer_load_dwordx4 v247, s[4:7], s66 offen offset:0 lds\n\t"
    "v_mfma_f32_16x16x32_bf16 a[64:67], a[212:215], v[160:163], a[64:67]\n\t"
    "buffer_load_dwordx4 v248, s[4:7], s66 offen offset:1024 lds\n\t"
    "v_exp_f32 v115, v115\n\t"
    "v_mfma_f32_16x16x32_bf16 a[96:99], a[212:215], v[164:167], a[96:99]\n\t"
    "buffer_load_dwordx4 v249, s[4:7], s66 offen offset:2048 lds\n\t"
    "v_exp_f32 v116, v116\n\t"
    "v_mfma_f32_16x16x32_bf16 a[4:7], a[216:219], v[152:155], a[4:7]\n\t"
    "buffer_load_dwordx4 v250, s[4:7], s66 offen offset:3072 lds\n\t"
    "v_mfma_f32_16x16x32_bf16 a[36:39], a[216:219], v[156:159], a[36:39]\n\t"
    "s_add_u32 m0, s70, 32768\n\t"
    "s_xor_b32 s66, s26, 512\n\t"
    "v_mfma_f32_16x16x32_bf16 a[68:71], a[216:219], v[160:163], a[68:71]\n\t"
    "buffer_load_dwordx4 v247, s[4:7], s66 offen offset:0 lds\n\t"
    "v_mfma_f32_16x16x32_bf16 a[100:103], a[216:219], v[164:167], a[100:103]\n\t"
    "buffer_load_dwordx4 v248, s[4:7], s66 offen offset:1024 lds\n\t"
    "v_mfma_f32_16x16x32_bf16 a[160:163], a[244:247], v[152:155], a[160:163]\n\t"
    "buffer_load_dwordx4 v249, s[4:7], s66 offen offset:2048 lds\n\t"
    "v_mfma_f32_16x16x32_bf16 a[8:11], a[220:223], v[152:155], a[8:11]\n\t"
    "buffer_load_dwordx4 v250, s[4:7], s66 offen offset:3072 lds\n\t"
    "v_mfma_f32_16x16x32_bf16 a[40:43], a[220:223], v[156:159], a[40:43]\n\t"
    "s_add_u32 m0, s70, 49152\n\t"
    "s_xor_b32 s66, s26, 768\n\t"
    "v_mfma_f32_16x16x32_bf16 a[72:75], a[220:223], v[160:163], a[72:75]\n\t"
    "buffer_load_dwordx4 v247, s[4:7], s66 offen offset:0 lds\n\t"
    "v_mfma_f32_16x16x32_bf16 a[104:107], a[220:223], v[164:167], a[104:107]\n\t"
    "buffer_load_dwordx4 v248, s[4:7], s66 offen offset:1024 lds\n\t"
    "v_mfma_f32_16x16x32_bf16 a[12:15], a[224:227], v[152:155], a[12:15]\n\t"
    "buffer_load_dwordx4 v249, s[4:7], s66 offen offset:2048 lds\n\t"
    "v_mfma_f32_16x16x32_bf16 a[44:47], a[224:227], v[156:159], a[44:47]\n\t"
    "buffer_load_dwordx4 v250, s[4:7], s66 offen offset:3072 lds\n\t"
    "v_mfma_f32_16x16x32_bf16 a[76:79], a[224:227], v[160:163], a[76:79]\n\t"
    "s_add_u32 m0, s70, 98304\n\t"
    "s_xor_b32 s66, s26, 0\n\t"
    "v_cvt_pk_bf16_f32 v169, v114, v115\n\t"
    "v_mfma_f32_16x16x32_bf16 a[108:111], a[224:227], v[164:167], a[108:111]\n\t"
    "buffer_load_dwordx4 v247, s[4:7], s66 offen offset:0 lds\n\t"
    "v_mfma_f32_16x16x32_bf16 a[164:167], a[244:247], v[156:159], a[164:167]\n\t"
    "buffer_load_dwordx4 v248, s[4:7], s66 offen offset:1024 lds\n\t"
    "v_exp_f32 v117, v117\n\t"
    "v_mfma_f32_16x16x32_bf16 a[16:19], a[228:231], v[152:155], a[16:19]\n\t"
    "buffer_load_dwordx4 v249, s[4:7], s66 offen offset:2048 lds\n\t"
    "v_exp_f32 v118, v118\n\t"
    "v_mfma_f32_16x16x32_bf16 a[48:51], a[228:231], v[156:159], a[48:51]\n\t"
    "buffer_load_dwordx4 v250, s[4:7], s66 offen offset:3072 lds\n\t"
    "v_exp_f32 v119, v119\n\t"
    "v_mfma_f32_16x16x32_bf16 a[80:83], a[228:231], v[160:163], a[80:83]\n\t"
    "s_add_u32 m0, s16, 65536\n\t"
    "v_cvt_pk_bf16_f32 v170, v116, v117\n\t"
    "v_cvt_pk_bf16_f32 v171, v118, v119\n\t"
    "v_mfma_f32_16x16x32_bf16 a[112:115], a[228:231], v[164:167], a[112:115]\n\t"
    "buffer_load_dwordx4 v251, s[4:7], s26 offen lds\n\t"
    "v_mfma_f32_16x16x32_bf16 a[20:23], a[232:235], v[152:155], a[20:23]\n\t"
    "s_add_u32 m0, s16, 66560\n\t"
    "s_add_u32 s66, s26, 36864\n\t"
    "v_mfma_f32_16x16x32_bf16 a[52:55], a[232:235], v[156:159], a[52:55]\n\t"
    "buffer_load_dwordx4 v251, s[4:7], s66 offen lds\n\t"
    "v_mfma_f32_16x16x32_bf16 a[84:87], a[232:235], v[160:163], a[84:87]\n\t"
    "s_add_u32 m0, s16, 73728\n\t"
    "v_mfma_f32_16x16x32_bf16 a[116:119], a[232:235], v[164:167], a[116:119]\n\t"
    "buffer_load_dwordx4 v251, s[4:7], s27 offen lds\n\t"
    "v_mfma_f32_16x16x32_bf16 a[168:171], a[244:247], v[160:163], a[168:171]\n\t"
    "s_add_u32 m0, s16, 74752\n\t"
    "s_add_u32 s66, s27, 36864\n\t"
    "v_mfma_f32_16x16x32_bf16 a[24:27], a[236:239], v[152:155], a[24:27]\n\t"
    "buffer_load_dwordx4 v251, s[4:7], s66 offen lds\n\t"
    "v_mfma_f32_16x16x32_bf16 a[56:59], a[236:239], v[156:159], a[56:59]\n\t"
    "global_load_dword v254, v252, s[60:61] sc1\n\t"
    "v_mfma_f32_16x16x32_bf16 a[88:91], a[236:239], v[160:163], a[88:91]\n\t"
    "v_add_u32 v227, 4096, v226\n\t"
    "v_add_u32 v228, 8192, v226\n\t"
    "v_mfma_f32_16x16x32_bf16 a[120:123], a[236:239], v[164:167], a[120:123]\n\t"
    "v_add_u32 v229, 12288, v226\n\t"
    "v_add_u32 v230, 16384, v226\n\t"
    "v_mfma_f32_16x16x32_bf16 a[28:31], a[240:243], v[152:155], a[28:31]\n\t"
    "s_mov_b32 s56, 0x5d800000\n\t"
    "s_lshl_b32 s54, s62, 2\n\t"
    "v_mfma_f32_16x16x32_bf16 a[60:63], a[240:243], v[156:159], a[60:63]\n\t"
    "v_mov_b32 v231, s54\n\t"
    "v_mfma_f32_16x16x32_bf16 a[92:95], a[240:243], v[160:163], a[92:95]\n\t"
    "v_mfma_f32_16x16x32_bf16 a[124:127], a[240:243], v[164:167], a[124:127]\n\t"
    "v_mfma_f32_16x16x32_bf16 a[172:175], a[244:247], v[164:167], a[172:175]\n\t"
    "v_mfma_f32_16x16x32_bf16 a[128:131], a[212:215], v[168:171], a[128:131]\n\t"
    "v_mfma_f32_16x16x32_bf16 a[132:135], a[216:219], v[168:171], a[132:135]\n\t"
    "v_mfma_f32_16x16x32_bf16 a[136:139], a[220:223], v[168:171], a[136:139]\n\t"
    "v_mfma_f32_16x16x32_bf16 a[140:143], a[224:227], v[168:171], a[140:143]\n\t"
    "v_mfma_f32_16x16x32_bf16 a[144:147], a[228:231], v[168:171], a[144:147]\n\t"
    "v_mfma_f32_16x16x32_bf16 a[148:151], a[232:235], v[168:171], a[148:151]\n\t"
    "v_mfma_f32_16x16x32_bf16 a[152:155], a[236:239], v[168:171], a[152:155]\n\t"
    "v_mfma_f32_16x16x32_bf16 a[156:159], a[240:243], v[168:171], a[156:159]\n\t"
    "v_mfma_f32_16x16x32_bf16 a[176:179], a[244:247], v[168:171], a[176:179]\n\t"
    "s_branch EPI1_%=\n\t"
    "EPI0_%=:\n\t"
    "v_accvgpr_read_b32 v236, a160\n\t"
    "v_accvgpr_read_b32 v237, a164\n\t"
    "v_accvgpr_read_b32 v238, a168\n\t"
    "s_nop 3\n\t"
    "v_accvgpr_read_b32 v239, a172\n\t"
    "v_accvgpr_read_b32 v205, a176\n\t"
    "ds_write_b128 v224, a[144:147] offset:0\n\t"
    "ds_write_b128 v224, a[148:151] offset:1024\n\t"
    "ds_write_b128 v224, a[152:155] offset:2048\n\t"
    "ds_write_b128 v224, a[156:159] offset:3072\n\t"
    "v_max3_f32 v232, v236, v237, v238\n\t"
    "v_add_f32 v233, v236, v237\n\t"
    "v_max3_f32 v232, v232, v239, v205\n\t"
    "v_add_f32 v234, v238, v239\n\t"
    "v_add_f32 v233, v233, v205\n\t"
    "v_cmp_nlt_f32 vcc, v232, s56\n\t"
    "v_cmp_u_f32 s[54:55], v233, v234\n\t"
    "ds_write_b64 v224, v[204:205] offset:4096\n\t"
    "s_or_b64 vcc, vcc, s[54:55]\n\t"
    "s_cselect_b32 s54, 1, 0\n\t"
    "v_mov_b32 v232, s54\n\t"
    "ds_write_b32 v231, v232 offset:6144\n\t"
    "v_mov_b32 v231, 0\n\t"
    "v_rcp_f32 v232, v236\n\t"
    "v_accvgpr_read_b32 v188, a0\n\t"
    "v_accvgpr_read_b32 v189, a1\n\t"
    "v_accvgpr_read_b32 v190, a2\n\t"
    "v_accvgpr_read_b32 v191, a3\n\t"
    "v_accvgpr_read_b32 v192, a4\n\t"
    "v_accvgpr_read_b32 v193, a5\n\t"
    "v_accvgpr_read_b32 v194, a6\n\t"
    "v_accvgpr_read_b32 v195, a7\n\t"
    "v_pk_mul_f32 v[188:189], v[188:189], v[232:233] op_sel_hi:[1,0]\n\t"
    "v_pk_mul_f32 v[190:191], v[190:191], v[232:233] op_sel_hi:[1,0]\n\t"
    "v_pk_mul_f32 v[192:193], v[192:193], v[232:233] op_sel_hi:[1,0]\n\t"
    "v_pk_mul_f32 v[194:195], v[194:195], v[232:233] op_sel_hi:[1,0]\n\t"
    "v_cvt_pk_bf16_f32 v80, v188, v189\n\t"
    "v_cvt_pk_bf16_f32 v81, v190, v191\n\t"
    "v_cvt_pk_bf16_f32 v82, v192, v193\n\t"
    "v_cvt_pk_bf16_f32 v83, v194, v195\n\t"
    "buffer_store_dwordx4 v[80:83], v226, s[8:11], s30 offen offset:0 sc1\n\t"
    "v_accvgpr_read_b32 v196, a8\n\t"
    "v_accvgpr_read_b32 v197, a9\n\t"
    "v_accvgpr_read_b32 v198, a10\n\t"
    "v_accvgpr_read_b32 v199, a11\n\t"
    "v_accvgpr_read_b32 v200, a12\n\t"
    "v_accvgpr_read_b32 v201, a13\n\t"
    "v_accvgpr_read_b32 v202, a14\n\t"
    "v_accvgpr_read_b32 v203, a15\n\t"
    "v_pk_mul_f32 v[196:197], v[196:197], v[232:233] op_sel_hi:[1,0]\n\t"
    "v_pk_mul_f32 v[198:199], v[198:199], v[232:233] op_sel_hi:[1,0]\n\t"
    "v_pk_mul_f32 v[200:201], v[200:201], v[232:233] op_sel_hi:[1,0]\n\t"
    "v_pk_mul_f32 v[202:203], v[202:203], v[232:233] op_sel_hi:[1,0]\n\t"
    "v_cvt_pk_bf16_f32 v100, v196, v197\n\t"
    "v_cvt_pk_bf16_f32 v101, v198, v199\n\t"
    "v_cvt_pk_bf16_f32 v102, v200, v201\n\t"
    "v_cvt_pk_bf16_f32 v103, v202, v203\n\t"
    "buffer_store_dwordx4 v[100:103], v227, s[8:11], s30 offen offset:1024 sc1\n\t"
    "v_accvgpr_read_b32 v188, a16\n\t"
    "v_accvgpr_read_b32 v189, a17\n\t"
    "v_accvgpr_read_b32 v190, a18\n\t"
    "v_accvgpr_read_b32 v191, a19\n\t"
    "v_accvgpr_read_b32 v192, a20\n\t"
    "v_accvgpr_read_b32 v193, a21\n\t"
    "v_accvgpr_read_b32 v194, a22\n\t"
    "v_accvgpr_read_b32 v195, a23\n\t"
    "v_pk_mul_f32 v[188:189], v[188:189], v[232:233] op_sel_hi:[1,0]\n\t"
    "v_pk_mul_f32 v[190:191], v[190:191], v[232:233] op_sel_hi:[1,0]\n\t"
    "v_pk_mul_f32 v[192:193], v[192:193], v[232:233] op_sel_hi:[1,0]\n\t"
    "v_pk_mul_f32 v[194:195], v[194:195], v[232:233] op_sel_hi:[1,0]\n\t"
    "v_cvt_pk_bf16_f32 v120, v188, v189\n\t"
    "v_cvt_pk_bf16_f32 v121, v190, v191\n\t"
    "v_cvt_pk_bf16_f32 v122, v192, v193\n\t"
    "v_cvt_pk_bf16_f32 v123, v194, v195\n\t"
    "buffer_store_dwordx4 v[120:123], v228, s[8:11], s30 offen offset:2048 sc1\n\t"
    "v_accvgpr_read_b32 v196, a24\n\t"
    "v_accvgpr_read_b32 v197, a25\n\t"
    "v_accvgpr_read_b32 v198, a26\n\t"
    "v_accvgpr_read_b32 v199, a27\n\t"
    "v_accvgpr_read_b32 v200, a28\n\t"
    "v_accvgpr_read_b32 v201, a29\n\t"
    "v_accvgpr_read_b32 v202, a30\n\t"
    "v_accvgpr_read_b32 v203, a31\n\t"
    "v_pk_mul_f32 v[196:197], v[196:197], v[232:233] op_sel_hi:[1,0]\n\t"
    "v_pk_mul_f32 v[198:199], v[198:199], v[232:233] op_sel_hi:[1,0]\n\t"
    "v_pk_mul_f32 v[200:201], v[200:201], v[232:233] op_sel_hi:[1,0]\n\t"
    "v_pk_mul_f32 v[202:203], v[202:203], v[232:233] op_sel_hi:[1,0]\n\t"
    "v_cvt_pk_bf16_f32 v136, v196, v197\n\t"
    "v_cvt_pk_bf16_f32 v137, v198, v199\n\t"
    "v_cvt_pk_bf16_f32 v138, v200, v201\n\t"
    "v_cvt_pk_bf16_f32 v139, v202, v203\n\t"
    "buffer_store_dwordx4 v[136:139], v229, s[8:11], s30 offen offset:2048 sc1\n\t"
    "v_rcp_f32 v234, v237\n\t"
    "v_accvgpr_read_b32 v188, a32\n\t"
    "v_accvgpr_read_b32 v189, a33\n\t"
    "v_accvgpr_read_b32 v190, a34\n\t"
    "v_accvgpr_read_b32 v191, a35\n\t"
    "v_accvgpr_read_b32 v192, a36\n\t"
    "v_accvgpr_read_b32 v193, a37\n\t"
    "v_accvgpr_read_b32 v194, a38\n\t"
    "v_accvgpr_read_b32 v195, a39\n\t"
    "v_pk_mul_f32 v[188:189], v[188:189], v[234:235] op_sel_hi:[1,0]\n\t"
    "v_pk_mul_f32 v[190:191], v[190:191], v[234:235] op_sel_hi:[1,0]\n\t"
    "v_pk_mul_f32 v[192:193], v[192:193], v[234:235] op_sel_hi:[1,0]\n\t"
    "v_pk_mul_f32 v[194:195], v[194:195], v[234:235] op_sel_hi:[1,0]\n\t"
    "v_cvt_pk_bf16_f32 v84, v188, v189\n\t"
    "v_cvt_pk_bf16_f32 v85, v190, v191\n\t"
    "v_cvt_pk_bf16_f32 v86, v192, v193\n\t"
    "v_cvt_pk_bf16_f32 v87, v194, v195\n\t"
    "buffer_store_dwordx4 v[84:87], v226, s[8:11], s30 offen offset:1024 sc1\n\t"
    "v_accvgpr_read_b32 v196, a40\n\t"
    "v_accvgpr_read_b32 v197, a41\n\t"
    "v_accvgpr_read_b32 v198, a42\n\t"
    "v_accvgpr_read_b32 v199, a43\n\t"
    "v_accvgpr_read_b32 v200, a44\n\t"
    "v_accvgpr_read_b32 v201, a45\n\t"
    "v_accvgpr_read_b32 v202, a46\n\t"
    "v_accvgpr_read_b32 v203, a47\n\t"
    "v_pk_mul_f32 v[196:197], v[196:197], v[234:235] op_sel_hi:[1,0]\n\t"
    "v_pk_mul_f32 v[198:199], v[198:199], v[234:235] op_sel_hi:[1,0]\n\t"
    "v_pk_mul_f32 v[200:201], v[200:201], v[234:235] op_sel_hi:[1,0]\n\t"
    "v_pk_mul_f32 v[202:203], v[202:203], v[234:235] op_sel_hi:[1,0]\n\t"
    "v_cvt_pk_bf16_f32 v104, v196, v197\n\t"
    "v_cvt_pk_bf16_f32 v105, v198, v199\n\t"
    "v_cvt_pk_bf16_f32 v106, v200, v201\n\t"
    "v_cvt_pk_bf16_f32 v107, v202, v203\n\t"
    "buffer_store_dwordx4 v[104:107], v227, s[8:11], s30 offen offset:2048 sc1\n\t"
    "v_accvgpr_read_b32 v188, a48\n\t"
    "v_accvgpr_read_b32 v189, a49\n\t"
    "v_accvgpr_read_b32 v190, a50\n\t"
    "v_accvgpr_read_b32 v191, a51\n\t"
    "v_accvgpr_read_b32 v192, a52\n\t"
    "v_accvgpr_read_b32 v193, a53\n\t"
    "v_accvgpr_read_b32 v194, a54\n\t"
    "v_accvgpr_read_b32 v195, a55\n\t"
    "v_pk_mul_f32 v[188:189], v[188:189], v[234:235] op_sel_hi:[1,0]\n\t"
    "v_pk_mul_f32 v[190:191], v[190:191], v[234:235] op_sel_hi:[1,0]\n\t"
    "v_pk_mul_f32 v[192:193], v[192:193], v[234:235] op_sel_hi:[1,0]\n\t"
    "v_pk_mul_f32 v[194:195], v[194:195], v[234:235] op_sel_hi:[1,0]\n\t"
    "v_cvt_pk_bf16_f32 v124, v188, v189\n\t"
    "v_cvt_pk_bf16_f32 v125, v190, v191\n\t"
    "v_cvt_pk_bf16_f32 v126, v192, v193\n\t"
    "v_cvt_pk_bf16_f32 v127, v194, v195\n\t"
    "buffer_store_dwordx4 v[124:127], v228, s[8:11], s30 offen offset:3072 sc1\n\t"
    "v_accvgpr_read_b32 v196, a56\n\t"
    "v_accvgpr_read_b32 v197, a57\n\t"
    "v_accvgpr_read_b32 v198, a58\n\t"
    "v_accvgpr_read_b32 v199, a59\n\t"
    "v_accvgpr_read_b32 v200, a60\n\t"
    "v_accvgpr_read_b32 v201, a61\n\t"
    "v_accvgpr_read_b32 v202, a62\n\t"
    "v_accvgpr_read_b32 v203, a63\n\t"
    "v_pk_mul_f32 v[196:197], v[196:197], v[234:235] op_sel_hi:[1,0]\n\t"
    "v_pk_mul_f32 v[198:199], v[198:199], v[234:235] op_sel_hi:[1,0]\n\t"
    "v_pk_mul_f32 v[200:201], v[200:201], v[234:235] op_sel_hi:[1,0]\n\t"
    "v_pk_mul_f32 v[202:203], v[202:203], v[234:235] op_sel_hi:[1,0]\n\t"
    "v_cvt_pk_bf16_f32 v140, v196, v197\n\t"
    "v_cvt_pk_bf16_f32 v141, v198, v199\n\t"
    "v_cvt_pk_bf16_f32 v142, v200, v201\n\t"
    "v_cvt_pk_bf16_f32 v143, v202, v203\n\t"
    "buffer_store_dwordx4 v[140:143], v229, s[8:11], s30 offen offset:3072 sc1\n\t"
    "s_waitcnt lgkmcnt(0)\n\t"
    "s_barrier\n\t"
    "ds_read_b128 v[168:171], v231 offset:6144\n\t"
    "ds_read_b128 v[172:175], v225 offset:0\n\t"
    "ds_read_b128 v[176:179], v225 offset:1024\n\t"
    "ds_read_b128 v[180:183], v225 offset:2048\n\t"
    "ds_read_b128 v[184:187], v225 offset:3072\n\t"
    "ds_read_b64 v[224:225], v225 offset:4096\n\t"
    "v_rcp_f32 v232, v238\n\t"
    "v_accvgpr_read_b32 v188, a64\n\t"
    "v_accvgpr_read_b32 v189, a65\n\t"
    "v_accvgpr_read_b32 v190, a66\n\t"
    "v_accvgpr_read_b32 v191, a67\n\t"
    "v_accvgpr_read_b32 v192, a68\n\t"
    "v_accvgpr_read_b32 v193, a69\n\t"
    "v_accvgpr_read_b32 v194, a70\n\t"
    "v_accvgpr_read_b32 v195, a71\n\t"
    "v_pk_mul_f32 v[188:189], v[188:189], v[232:233] op_sel_hi:[1,0]\n\t"
    "v_pk_mul_f32 v[190:191], v[190:191], v[232:233] op_sel_hi:[1,0]\n\t"
    "v_pk_mul_f32 v[192:193], v[192:193], v[232:233] op_sel_hi:[1,0]\n\t"
    "v_pk_mul_f32 v[194:195], v[194:195], v[232:233] op_sel_hi:[1,0]\n\t"
    "v_cvt_pk_bf16_f32 v88, v188, v189\n\t"
    "v_cvt_pk_bf16_f32 v89, v190, v191\n\t"
    "v_cvt_pk_bf16_f32 v90, v192, v193\n\t"
    "v_cvt_pk_bf16_f32 v91, v194, v195\n\t"
    "buffer_store_dwordx4 v[88:91], v226, s[8:11], s30 offen offset:2048 sc1\n\t"
    "v_accvgpr_read_b32 v196, a72\n\t"
    "v_accvgpr_read_b32 v197, a73\n\t"
    "v_accvgpr_read_b32 v198, a74\n\t"
    "v_accvgpr_read_b32 v199, a75\n\t"
    "v_accvgpr_read_b32 v200, a76\n\t"
    "v_accvgpr_read_b32 v201, a77\n\t"
    "v_accvgpr_read_b32 v202, a78\n\t"
    "v_accvgpr_read_b32 v203, a79\n\t"
    "v_pk_mul_f32 v[196:197], v[196:197], v[232:233] op_sel_hi:[1,0]\n\t"
    "v_pk_mul_f32 v[198:199], v[198:199], v[232:233] op_sel_hi:[1,0]\n\t"
    "v_pk_mul_f32 v[200:201], v[200:201], v[232:233] op_sel_hi:[1,0]\n\t"
    "v_pk_mul_f32 v[202:203], v[202:203], v[232:233] op_sel_hi:[1,0]\n\t"
    "v_cvt_pk_bf16_f32 v108, v196, v197\n\t"
    "v_cvt_pk_bf16_f32 v109, v198, v199\n\t"
    "v_cvt_pk_bf16_f32 v110, v200, v201\n\t"
    "v_cvt_pk_bf16_f32 v111, v202, v203\n\t"
    "buffer_store_dwordx4 v[108:111], v227, s[8:11], s30 offen offset:3072 sc1\n\t"
    "v_accvgpr_read_b32 v188, a80\n\t"
    "v_accvgpr_read_b32 v189, a81\n\t"
    "v_accvgpr_read_b32 v190, a82\n\t"
    "v_accvgpr_read_b32 v191, a83\n\t"
    "v_accvgpr_read_b32 v192, a84\n\t"
    "v_accvgpr_read_b32 v193, a85\n\t"
    "v_accvgpr_read_b32 v194, a86\n\t"
    "v_accvgpr_read_b32 v195, a87\n\t"
    "v_pk_mul_f32 v[188:189], v[188:189], v[232:233] op_sel_hi:[1,0]\n\t"
    "v_pk_mul_f32 v[190:191], v[190:191], v[232:233] op_sel_hi:[1,0]\n\t"
    "v_pk_mul_f32 v[192:193], v[192:193], v[232:233] op_sel_hi:[1,0]\n\t"
    "v_pk_mul_f32 v[194:195], v[194:195], v[232:233] op_sel_hi:[1,0]\n\t"
    "v_cvt_pk_bf16_f32 v128, v188, v189\n\t"
    "v_cvt_pk_bf16_f32 v129, v190, v191\n\t"
    "v_cvt_pk_bf16_f32 v130, v192, v193\n\t"
    "v_cvt_pk_bf16_f32 v131, v194, v195\n\t"
    "buffer_store_dwordx4 v[128:131], v229, s[8:11], s30 offen offset:0 sc1\n\t"
    "v_accvgpr_read_b32 v196, a88\n\t"
    "v_accvgpr_read_b32 v197, a89\n\t"
    "v_accvgpr_read_b32 v198, a90\n\t"
    "v_accvgpr_read_b32 v199, a91\n\t"
    "v_accvgpr_read_b32 v200, a92\n\t"
    "v_accvgpr_read_b32 v201, a93\n\t"
    "v_accvgpr_read_b32 v202, a94\n\t"
    "v_accvgpr_read_b32 v203, a95\n\t"
    "v_pk_mul_f32 v[196:197], v[196:197], v[232:233] op_sel_hi:[1,0]\n\t"
    "v_pk_mul_f32 v[198:199], v[198:199], v[232:233] op_sel_hi:[1,0]\n\t"
    "v_pk_mul_f32 v[200:201], v[200:201], v[232:233] op_sel_hi:[1,0]\n\t"
    "v_pk_mul_f32 v[202:203], v[202:203], v[232:233] op_sel_hi:[1,0]\n\t"
    "v_cvt_pk_bf16_f32 v144, v196, v197\n\t"
    "v_cvt_pk_bf16_f32 v145, v198, v199\n\t"
    "v_cvt_pk_bf16_f32 v146, v200, v201\n\t"
    "v_cvt_pk_bf16_f32 v147, v202, v203\n\t"
    "buffer_store_dwordx4 v[144:147], v230, s[8:11], s30 offen offset:0 sc1\n\t"
    "s_waitcnt lgkmcnt(0)\n\t"
    "v_or3_b32 v168, v168, v169, v170\n\t"
    "v_or_b32 v168, v168, v171\n\t"
    "v_min_f32 v231, v204, v224\n\t"
    "v_readfirstlane_b32 s54, v168\n\t"
    "v_sub_f32 v170, v231, v204\n\t"
    "v_sub_f32 v206, v231, v224\n\t"
    "v_exp_f32 v170, v170\n\t"
    "v_exp_f32 v206, v206\n\t"
    "s_andn2_b32 s54, s54, s63\n\t"
    "s_cbranch_scc1 REDO_%=\n\t"
    "v_accvgpr_read_b32 v0, a128\n\t"
    "v_accvgpr_read_b32 v1, a129\n\t"
    "v_accvgpr_read_b32 v2, a130\n\t"
    "v_accvgpr_read_b32 v3, a131\n\t"
    "v_accvgpr_read_b32 v4, a132\n\t"
    "v_accvgpr_read_b32 v5, a133\n\t"
    "v_accvgpr_read_b32 v6, a134\n\t"
    "v_accvgpr_read_b32 v7, a135\n\t"
    "v_mul_f32 v233, v170, v205\n\t"
    "v_fma_f32 v233, v206, v225, v233\n\t"
    "v_rcp_f32 v233, v233\n\t"
    "v_accvgpr_read_b32 v8, a136\n\t"
    "v_accvgpr_read_b32 v9, a137\n\t"
    "v_accvgpr_read_b32 v10, a138\n\t"
    "v_accvgpr_read_b32 v11, a139\n\t"
    "v_accvgpr_read_b32 v12, a140\n\t"
    "v_accvgpr_read_b32 v13, a141\n\t"
    "v_accvgpr_read_b32 v14, a142\n\t"
    "v_accvgpr_read_b32 v15, a143\n\t"
    "v_mul_f32 v170, v170, v233\n\t"
    "v_mul_f32 v206, v206, v233\n\t"
    "v_pk_mul_f32 v[0:1], v[0:1], v[170:171] op_sel_hi:[1,0]\n\t"
    "v_pk_mul_f32 v[2:3], v[2:3], v[170:171] op_sel_hi:[1,0]\n\t"
    "v_pk_mul_f32 v[4:5], v[4:5], v[170:171] op_sel_hi:[1,0]\n\t"
    "v_pk_mul_f32 v[6:7], v[6:7], v[170:171] op_sel_hi:[1,0]\n\t"
    "v_pk_fma_f32 v[0:1], v[172:173], v[206:207], v[0:1] op_sel_hi:[1,0,1]\n\t"
    "v_pk_fma_f32 v[2:3], v[174:175], v[206:207], v[2:3] op_sel_hi:[1,0,1]\n\t"
    "v_pk_fma_f32 v[4:5], v[176:177], v[206:207], v[4:5] op_sel_hi:[1,0,1]\n\t"
    "v_pk_fma_f32 v[6:7], v[178:179], v[206:207], v[6:7] op_sel_hi:[1,0,1]\n\t"
    "v_cvt_pk_bf16_f32 v96, v0, v1\n\t"
    "v_cvt_pk_bf16_f32 v97, v2, v3\n\t"
    "v_cvt_pk_bf16_f32 v98, v4, v5\n\t"
    "v_cvt_pk_bf16_f32 v99, v6, v7\n\t"
    "buffer_store_dwordx4 v[96:99], v227, s[8:11], s30 offen offset:0 sc1\n\t"
    "v_pk_mul_f32 v[8:9], v[8:9], v[170:171] op_sel_hi:[1,0]\n\t"
    "v_pk_mul_f32 v[10:11], v[10:11], v[170:171] op_sel_hi:[1,0]\n\t"
    "v_pk_mul_f32 v[12:13], v[12:13], v[170:171] op_sel_hi:[1,0]\n\t"
    "v_pk_mul_f32 v[14:15], v[14:15], v[170:171] op_sel_hi:[1,0]\n\t"
    "v_pk_fma_f32 v[8:9], v[180:181], v[206:207], v[8:9] op_sel_hi:[1,0,1]\n\t"
    "v_pk_fma_f32 v[10:11], v[182:183], v[206:207], v[10:11] op_sel_hi:[1,0,1]\n\t"
    "v_pk_fma_f32 v[12:13], v[184:185], v[206:207], v[12:13] op_sel_hi:[1,0,1]\n\t"
    "v_pk_fma_f32 v[14:15], v[186:187], v[206:207], v[14:15] op_sel_hi:[1,0,1]\n\t"
    "v_cvt_pk_bf16_f32 v116, v8, v9\n\t"
    "v_cvt_pk_bf16_f32 v117, v10, v11\n\t"
    "v_cvt_pk_bf16_f32 v118, v12, v13\n\t"
    "v_cvt_pk_bf16_f32 v119, v14, v15\n\t"
    "buffer_store_dwordx4 v[116:119], v228, s[8:11], s30 offen offset:1024 sc1\n\t"
    "v_lshrrev_b32 v16, 2, v222\n\t"
    "v_and_b32 v17, 3, v222\n\t"
    "v_lshlrev_b32 v16, 6, v16\n\t"
    "v_lshl_add_u32 v16, v17, 3, v16\n\t"
    "v_and_b32 v17, 16, v222\n\t"
    "s_mul_i32 s52, s51, 8224\n\t"
    "v_lshl_add_u32 v16, v17, 1, v16\n\t"
    "s_lshl_b32 s53, s51, 12\n\t"
    "v_xor_b32 v16, s52, v16\n\t"
    "v_lshl_add_u32 v17, v222, 3, s53\n\t"
    "v_xor_b32 v208, 0, v16\n\t"
    "v_xor_b32 v209, 4096, v16\n\t"
    "v_xor_b32 v210, 65536, v16\n\t"
    "v_xor_b32 v211, 69632, v16\n\t"
    "v_xor_b32 v212, 131072, v16\n\t"
    "v_xor_b32 v213, 135168, v16\n\t"
    "v_xor_b32 v214, 32, v16\n\t"
    "v_xor_b32 v215, 4128, v16\n\t"
    "v_xor_b32 v216, 65568, v16\n\t"
    "v_xor_b32 v217, 69664, v16\n\t"
    "v_xor_b32 v218, 131104, v16\n\t"
    "v_xor_b32 v219, 135200, v16\n\t"
    "v_add_u32 v220, 32768, v17\n\t"
    "v_add_u32 v221, 34816, v17\n\t"
    "v_rcp_f32 v234, v239\n\t"
    "v_accvgpr_read_b32 v188, a96\n\t"
    "v_accvgpr_read_b32 v189, a97\n\t"
    "v_accvgpr_read_b32 v190, a98\n\t"
    "v_accvgpr_read_b32 v191, a99\n\t"
    "v_accvgpr_read_b32 v192, a100\n\t"
    "v_accvgpr_read_b32 v193, a101\n\t"
    "v_accvgpr_read_b32 v194, a102\n\t"
    "v_accvgpr_read_b32 v195, a103\n\t"
    "v_pk_mul_f32 v[188:189], v[188:189], v[234:235] op_sel_hi:[1,0]\n\t"
    "v_pk_mul_f32 v[190:191], v[190:191], v[234:235] op_sel_hi:[1,0]\n\t"
    "v_pk_mul_f32 v[192:193], v[192:193], v[234:235] op_sel_hi:[1,0]\n\t"
    "v_pk_mul_f32 v[194:195], v[194:195], v[234:235] op_sel_hi:[1,0]\n\t"
    "v_cvt_pk_bf16_f32 v92, v188, v189\n\t"
    "v_cvt_pk_bf16_f32 v93, v190, v191\n\t"
    "v_cvt_pk_bf16_f32 v94, v192, v193\n\t"
    "v_cvt_pk_bf16_f32 v95, v194, v195\n\t"
    "buffer_store_dwordx4 v[92:95], v226, s[8:11], s30 offen offset:3072 sc1\n\t"
    "v_accvgpr_read_b32 v196, a104\n\t"
    "v_accvgpr_read_b32 v197, a105\n\t"
    "v_accvgpr_read_b32 v198, a106\n\t"
    "v_accvgpr_read_b32 v199, a107\n\t"
    "v_accvgpr_read_b32 v200, a108\n\t"
    "v_accvgpr_read_b32 v201, a109\n\t"
    "v_accvgpr_read_b32 v202, a110\n\t"
    "v_accvgpr_read_b32 v203, a111\n\t"
    "v_pk_mul_f32 v[196:197], v[196:197], v[234:235] op_sel_hi:[1,0]\n\t"
    "v_pk_mul_f32 v[198:199], v[198:199], v[234:235] op_sel_hi:[1,0]\n\t"
    "v_pk_mul_f32 v[200:201], v[200:201], v[234:235] op_sel_hi:[1,0]\n\t"
    "v_pk_mul_f32 v[202:203], v[202:203], v[234:235] op_sel_hi:[1,0]\n\t"
    "v_cvt_pk_bf16_f32 v112, v196, v197\n\t"
    "v_cvt_pk_bf16_f32 v113, v198, v199\n\t"
    "v_cvt_pk_bf16_f32 v114, v200, v201\n\t"
    "v_cvt_pk_bf16_f32 v115, v202, v203\n\t"
    "buffer_store_dwordx4 v[112:115], v228, s[8:11], s30 offen offset:0 sc1\n\t"
    "v_accvgpr_read_b32 v188, a112\n\t"
    "v_accvgpr_read_b32 v189, a113\n\t"
    "v_accvgpr_read_b32 v190, a114\n\t"
    "v_accvgpr_read_b32 v191, a115\n\t"
    "v_accvgpr_read_b32 v192, a116\n\t"
    "v_accvgpr_read_b32 v193, a117\n\t"
    "v_accvgpr_read_b32 v194, a118\n\t"
    "v_accvgpr_read_b32 v195, a119\n\t"
    "v_pk_mul_f32 v[188:189], v[188:189], v[234:235] op_sel_hi:[1,0]\n\t"
    "v_pk_mul_f32 v[190:191], v[190:191], v[234:235] op_sel_hi:[1,0]\n\t"
    "v_pk_mul_f32 v[192:193], v[192:193], v[234:235] op_sel_hi:[1,0]\n\t"
    "v_pk_mul_f32 v[194:195], v[194:195], v[234:235] op_sel_hi:[1,0]\n\t"
    "v_cvt_pk_bf16_f32 v132, v188, v189\n\t"
    "v_cvt_pk_bf16_f32 v133, v190, v191\n\t"
    "v_cvt_pk_bf16_f32 v134, v192, v193\n\t"
    "v_cvt_pk_bf16_f32 v135, v194, v195\n\t"
    "buffer_store_dwordx4 v[132:135], v229, s[8:11], s30 offen offset:1024 sc1\n\t"
    "v_accvgpr_read_b32 v196, a120\n\t"
    "v_accvgpr_read_b32 v197, a121\n\t"
    "v_accvgpr_read_b32 v198, a122\n\t"
    "v_accvgpr_read_b32 v199, a123\n\t"
    "v_accvgpr_read_b32 v200, a124\n\t"
    "v_accvgpr_read_b32 v201, a125\n\t"
    "v_accvgpr_read_b32 v202, a126\n\t"
    "v_accvgpr_read_b32 v203, a127\n\t"
    "v_pk_mul_f32 v[196:197], v[196:197], v[234:235] op_sel_hi:[1,0]\n\t"
    "v_pk_mul_f32 v[198:199], v[198:199], v[234:235] op_sel_hi:[1,0]\n\t"
    "v_pk_mul_f32 v[200:201], v[200:201], v[234:235] op_sel_hi:[1,0]\n\t"
    "v_pk_mul_f32 v[202:203], v[202:203], v[234:235] op_sel_hi:[1,0]\n\t"
    "v_cvt_pk_bf16_f32 v148, v196, v197\n\t"
    "v_cvt_pk_bf16_f32 v149, v198, v199\n\t"
    "v_cvt_pk_bf16_f32 v150, v200, v201\n\t"
    "v_cvt_pk_bf16_f32 v151, v202, v203\n\t"
    "buffer_store_dwordx4 v[148:151], v230, s[8:11], s30 offen offset:1024 sc1\n\t"
    "s_waitcnt vmcnt(19) lgkmcnt(0)\n\t"
    "s_barrier\n\t"
    "ds_read_b64_tr_b16 v[152:153], v210 offset:16384\n\t"
    "ds_read_b64_tr_b16 v[154:155], v210 offset:17408\n\t"
    "ds_read_b64_tr_b16 v[156:157], v216 offset:16384\n\t"
    "ds_read_b64_tr_b16 v[158:159], v216 offset:17408\n\t"
    "ds_read_b64_tr_b16 v[160:161], v208 offset:32768\n\t"
    "ds_read_b64_tr_b16 v[162:163], v208 offset:33792\n\t"
    "ds_read_b64_tr_b16 v[164:165], v214 offset:32768\n\t"
    "ds_read_b64_tr_b16 v[166:167], v214 offset:33792\n\t"
    "s_branch P3_%=\n\t"
    "EPI1_%=:\n\t"
    "v_accvgpr_read_b32 v236, a160\n\t"
    "v_accvgpr_read_b32 v237, a164\n\t"
    "v_accvgpr_read_b32 v238, a168\n\t"
    "v_accvgpr_read_b32 v239, a172\n\t"
    "s_nop 2\n\t"
    "v_accvgpr_read_b32 v205, a176\n\t"
    "ds_write_b128 v224, a[128:131] offset:0\n\t"
    "ds_write_b128 v224, a[132:135] offset:1024\n\t"
    "ds_write_b128 v224, a[136:139] offset:2048\n\t"
    "ds_write_b128 v224, a[140:143] offset:3072\n\t"
    "v_max3_f32 v232, v236, v237, v238\n\t"
    "v_add_f32 v233, v236, v237\n\t"
    "v_max3_f32 v232, v232, v239, v205\n\t"
    "v_add_f32 v234, v238, v239\n\t"
    "v_add_f32 v233, v233, v205\n\t"
    "v_cmp_nlt_f32 vcc, v232, s56\n\t"
    "v_cmp_u_f32 s[54:55], v233, v234\n\t"
    "ds_write_b64 v224, v[204:205] offset:4096\n\t"
    "s_or_b64 vcc, vcc, s[54:55]\n\t"
    "s_cselect_b32 s54, 1, 0\n\t"
    "v_mov_b32 v232, s54\n\t"
    "ds_write_b32 v231, v232 offset:6144\n\t"
    "v_mov_b32 v231, 0\n\t"
    "v_rcp_f32 v232, v236\n\t"
    "v_accvgpr_read_b32 v188, a16\n\t"
    "v_accvgpr_read_b32 v189, a17\n\t"
    "v_accvgpr_read_b32 v190, a18\n\t"
    "v_accvgpr_read_b32 v191, a19\n\t"
    "v_accvgpr_read_b32 v192, a20\n\t"
    "v_accvgpr_read_b32 v193, a21\n\t"
    "v_accvgpr_read_b32 v194, a22\n\t"
    "v_accvgpr_read_b32 v195, a23\n\t"
    "v_pk_mul_f32 v[188:189], v[188:189], v[232:233] op_sel_hi:[1,0]\n\t"
    "v_pk_mul_f32 v[190:191], v[190:191], v[232:233] op_sel_hi:[1,0]\n\t"
    "v_pk_mul_f32 v[192:193], v[192:193], v[232:233] op_sel_hi:[1,0]\n\t"
    "v_pk_mul_f32 v[194:195], v[194:195], v[232:233] op_sel_hi:[1,0]\n\t"
    "v_cvt_pk_bf16_f32 v80, v188, v189\n\t"
    "v_cvt_pk_bf16_f32 v81, v190, v191\n\t"
    "v_cvt_pk_bf16_f32 v82, v192, v193\n\t"
    "v_cvt_pk_bf16_f32 v83, v194, v195\n\t"
    "buffer_store_dwordx4 v[80:83], v226, s[8:11], s30 offen offset:0 sc1\n\t"
    "v_accvgpr_read_b32 v196, a24\n\t"
    "v_accvgpr_read_b32 v197, a25\n\t"
    "v_accvgpr_read_b32 v198, a26\n\t"
    "v_accvgpr_read_b32 v199, a27\n\t"
    "v_accvgpr_read_b32 v200, a28\n\t"
    "v_accvgpr_read_b32 v201, a29\n\t"
    "v_accvgpr_read_b32 v202, a30\n\t"
    "v_accvgpr_read_b32 v203, a31\n\t"
    "v_pk_mul_f32 v[196:197], v[196:197], v[232:233] op_sel_hi:[1,0]\n\t"
    "v_pk_mul_f32 v[198:199], v[198:199], v[232:233] op_sel_hi:[1,0]\n\t"
    "v_pk_mul_f32 v[200:201], v[200:201], v[232:233] op_sel_hi:[1,0]\n\t"
    "v_pk_mul_f32 v[202:203], v[202:203], v[232:233] op_sel_hi:[1,0]\n\t"
    "v_cvt_pk_bf16_f32 v100, v196, v197\n\t"
    "v_cvt_pk_bf16_f32 v101, v198, v199\n\t"
    "v_cvt_pk_bf16_f32 v102, v200, v201\n\t"
    "v_cvt_pk_bf16_f32 v103, v202, v203\n\t"
    "buffer_store_dwordx4 v[100:103], v227, s[8:11], s30 offen offset:1024 sc1\n\t"
    "v_accvgpr_read_b32 v188, a0\n\t"
    "v_accvgpr_read_b32 v189, a1\n\t"
    "v_accvgpr_read_b32 v190, a2\n\t"
    "v_accvgpr_read_b32 v191, a3\n\t"
    "v_accvgpr_read_b32 v192, a4\n\t"
    "v_accvgpr_read_b32 v193, a5\n\t"
    "v_accvgpr_read_b32 v194, a6\n\t"
    "v_accvgpr_read_b32 v195, a7\n\t"
    "v_pk_mul_f32 v[188:189], v[188:189], v[232:233] op_sel_hi:[1,0]\n\t"
    "v_pk_mul_f32 v[190:191], v[190:191], v[232:233] op_sel_hi:[1,0]\n\t"
    "v_pk_mul_f32 v[192:193], v[192:193], v[232:233] op_sel_hi:[1,0]\n\t"
    "v_pk_mul_f32 v[194:195], v[194:195], v[232:233] op_sel_hi:[1,0]\n\t"
    "v_cvt_pk_bf16_f32 v120, v188, v189\n\t"
    "v_cvt_pk_bf16_f32 v121, v190, v191\n\t"
    "v_cvt_pk_bf16_f32 v122, v192, v193\n\t"
    "v_cvt_pk_bf16_f32 v123, v194, v195\n\t"
    "buffer_store_dwordx4 v[120:123], v228, s[8:11], s30 offen offset:2048 sc1\n\t"
    "v_accvgpr_read_b32 v196, a8\n\t"
    "v_accvgpr_read_b32 v197, a9\n\t"
    "v_accvgpr_read_b32 v198, a10\n\t"
    "v_accvgpr_read_b32 v199, a11\n\t"
    "v_accvgpr_read_b32 v200, a12\n\t"
    "v_accvgpr_read_b32 v201, a13\n\t"
    "v_accvgpr_read_b32 v202, a14\n\t"
    "v_accvgpr_read_b32 v203, a15\n\t"
    "v_pk_mul_f32 v[196:197], v[196:197], v[232:233] op_sel_hi:[1,0]\n\t"
    "v_pk_mul_f32 v[198:199], v[198:199], v[232:233] op_sel_hi:[1,0]\n\t"
    "v_pk_mul_f32 v[200:201], v[200:201], v[232:233] op_sel_hi:[1,0]\n\t"
    "v_pk_mul_f32 v[202:203], v[202:203], v[232:233] op_sel_hi:[1,0]\n\t"
    "v_cvt_pk_bf16_f32 v136, v196, v197\n\t"
    "v_cvt_pk_bf16_f32 v137, v198, v199\n\t"
    "v_cvt_pk_bf16_f32 v138, v200, v201\n\t"
    "v_cvt_pk_bf16_f32 v139, v202, v203\n\t"
    "buffer_store_dwordx4 v[136:139], v229, s[8:11], s30 offen offset:2048 sc1\n\t"
    "v_rcp_f32 v234, v237\n\t"
    "v_accvgpr_read_b32 v188, a48\n\t"
    "v_accvgpr_read_b32 v189, a49\n\t"
    "v_accvgpr_read_b32 v190, a50\n\t"
    "v_accvgpr_read_b32 v191, a51\n\t"
    "v_accvgpr_read_b32 v192, a52\n\t"
    "v_accvgpr_read_b32 v193, a53\n\t"
    "v_accvgpr_read_b32 v194, a54\n\t"
    "v_accvgpr_read_b32 v195, a55\n\t"
    "v_pk_mul_f32 v[188:189], v[188:189], v[234:235] op_sel_hi:[1,0]\n\t"
    "v_pk_mul_f32 v[190:191], v[190:191], v[234:235] op_sel_hi:[1,0]\n\t"
    "v_pk_mul_f32 v[192:193], v[192:193], v[234:235] op_sel_hi:[1,0]\n\t"
    "v_pk_mul_f32 v[194:195], v[194:195], v[234:235] op_sel_hi:[1,0]\n\t"
    "v_cvt_pk_bf16_f32 v84, v188, v189\n\t"
    "v_cvt_pk_bf16_f32 v85, v190, v191\n\t"
    "v_cvt_pk_bf16_f32 v86, v192, v193\n\t"
    "v_cvt_pk_bf16_f32 v87, v194, v195\n\t"
    "buffer_store_dwordx4 v[84:87], v226, s[8:11], s30 offen offset:1024 sc1\n\t"
    "v_accvgpr_read_b32 v196, a56\n\t"
    "v_accvgpr_read_b32 v197, a57\n\t"
    "v_accvgpr_read_b32 v198, a58\n\t"
    "v_accvgpr_read_b32 v199, a59\n\t"
    "v_accvgpr_read_b32 v200, a60\n\t"
    "v_accvgpr_read_b32 v201, a61\n\t"
    "v_accvgpr_read_b32 v202, a62\n\t"
    "v_accvgpr_read_b32 v203, a63\n\t"
    "v_pk_mul_f32 v[196:197], v[196:197], v[234:235] op_sel_hi:[1,0]\n\t"
    "v_pk_mul_f32 v[198:199], v[198:199], v[234:235] op_sel_hi:[1,0]\n\t"
    "v_pk_mul_f32 v[200:201], v[200:201], v[234:235] op_sel_hi:[1,0]\n\t"
    "v_pk_mul_f32 v[202:203], v[202:203], v[234:235] op_sel_hi:[1,0]\n\t"
    "v_cvt_pk_bf16_f32 v104, v196, v197\n\t"
    "v_cvt_pk_bf16_f32 v105, v198, v199\n\t"
    "v_cvt_pk_bf16_f32 v106, v200, v201\n\t"
    "v_cvt_pk_bf16_f32 v107, v202, v203\n\t"
    "buffer_store_dwordx4 v[104:107], v227, s[8:11], s30 offen offset:2048 sc1\n\t"
    "v_accvgpr_read_b32 v188, a32\n\t"
    "v_accvgpr_read_b32 v189, a33\n\t"
    "v_accvgpr_read_b32 v190, a34\n\t"
    "v_accvgpr_read_b32 v191, a35\n\t"
    "v_accvgpr_read_b32 v192, a36\n\t"
    "v_accvgpr_read_b32 v193, a37\n\t"
    "v_accvgpr_read_b32 v194, a38\n\t"
    "v_accvgpr_read_b32 v195, a39\n\t"
    "v_pk_mul_f32 v[188:189], v[188:189], v[234:235] op_sel_hi:[1,0]\n\t"
    "v_pk_mul_f32 v[190:191], v[190:191], v[234:235] op_sel_hi:[1,0]\n\t"
    "v_pk_mul_f32 v[192:193], v[192:193], v[234:235] op_sel_hi:[1,0]\n\t"
    "v_pk_mul_f32 v[194:195], v[194:195], v[234:235] op_sel_hi:[1,0]\n\t"
    "v_cvt_pk_bf16_f32 v124, v188, v189\n\t"
    "v_cvt_pk_bf16_f32 v125, v190, v191\n\t"
    "v_cvt_pk_bf16_f32 v126, v192, v193\n\t"
    "v_cvt_pk_bf16_f32 v127, v194, v195\n\t"
    "buffer_store_dwordx4 v[124:127], v228, s[8:11], s30 offen offset:3072 sc1\n\t"
    "v_accvgpr_read_b32 v196, a40\n\t"
    "v_accvgpr_read_b32 v197, a41\n\t"
    "v_accvgpr_read_b32 v198, a42\n\t"
    "v_accvgpr_read_b32 v199, a43\n\t"
    "v_accvgpr_read_b32 v200, a44\n\t"
    "v_accvgpr_read_b32 v201, a45\n\t"
    "v_accvgpr_read_b32 v202, a46\n\t"
    "v_accvgpr_read_b32 v203, a47\n\t"
    "v_pk_mul_f32 v[196:197], v[196:197], v[234:235] op_sel_hi:[1,0]\n\t"
    "v_pk_mul_f32 v[198:199], v[198:199], v[234:235] op_sel_hi:[1,0]\n\t"
    "v_pk_mul_f32 v[200:201], v[200:201], v[234:235] op_sel_hi:[1,0]\n\t"
    "v_pk_mul_f32 v[202:203], v[202:203], v[234:235] op_sel_hi:[1,0]\n\t"
    "v_cvt_pk_bf16_f32 v140, v196, v197\n\t"
    "v_cvt_pk_bf16_f32 v141, v198, v199\n\t"
    "v_cvt_pk_bf16_f32 v142, v200, v201\n\t"
    "v_cvt_pk_bf16_f32 v143, v202, v203\n\t"
    "buffer_store_dwordx4 v[140:143], v229, s[8:11], s30 offen offset:3072 sc1\n\t"
    "s_waitcnt lgkmcnt(0)\n\t"
    "s_barrier\n\t"
    "ds_read_b128 v[168:171], v231 offset:6144\n\t"
    "ds_read_b128 v[172:175], v225 offset:0\n\t"
    "ds_read_b128 v[176:179], v225 offset:1024\n\t"
    "ds_read_b128 v[180:183], v225 offset:2048\n\t"
    "ds_read_b128 v[184:187], v225 offset:3072\n\t"
    "ds_read_b64 v[224:225], v225 offset:4096\n\t"
    "v_rcp_f32 v232, v238\n\t"
    "v_accvgpr_read_b32 v188, a80\n\t"
    "v_accvgpr_read_b32 v189, a81\n\t"
    "v_accvgpr_read_b32 v190, a82\n\t"
    "v_accvgpr_read_b32 v191, a83\n\t"
    "v_accvgpr_read_b32 v192, a84\n\t"
    "v_accvgpr_read_b32 v193, a85\n\t"
    "v_accvgpr_read_b32 v194, a86\n\t"
    "v_accvgpr_read_b32 v195, a87\n\t"
    "v_pk_mul_f32 v[188:189], v[188:189], v[232:233] op_sel_hi:[1,0]\n\t"
    "v_pk_mul_f32 v[190:191], v[190:191], v[232:233] op_sel_hi:[1,0]\n\t"
    "v_pk_mul_f32 v[192:193], v[192:193], v[232:233] op_sel_hi:[1,0]\n\t"
    "v_pk_mul_f32 v[194:195], v[194:195], v[232:233] op_sel_hi:[1,0]\n\t"
    "v_cvt_pk_bf16_f32 v88, v188, v189\n\t"
    "v_cvt_pk_bf16_f32 v89, v190, v191\n\t"
    "v_cvt_pk_bf16_f32 v90, v192, v193\n\t"
    "v_cvt_pk_bf16_f32 v91, v194, v195\n\t"
    "buffer_store_dwordx4 v[88:91], v226, s[8:11], s30 offen offset:2048 sc1\n\t"
    "v_accvgpr_read_b32 v196, a88\n\t"
    "v_accvgpr_read_b32 v197, a89\n\t"
    "v_accvgpr_read_b32 v198, a90\n\t"
    "v_accvgpr_read_b32 v199, a91\n\t"
    "v_accvgpr_read_b32 v200, a92\n\t"
    "v_accvgpr_read_b32 v201, a93\n\t"
    "v_accvgpr_read_b32 v202, a94\n\t"
    "v_accvgpr_read_b32 v203, a95\n\t"
    "v_pk_mul_f32 v[196:197], v[196:197], v[232:233] op_sel_hi:[1,0]\n\t"
    "v_pk_mul_f32 v[198:199], v[198:199], v[232:233] op_sel_hi:[1,0]\n\t"
    "v_pk_mul_f32 v[200:201], v[200:201], v[232:233] op_sel_hi:[1,0]\n\t"
    "v_pk_mul_f32 v[202:203], v[202:203], v[232:233] op_sel_hi:[1,0]\n\t"
    "v_cvt_pk_bf16_f32 v108, v196, v197\n\t"
    "v_cvt_pk_bf16_f32 v109, v198, v199\n\t"
    "v_cvt_pk_bf16_f32 v110, v200, v201\n\t"
    "v_cvt_pk_bf16_f32 v111, v202, v203\n\t"
    "buffer_store_dwordx4 v[108:111], v227, s[8:11], s30 offen offset:3072 sc1\n\t"
    "v_accvgpr_read_b32 v188, a64\n\t"
    "v_accvgpr_read_b32 v189, a65\n\t"
    "v_accvgpr_read_b32 v190, a66\n\t"
    "v_accvgpr_read_b32 v191, a67\n\t"
    "v_accvgpr_read_b32 v192, a68\n\t"
    "v_accvgpr_read_b32 v193, a69\n\t"
    "v_accvgpr_read_b32 v194, a70\n\t"
    "v_accvgpr_read_b32 v195, a71\n\t"
    "v_pk_mul_f32 v[188:189], v[188:189], v[232:233] op_sel_hi:[1,0]\n\t"
    "v_pk_mul_f32 v[190:191], v[190:191], v[232:233] op_sel_hi:[1,0]\n\t"
    "v_pk_mul_f32 v[192:193], v[192:193], v[232:233] op_sel_hi:[1,0]\n\t"
    "v_pk_mul_f32 v[194:195], v[194:195], v[232:233] op_sel_hi:[1,0]\n\t"
    "v_cvt_pk_bf16_f32 v128, v188, v189\n\t"
    "v_cvt_pk_bf16_f32 v129, v190, v191\n\t"
    "v_cvt_pk_bf16_f32 v130, v192, v193\n\t"
    "v_cvt_pk_bf16_f32 v131, v194, v195\n\t"
    "buffer_store_dwordx4 v[128:131], v229, s[8:11], s30 offen offset:0 sc1\n\t"
    "v_accvgpr_read_b32 v196, a72\n\t"
    "v_accvgpr_read_b32 v197, a73\n\t"
    "v_accvgpr_read_b32 v198, a74\n\t"
    "v_accvgpr_read_b32 v199, a75\n\t"
    "v_accvgpr_read_b32 v200, a76\n\t"
    "v_accvgpr_read_b32 v201, a77\n\t"
    "v_accvgpr_read_b32 v202, a78\n\t"
    "v_accvgpr_read_b32 v203, a79\n\t"
    "v_pk_mul_f32 v[196:197], v[196:197], v[232:233] op_sel_hi:[1,0]\n\t"
    "v_pk_mul_f32 v[198:199], v[198:199], v[232:233] op_sel_hi:[1,0]\n\t"
    "v_pk_mul_f32 v[200:201], v[200:201], v[232:233] op_sel_hi:[1,0]\n\t"
    "v_pk_mul_f32 v[202:203], v[202:203], v[232:233] op_sel_hi:[1,0]\n\t"
    "v_cvt_pk_bf16_f32 v144, v196, v197\n\t"
    "v_cvt_pk_bf16_f32 v145, v198, v199\n\t"
    "v_cvt_pk_bf16_f32 v146, v200, v201\n\t"
    "v_cvt_pk_bf16_f32 v147, v202, v203\n\t"
    "buffer_store_dwordx4 v[144:147], v230, s[8:11], s30 offen offset:0 sc1\n\t"
    "s_waitcnt lgkmcnt(0)\n\t"
    "v_or3_b32 v168, v168, v169, v170\n\t"
    "v_or_b32 v168, v168, v171\n\t"
    "v_min_f32 v231, v204, v224\n\t"
    "v_readfirstlane_b32 s54, v168\n\t"
    "v_sub_f32 v170, v231, v204\n\t"
    "v_sub_f32 v206, v231, v224\n\t"
    "v_exp_f32 v170, v170\n\t"
    "v_exp_f32 v206, v206\n\t"
    "s_andn2_b32 s54, s54, s63\n\t"
    "s_cbranch_scc1 REDO_%=\n\t"
    "v_accvgpr_read_b32 v0, a144\n\t"
    "v_accvgpr_read_b32 v1, a145\n\t"
    "v_accvgpr_read_b32 v2, a146\n\t"
    "v_accvgpr_read_b32 v3, a147\n\t"
    "v_accvgpr_read_b32 v4, a148\n\t"
    "v_accvgpr_read_b32 v5, a149\n\t"
    "v_accvgpr_read_b32 v6, a150\n\t"
    "v_accvgpr_read_b32 v7, a151\n\t"
    "v_mul_f32 v233, v170, v205\n\t"
    "v_fma_f32 v233, v206, v225, v233\n\t"
    "v_rcp_f32 v233, v233\n\t"
    "v_accvgpr_read_b32 v8, a152\n\t"
    "v_accvgpr_read_b32 v9, a153\n\t"
    "v_accvgpr_read_b32 v10, a154\n\t"
    "v_accvgpr_read_b32 v11, a155\n\t"
    "v_accvgpr_read_b32 v12, a156\n\t"
    "v_accvgpr_read_b32 v13, a157\n\t"
    "v_accvgpr_read_b32 v14, a158\n\t"
    "v_accvgpr_read_b32 v15, a159\n\t"
    "v_mul_f32 v170, v170, v233\n\t"
    "v_mul_f32 v206, v206, v233\n\t"
    "v_pk_mul_f32 v[0:1], v[0:1], v[170:171] op_sel_hi:[1,0]\n\t"
    "v_pk_mul_f32 v[2:3], v[2:3], v[170:171] op_sel_hi:[1,0]\n\t"
    "v_pk_mul_f32 v[4:5], v[4:5], v[170:171] op_sel_hi:[1,0]\n\t"
    "v_pk_mul_f32 v[6:7], v[6:7], v[170:171] op_sel_hi:[1,0]\n\t"
    "v_pk_fma_f32 v[0:1], v[172:173], v[206:207], v[0:1] op_sel_hi:[1,0,1]\n\t"
    "v_pk_fma_f32 v[2:3], v[174:175], v[206:207], v[2:3] op_sel_hi:[1,0,1]\n\t"
    "v_pk_fma_f32 v[4:5], v[176:177], v[206:207], v[4:5] op_sel_hi:[1,0,1]\n\t"
    "v_pk_fma_f32 v[6:7], v[178:179], v[206:207], v[6:7] op_sel_hi:[1,0,1]\n\t"
    "v_cvt_pk_bf16_f32 v96, v0, v1\n\t"
    "v_cvt_pk_bf16_f32 v97, v2, v3\n\t"
    "v_cvt_pk_bf16_f32 v98, v4, v5\n\t"
    "v_cvt_pk_bf16_f32 v99, v6, v7\n\t"
    "buffer_store_dwordx4 v[96:99], v227, s[8:11], s30 offen offset:0 sc1\n\t"
    "v_pk_mul_f32 v[8:9], v[8:9], v[170:171] op_sel_hi:[1,0]\n\t"
    "v_pk_mul_f32 v[10:11], v[10:11], v[170:171] op_sel_hi:[1,0]\n\t"
    "v_pk_mul_f32 v[12:13], v[12:13], v[170:171] op_sel_hi:[1,0]\n\t"
    "v_pk_mul_f32 v[14:15], v[14:15], v[170:171] op_sel_hi:[1,0]\n\t"
    "v_pk_fma_f32 v[8:9], v[180:181], v[206:207], v[8:9] op_sel_hi:[1,0,1]\n\t"
    "v_pk_fma_f32 v[10:11], v[182:183], v[206:207], v[10:11] op_sel_hi:[1,0,1]\n\t"
    "v_pk_fma_f32 v[12:13], v[184:185], v[206:207], v[12:13] op_sel_hi:[1,0,1]\n\t"
    "v_pk_fma_f32 v[14:15], v[186:187], v[206:207], v[14:15] op_sel_hi:[1,0,1]\n\t"
    "v_cvt_pk_bf16_f32 v116, v8, v9\n\t"
    "v_cvt_pk_bf16_f32 v117, v10, v11\n\t"
    "v_cvt_pk_bf16_f32 v118, v12, v13\n\t"
    "v_cvt_pk_bf16_f32 v119, v14, v15\n\t"
    "buffer_store_dwordx4 v[116:119], v228, s[8:11], s30 offen offset:1024 sc1\n\t"
    "v_lshrrev_b32 v16, 2, v222\n\t"
    "v_and_b32 v17, 3, v222\n\t"
    "v_lshlrev_b32 v16, 6, v16\n\t"
    "v_lshl_add_u32 v16, v17, 3, v16\n\t"
    "v_and_b32 v17, 16, v222\n\t"
    "s_mul_i32 s52, s51, 8224\n\t"
    "v_lshl_add_u32 v16, v17, 1, v16\n\t"
    "s_lshl_b32 s53, s51, 12\n\t"
    "v_xor_b32 v16, s52, v16\n\t"
    "v_lshl_add_u32 v17, v222, 3, s53\n\t"
    "v_xor_b32 v208, 4096, v16\n\t"
    "v_xor_b32 v209, 0, v16\n\t"
    "v_xor_b32 v210, 69632, v16\n\t"
    "v_xor_b32 v211, 65536, v16\n\t"
    "v_xor_b32 v212, 135168, v16\n\t"
    "v_xor_b32 v213, 131072, v16\n\t"
    "v_xor_b32 v214, 4128, v16\n\t"
    "v_xor_b32 v215, 32, v16\n\t"
    "v_xor_b32 v216, 69664, v16\n\t"
    "v_xor_b32 v217, 65568, v16\n\t"
    "v_xor_b32 v218, 135200, v16\n\t"
    "v_xor_b32 v219, 131104, v16\n\t"
    "v_add_u32 v220, 34816, v17\n\t"
    "v_add_u32 v221, 32768, v17\n\t"
    "v_rcp_f32 v234, v239\n\t"
    "v_accvgpr_read_b32 v188, a112\n\t"
    "v_accvgpr_read_b32 v189, a113\n\t"
    "v_accvgpr_read_b32 v190, a114\n\t"
    "v_accvgpr_read_b32 v191, a115\n\t"
    "v_accvgpr_read_b32 v192, a116\n\t"
    "v_accvgpr_read_b32 v193, a117\n\t"
    "v_accvgpr_read_b32 v194, a118\n\t"
    "v_accvgpr_read_b32 v195, a119\n\t"
    "v_pk_mul_f32 v[188:189], v[188:189], v[234:235] op_sel_hi:[1,0]\n\t"
    "v_pk_mul_f32 v[190:191], v[190:191], v[234:235] op_sel_hi:[1,0]\n\t"
    "v_pk_mul_f32 v[192:193], v[192:193], v[234:235] op_sel_hi:[1,0]\n\t"
    "v_pk_mul_f32 v[194:195], v[194:195], v[234:235] op_sel_hi:[1,0]\n\t"
    "v_cvt_pk_bf16_f32 v92, v188, v189\n\t"
    "v_cvt_pk_bf16_f32 v93, v190, v191\n\t"
    "v_cvt_pk_bf16_f32 v94, v192, v193\n\t"
    "v_cvt_pk_bf16_f32 v95, v194, v195\n\t"
    "buffer_store_dwordx4 v[92:95], v226, s[8:11], s30 offen offset:3072 sc1\n\t"
    "v_accvgpr_read_b32 v196, a120\n\t"
    "v_accvgpr_read_b32 v197, a121\n\t"
    "v_accvgpr_read_b32 v198, a122\n\t"
    "v_accvgpr_read_b32 v199, a123\n\t"
    "v_accvgpr_read_b32 v200, a124\n\t"
    "v_accvgpr_read_b32 v201, a125\n\t"
    "v_accvgpr_read_b32 v202, a126\n\t"
    "v_accvgpr_read_b32 v203, a127\n\t"
    "v_pk_mul_f32 v[196:197], v[196:197], v[234:235] op_sel_hi:[1,0]\n\t"
    "v_pk_mul_f32 v[198:199], v[198:199], v[234:235] op_sel_hi:[1,0]\n\t"
    "v_pk_mul_f32 v[200:201], v[200:201], v[234:235] op_sel_hi:[1,0]\n\t"
    "v_pk_mul_f32 v[202:203], v[202:203], v[234:235] op_sel_hi:[1,0]\n\t"
    "v_cvt_pk_bf16_f32 v112, v196, v197\n\t"
    "v_cvt_pk_bf16_f32 v113, v198, v199\n\t"
    "v_cvt_pk_bf16_f32 v114, v200, v201\n\t"
    "v_cvt_pk_bf16_f32 v115, v202, v203\n\t"
    "buffer_store_dwordx4 v[112:115], v228, s[8:11], s30 offen offset:0 sc1\n\t"
    "v_accvgpr_read_b32 v188, a96\n\t"
    "v_accvgpr_read_b32 v189, a97\n\t"
    "v_accvgpr_read_b32 v190, a98\n\t"
    "v_accvgpr_read_b32 v191, a99\n\t"
    "v_accvgpr_read_b32 v192, a100\n\t"
    "v_accvgpr_read_b32 v193, a101\n\t"
    "v_accvgpr_read_b32 v194, a102\n\t"
    "v_accvgpr_read_b32 v195, a103\n\t"
    "v_pk_mul_f32 v[188:189], v[188:189], v[234:235] op_sel_hi:[1,0]\n\t"
    "v_pk_mul_f32 v[190:191], v[190:191], v[234:235] op_sel_hi:[1,0]\n\t"
    "v_pk_mul_f32 v[192:193], v[192:193], v[234:235] op_sel_hi:[1,0]\n\t"
    "v_pk_mul_f32 v[194:195], v[194:195], v[234:235] op_sel_hi:[1,0]\n\t"
    "v_cvt_pk_bf16_f32 v132, v188, v189\n\t"
    "v_cvt_pk_bf16_f32 v133, v190, v191\n\t"
    "v_cvt_pk_bf16_f32 v134, v192, v193\n\t"
    "v_cvt_pk_bf16_f32 v135, v194, v195\n\t"
    "buffer_store_dwordx4 v[132:135], v229, s[8:11], s30 offen offset:1024 sc1\n\t"
    "v_accvgpr_read_b32 v196, a104\n\t"
    "v_accvgpr_read_b32 v197, a105\n\t"
    "v_accvgpr_read_b32 v198, a106\n\t"
    "v_accvgpr_read_b32 v199, a107\n\t"
    "v_accvgpr_read_b32 v200, a108\n\t"
    "v_accvgpr_read_b32 v201, a109\n\t"
    "v_accvgpr_read_b32 v202, a110\n\t"
    "v_accvgpr_read_b32 v203, a111\n\t"
    "v_pk_mul_f32 v[196:197], v[196:197], v[234:235] op_sel_hi:[1,0]\n\t"
    "v_pk_mul_f32 v[198:199], v[198:199], v[234:235] op_sel_hi:[1,0]\n\t"
    "v_pk_mul_f32 v[200:201], v[200:201], v[234:235] op_sel_hi:[1,0]\n\t"
    "v_pk_mul_f32 v[202:203], v[202:203], v[234:235] op_sel_hi:[1,0]\n\t"
    "v_cvt_pk_bf16_f32 v148, v196, v197\n\t"
    "v_cvt_pk_bf16_f32 v149, v198, v199\n\t"
    "v_cvt_pk_bf16_f32 v150, v200, v201\n\t"
    "v_cvt_pk_bf16_f32 v151, v202, v203\n\t"
    "buffer_store_dwordx4 v[148:151], v230, s[8:11], s30 offen offset:1024 sc1\n\t"
    "s_waitcnt vmcnt(19) lgkmcnt(0)\n\t"
    "s_barrier\n\t"
    "ds_read_b64_tr_b16 v[152:153], v210 offset:16384\n\t"
    "ds_read_b64_tr_b16 v[154:155], v210 offset:17408\n\t"
    "ds_read_b64_tr_b16 v[156:157], v216 offset:16384\n\t"
    "ds_read_b64_tr_b16 v[158:159], v216 offset:17408\n\t"
    "ds_read_b64_tr_b16 v[160:161], v208 offset:32768\n\t"
    "ds_read_b64_tr_b16 v[162:163], v208 offset:33792\n\t"
    "ds_read_b64_tr_b16 v[164:165], v214 offset:32768\n\t"
    "ds_read_b64_tr_b16 v[166:167], v214 offset:33792\n\t"
    "P3_%=:\n\t"
    "s_waitcnt lgkmcnt(6)\n\t"
    "v_mfma_f32_16x16x32_bf16 v[16:19], v[152:155], v[80:83], 0\n\t"
    "ds_read_b64_tr_b16 v[168:169], v208 offset:49152\n\t"
    "ds_read_b64_tr_b16 v[170:171], v208 offset:50176\n\t"
    "v_mfma_f32_16x16x32_bf16 v[20:23], v[152:155], v[84:87], 0\n\t"
    "ds_read_b64_tr_b16 v[172:173], v214 offset:49152\n\t"
    "ds_read_b64_tr_b16 v[174:175], v214 offset:50176\n\t"
    "v_mfma_f32_16x16x32_bf16 v[24:27], v[152:155], v[88:91], 0\n\t"
    "s_xor_b32 s67, %1, 2\n\t"
    "s_mul_i32 s67, s67, 36864\n\t"
    "v_mfma_f32_16x16x32_bf16 v[28:31], v[152:155], v[92:95], 0\n\t"
    "s_xor_b32 s68, %1, 4\n\t"
    "s_mul_i32 s68, s68, 36864\n\t"
    "v_mfma_f32_16x16x32_bf16 v[68:71], v[152:155], v[96:99], 0\n\t"
    "ds_read_b64_tr_b16 v[152:153], v210 offset:32768\n\t"
    "ds_read_b64_tr_b16 v[154:155], v210 offset:33792\n\t"
    "s_waitcnt lgkmcnt(10)\n\t"
    "v_mfma_f32_16x16x32_bf16 a[196:199], v[156:159], v[80:83], 0\n\t"
    "s_xor_b32 s69, %1, 6\n\t"
    "s_mul_i32 s69, s69, 36864\n\t"
    "v_mfma_f32_16x16x32_bf16 a[200:203], v[156:159], v[84:87], 0\n\t"
    "s_mul_i32 s52, s62, 3072\n\t"
    "s_mul_i32 s40, %1, 36864\n\t"
    "v_mfma_f32_16x16x32_bf16 a[204:207], v[156:159], v[88:91], 0\n\t"
    "s_add_u32 s52, s52, 33554432\n\t"
    "s_add_u32 s40, s40, s52\n\t"
    "v_mfma_f32_16x16x32_bf16 a[208:211], v[156:159], v[92:95], 0\n\t"
    "s_add_u32 m0, s70, 114688\n\t"
    "s_xor_b32 s66, s27, 256\n\t"
    "v_mfma_f32_16x16x32_bf16 a[148:151], v[156:159], v[96:99], 0\n\t"
    "ds_read_b64_tr_b16 v[156:157], v216 offset:32768\n\t"
    "ds_read_b64_tr_b16 v[158:159], v216 offset:33792\n\t"
    "s_waitcnt lgkmcnt(4)\n\t"
    "v_mfma_f32_16x16x32_bf16 v[32:35], v[160:163], v[80:83], 0\n\t"
    "s_add_u32 s67, s67, s52\n\t"
    "s_add_u32 s52, s52, 12288\n\t"
    "v_mfma_f32_16x16x32_bf16 v[36:39], v[160:163], v[84:87], 0\n\t"
    "s_add_u32 s68, s68, s52\n\t"
    "s_add_u32 s52, s52, 12288\n\t"
    "v_mfma_f32_16x16x32_bf16 v[40:43], v[160:163], v[88:91], 0\n\t"
    "s_add_u32 s69, s69, s52\n\t"
    "s_add_u32 s52, s52, 12288\n\t"
    "v_mfma_f32_16x16x32_bf16 v[44:47], v[160:163], v[92:95], 0\n\t"
    "buffer_load_dwordx4 v247, s[4:7], s66 offen offset:0 lds\n\t"
    "v_mfma_f32_16x16x32_bf16 v[72:75], v[160:163], v[96:99], 0\n\t"
    "ds_read_b64_tr_b16 v[160:161], v220 offset:32768\n\t"
    "ds_read_b64_tr_b16 v[162:163], v220 offset:33280\n\t"
    "v_mfma_f32_16x16x32_bf16 a[212:215], v[164:167], v[80:83], 0\n\t"
    "s_mov_b32 s45, 0\n\t"
    "s_or_b32 s43, s62, s17\n\t"
    "v_mfma_f32_16x16x32_bf16 a[216:219], v[164:167], v[84:87], 0\n\t"
    "s_lshl_b32 s52, s62, 10\n\t"
    "v_add_u32 v224, s52, v226\n\t"
    "v_mfma_f32_16x16x32_bf16 a[220:223], v[164:167], v[88:91], 0\n\t"
    "buffer_load_dwordx4 v248, s[4:7], s66 offen offset:1024 lds\n\t"
    "v_mfma_f32_16x16x32_bf16 a[224:227], v[164:167], v[92:95], 0\n\t"
    "v_xor_b32 v225, 0x800, v224\n\t"
    "s_lshl_b32 s52, s51, 12\n\t"
    "v_mfma_f32_16x16x32_bf16 a[152:155], v[164:167], v[96:99], 0\n\t"
    "ds_read_b64_tr_b16 v[164:165], v210 offset:18432\n\t"
    "ds_read_b64_tr_b16 v[166:167], v210 offset:19456\n\t"
    "v_mfma_f32_16x16x32_bf16 v[48:51], v[168:171], v[80:83], 0\n\t"
    "v_add_u32 v231, s52, v224\n\t"
    "v_xor_b32 v244, 0x1000, v231\n\t"
    "v_mfma_f32_16x16x32_bf16 v[52:55], v[168:171], v[84:87], 0\n\t"
    "buffer_load_dwordx4 v249, s[4:7], s66 offen offset:2048 lds\n\t"
    "v_mfma_f32_16x16x32_bf16 v[56:59], v[168:171], v[88:91], 0\n\t"
    "s_lshl1_add_u32 s53, s50, s51\n\t"
    "s_lshl_b32 s53, s53, 10\n\t"
    "v_mfma_f32_16x16x32_bf16 v[60:63], v[168:171], v[92:95], 0\n\t"
    "s_add_u32 s53, s53, 98304\n\t"
    "v_add_u32 v245, s53, v226\n\t"
    "v_mfma_f32_16x16x32_bf16 v[76:79], v[168:171], v[96:99], 0\n\t"
    "ds_read_b64_tr_b16 v[168:169], v216 offset:18432\n\t"
    "ds_read_b64_tr_b16 v[170:171], v216 offset:19456\n\t"
    "v_mfma_f32_16x16x32_bf16 a[228:231], v[172:175], v[80:83], 0\n\t"
    "buffer_load_dwordx4 v250, s[4:7], s66 offen offset:3072 lds\n\t"
    "v_mfma_f32_16x16x32_bf16 a[232:235], v[172:175], v[84:87], 0\n\t"
    "s_add_u32 m0, s70, 131072\n\t"
    "s_xor_b32 s66, s27, 512\n\t"
    "v_mfma_f32_16x16x32_bf16 a[236:239], v[172:175], v[88:91], 0\n\t"
    "s_lshl_b32 s53, s17, 4\n\t"
    "s_lshl_b32 s55, s51, 11\n\t"
    "v_mfma_f32_16x16x32_bf16 a[240:243], v[172:175], v[92:95], 0\n\t"
    "s_add_u32 s53, s53, s55\n\t"
    "s_add_u32 s53, s53, 98304\n\t"
    "v_mfma_f32_16x16x32_bf16 a[156:159], v[172:175], v[96:99], 0\n\t"
    "buffer_load_dwordx4 v247, s[4:7], s66 offen offset:0 lds\n\t"
    "s_waitcnt lgkmcnt(2)\n\t"
    "v_mfma_f32_16x16x32_bf16 v[0:3], v[152:155], v[80:83], 0\n\t"
    "ds_read_b64_tr_b16 v[172:173], v208 offset:34816\n\t"
    "ds_read_b64_tr_b16 v[174:175], v208 offset:35840\n\t"
    "v_mfma_f32_16x16x32_bf16 v[4:7], v[152:155], v[84:87], 0\n\t"
    "s_add_u32 s52, s52, 16384\n\t"
    "s_xor_b32 s88, s53, 0\n\t"
    "v_mfma_f32_16x16x32_bf16 v[8:11], v[152:155], v[88:91], 0\n\t"
    "s_add_u32 s55, s52, 32768\n\t"
    "v_mfma_f32_16x16x32_bf16 v[12:15], v[152:155], v[92:95], 0\n\t"
    "buffer_load_dwordx4 v248, s[4:7], s66 offen offset:1024 lds\n\t"
    "v_mfma_f32_16x16x32_bf16 v[64:67], v[152:155], v[96:99], 0\n\t"
    "ds_read_b64_tr_b16 v[152:153], v214 offset:34816\n\t"
    "ds_read_b64_tr_b16 v[154:155], v214 offset:35840\n\t"
    "v_mfma_f32_16x16x32_bf16 a[180:183], v[156:159], v[80:83], 0\n\t"
    "s_cmp_eq_u32 s50, 0\n\t"
    "s_cselect_b32 s88, s55, s88\n\t"
    "v_mfma_f32_16x16x32_bf16 a[184:187], v[156:159], v[84:87], 0\n\t"
    "s_xor_b32 s89, s53, 4096\n\t"
    "s_add_u32 s55, s52, 0\n\t"
    "v_mfma_f32_16x16x32_bf16 a[188:191], v[156:159], v[88:91], 0\n\t"
    "s_cmp_eq_u32 s50, 0\n\t"
    "s_cselect_b32 s89, s55, s89\n\t"
    "v_mfma_f32_16x16x32_bf16 a[192:195], v[156:159], v[92:95], 0\n\t"
    "buffer_load_dwordx4 v249, s[4:7], s66 offen offset:2048 lds\n\t"
    "v_mfma_f32_16x16x32_bf16 a[144:147], v[156:159], v[96:99], 0\n\t"
    "ds_read_b64_tr_b16 v[156:157], v208 offset:51200\n\t"
    "ds_read_b64_tr_b16 v[158:159], v208 offset:52224\n\t"
    "v_mfma_f32_16x16x32_bf16 a[160:163], v[160:163], v[80:83], 0\n\t"
    "s_waitcnt vmcnt(7)\n\t"
    "s_mov_b32 exec_lo, 0x8040200\n\t"
    "s_mov_b32 exec_hi, 0\n\t"
    "global_store_dword v253, v254, s[60:61] sc1\n\t"
    "v_mov_b32 v223, v254\n\t"
    "s_mov_b64 exec, -1\n\t"
    "v_mfma_f32_16x16x32_bf16 a[164:167], v[160:163], v[84:87], 0\n\t"
    "s_xor_b32 s90, s53, 8192\n\t"
    "s_add_u32 s55, s52, 32768\n\t"
    "v_mfma_f32_16x16x32_bf16 a[168:171], v[160:163], v[88:91], 0\n\t"
    "buffer_load_dwordx4 v250, s[4:7], s66 offen offset:3072 lds\n\t"
    "v_mfma_f32_16x16x32_bf16 a[172:175], v[160:163], v[92:95], 0\n\t"
    "s_add_u32 m0, s70, 147456\n\t"
    "s_xor_b32 s66, s27, 768\n\t"
    "v_mfma_f32_16x16x32_bf16 a[176:179], v[160:163], v[96:99], 0\n\t"
    "ds_read_b64_tr_b16 v[160:161], v214 offset:51200\n\t"
    "ds_read_b64_tr_b16 v[162:163], v214 offset:52224\n\t"
    "v_mfma_f32_16x16x32_bf16 v[16:19], v[164:167], v[100:103], v[16:19]\n\t"
    "s_cmp_eq_u32 s50, 0\n\t"
    "s_cselect_b32 s90, s55, s90\n\t"
    "v_mfma_f32_16x16x32_bf16 v[20:23], v[164:167], v[104:107], v[20:23]\n\t"
    "buffer_load_dwordx4 v247, s[4:7], s66 offen offset:0 lds\n\t"
    "v_mfma_f32_16x16x32_bf16 v[24:27], v[164:167], v[108:111], v[24:27]\n\t"
    "s_xor_b32 s91, s53, 12288\n\t"
    "s_add_u32 s55, s52, 0\n\t"
    "v_mfma_f32_16x16x32_bf16 v[28:31], v[164:167], v[112:115], v[28:31]\n\t"
    "s_cmp_eq_u32 s50, 0\n\t"
    "s_cselect_b32 s91, s55, s91\n\t"
    "v_mfma_f32_16x16x32_bf16 v[68:71], v[164:167], v[116:119], v[68:71]\n\t"
    "ds_read_b64_tr_b16 v[164:165], v210 offset:34816\n\t"
    "ds_read_b64_tr_b16 v[166:167], v210 offset:35840\n\t"
    "s_waitcnt lgkmcnt(4)\n\t"
    "v_mfma_f32_16x16x32_bf16 a[196:199], v[168:171], v[100:103], a[196:199]\n\t"
    "buffer_load_dwordx4 v248, s[4:7], s66 offen offset:1024 lds\n\t"
    "v_mfma_f32_16x16x32_bf16 a[200:203], v[168:171], v[104:107], a[200:203]\n\t"
    "s_cmp_eq_u32 s50, 0\n\t"
    "s_cselect_b32 s41, 1.0, 0\n\t"
    "v_mfma_f32_16x16x32_bf16 a[204:207], v[168:171], v[108:111], a[204:207]\n\t"
    "s_cmp_eq_u32 s50, 0\n\t"
    "s_cselect_b32 s42, 0x800, 0\n\t"
    "v_mfma_f32_16x16x32_bf16 a[208:211], v[168:171], v[112:115], a[208:211]\n\t"
    "s_lshl1_add_u32 s52, s19, s51\n\t"
    "s_lshl_b32 s52, s52, 5\n\t"
    "v_mfma_f32_16x16x32_bf16 a[148:151], v[168:171], v[116:119], a[148:151]\n\t"
    "buffer_load_dwordx4 v249, s[4:7], s66 offen offset:2048 lds\n\t"
    "v_mfma_f32_16x16x32_bf16 v[32:35], v[172:175], v[100:103], v[32:35]\n\t"
    "ds_read_b64_tr_b16 v[168:169], v216 offset:34816\n\t"
    "ds_read_b64_tr_b16 v[170:171], v216 offset:35840\n\t"
    "v_mfma_f32_16x16x32_bf16 v[36:39], v[172:175], v[104:107], v[36:39]\n\t"
    "s_add_u32 s52, s52, s17\n\t"
    "s_mul_i32 s92, s50, 147456\n\t"
    "v_mfma_f32_16x16x32_bf16 v[40:43], v[172:175], v[108:111], v[40:43]\n\t"
    "s_add_u32 s92, s92, s52\n\t"
    "s_add_u32 s93, s92, 36864\n\t"
    "v_mfma_f32_16x16x32_bf16 v[44:47], v[172:175], v[112:115], v[44:47]\n\t"
    "buffer_load_dwordx4 v250, s[4:7], s66 offen offset:3072 lds\n\t"
    "v_mfma_f32_16x16x32_bf16 v[72:75], v[172:175], v[116:119], v[72:75]\n\t"
    "s_add_u32 m0, s70, 0\n\t"
    "s_xor_b32 s66, s28, 256\n\t"
    "v_mfma_f32_16x16x32_bf16 a[212:215], v[152:155], v[100:103], a[212:215]\n\t"
    "ds_read_b64_tr_b16 v[172:173], v220 offset:33792\n\t"
    "ds_read_b64_tr_b16 v[174:175], v220 offset:34304\n\t"
    "v_mfma_f32_16x16x32_bf16 a[216:219], v[152:155], v[104:107], a[216:219]\n\t"
    "s_add_u32 s94, s92, 73728\n\t"
    "s_add_u32 s95, s92, 110592\n\t"
    "v_mfma_f32_16x16x32_bf16 a[220:223], v[152:155], v[108:111], a[220:223]\n\t"
    "s_lshl_b32 s53, s19, 5\n\t"
    "s_add_u32 s97, s53, 295936\n\t"
    "v_mfma_f32_16x16x32_bf16 a[224:227], v[152:155], v[112:115], a[224:227]\n\t"
    "buffer_load_dwordx4 v247, s[4:7], s66 offen offset:0 lds\n\t"
    "v_mfma_f32_16x16x32_bf16 a[152:155], v[152:155], v[116:119], a[152:155]\n\t"
    "ds_read_b64_tr_b16 v[152:153], v211 offset:16384\n\t"
    "ds_read_b64_tr_b16 v[154:155], v211 offset:17408\n\t"
    "v_mfma_f32_16x16x32_bf16 v[48:51], v[156:159], v[100:103], v[48:51]\n\t"
    "s_lshl_b32 s55, s51, 10\n\t"
    "s_add_u32 s55, s55, s17\n\t"
    "v_mfma_f32_16x16x32_bf16 v[52:55], v[156:159], v[104:107], v[52:55]\n\t"
    "s_mul_i32 s55, s55, 144\n\t"
    "s_add_u32 s55, s55, s53\n\t"
    "v_mfma_f32_16x16x32_bf16 v[56:59], v[156:159], v[108:111], v[56:59]\n\t"
    "s_add_u32 s55, s55, 1024\n\t"
    "s_add_u32 s52, s52, 294912\n\t"
    "v_mfma_f32_16x16x32_bf16 v[60:63], v[156:159], v[112:115], v[60:63]\n\t"
    "buffer_load_dwordx4 v248, s[4:7], s66 offen offset:1024 lds\n\t"
    "v_mfma_f32_16x16x32_bf16 v[76:79], v[156:159], v[116:119], v[76:79]\n\t"
    "ds_read_b64_tr_b16 v[156:157], v217 offset:16384\n\t"
    "ds_read_b64_tr_b16 v[158:159], v217 offset:17408\n\t"
    "s_waitcnt lgkmcnt(4)\n\t"
    "v_mfma_f32_16x16x32_bf16 a[228:231], v[160:163], v[100:103], a[228:231]\n\t"
    "s_cmp_eq_u32 s50, 0\n\t"
    "s_cselect_b32 s96, s52, s55\n\t"
    "v_mfma_f32_16x16x32_bf16 a[232:235], v[160:163], v[104:107], a[232:235]\n\t"
    "v_mfma_f32_16x16x32_bf16 a[236:239], v[160:163], v[108:111], a[236:239]\n\t"
    "v_mfma_f32_16x16x32_bf16 a[240:243], v[160:163], v[112:115], a[240:243]\n\t"
    "buffer_load_dwordx4 v249, s[4:7], s66 offen offset:2048 lds\n\t"
    "v_mfma_f32_16x16x32_bf16 a[156:159], v[160:163], v[116:119], a[156:159]\n\t"
    "ds_read_b64_tr_b16 v[160:161], v209 offset:32768\n\t"
    "ds_read_b64_tr_b16 v[162:163], v209 offset:33792\n\t"
    "v_mfma_f32_16x16x32_bf16 v[0:3], v[164:167], v[100:103], v[0:3]\n\t"
    "v_mfma_f32_16x16x32_bf16 v[4:7], v[164:167], v[104:107], v[4:7]\n\t"
    "v_mfma_f32_16x16x32_bf16 v[8:11], v[164:167], v[108:111], v[8:11]\n\t"
    "v_mfma_f32_16x16x32_bf16 v[12:15], v[164:167], v[112:115], v[12:15]\n\t"
    "buffer_load_dwordx4 v250, s[4:7], s66 offen offset:3072 lds\n\t"
    "v_mfma_f32_16x16x32_bf16 v[64:67], v[164:167], v[116:119], v[64:67]\n\t"
    "s_add_u32 m0, s70, 16384\n\t"
    "s_xor_b32 s66, s28, 512\n\t"
    "v_mfma_f32_16x16x32_bf16 a[180:183], v[168:171], v[100:103], a[180:183]\n\t"
    "ds_read_b64_tr_b16 v[164:165], v215 offset:32768\n\t"
    "ds_read_b64_tr_b16 v[166:167], v215 offset:33792\n\t"
    "v_mfma_f32_16x16x32_bf16 a[184:187], v[168:171], v[104:107], a[184:187]\n\t"
    "v_mfma_f32_16x16x32_bf16 a[188:191], v[168:171], v[108:111], a[188:191]\n\t"
    "v_mfma_f32_16x16x32_bf16 a[192:195], v[168:171], v[112:115], a[192:195]\n\t"
    "buffer_load_dwordx4 v247, s[4:7], s66 offen offset:0 lds\n\t"
    "v_mfma_f32_16x16x32_bf16 a[144:147], v[168:171], v[116:119], a[144:147]\n\t"
    "ds_read_b64_tr_b16 v[168:169], v209 offset:49152\n\t"
    "ds_read_b64_tr_b16 v[170:171], v209 offset:50176\n\t"
    "v_mfma_f32_16x16x32_bf16 a[160:163], v[172:175], v[100:103], a[160:163]\n\t"
    "v_mfma_f32_16x16x32_bf16 a[164:167], v[172:175], v[104:107], a[164:167]\n\t"
    "v_mfma_f32_16x16x32_bf16 a[168:171], v[172:175], v[108:111], a[168:171]\n\t"
    "v_mfma_f32_16x16x32_bf16 a[172:175], v[172:175], v[112:115], a[172:175]\n\t"
    "buffer_load_dwordx4 v248, s[4:7], s66 offen offset:1024 lds\n\t"
    "v_mfma_f32_16x16x32_bf16 a[176:179], v[172:175], v[116:119], a[176:179]\n\t"
    "ds_read_b64_tr_b16 v[172:173], v215 offset:49152\n\t"
    "ds_read_b64_tr_b16 v[174:175], v215 offset:50176\n\t"
    "s_waitcnt lgkmcnt(4)\n\t"
    "v_mfma_f32_16x16x32_bf16 v[16:19], v[152:155], v[120:123], v[16:19]\n\t"
    "v_mfma_f32_16x16x32_bf16 v[20:23], v[152:155], v[124:127], v[20:23]\n\t"
    "v_mfma_f32_16x16x32_bf16 v[24:27], v[152:155], v[128:131], v[24:27]\n\t"
    "v_mfma_f32_16x16x32_bf16 v[28:31], v[152:155], v[132:135], v[28:31]\n\t"
    "buffer_load_dwordx4 v249, s[4:7], s66 offen offset:2048 lds\n\t"
    "v_mfma_f32_16x16x32_bf16 a[196:199], v[156:159], v[120:123], a[196:199]\n\t"
    "ds_read_b64_tr_b16 v[152:153], v211 offset:32768\n\t"
    "ds_read_b64_tr_b16 v[154:155], v211 offset:33792\n\t"
    "v_mfma_f32_16x16x32_bf16 a[200:203], v[156:159], v[124:127], a[200:203]\n\t"
    "v_mfma_f32_16x16x32_bf16 a[204:207], v[156:159], v[128:131], a[204:207]\n\t"
    "v_mfma_f32_16x16x32_bf16 a[208:211], v[156:159], v[132:135], a[208:211]\n\t"
    "ds_read_b64_tr_b16 v[156:157], v217 offset:32768\n\t"
    "ds_read_b64_tr_b16 v[158:159], v217 offset:33792\n\t"
    "v_mfma_f32_16x16x32_bf16 v[32:35], v[160:163], v[120:123], v[32:35]\n\t"
    "buffer_load_dwordx4 v250, s[4:7], s66 offen offset:3072 lds\n\t"
    "v_mfma_f32_16x16x32_bf16 v[36:39], v[160:163], v[124:127], v[36:39]\n\t"
    "v_mfma_f32_16x16x32_bf16 v[40:43], v[160:163], v[128:131], v[40:43]\n\t"
    "v_mfma_f32_16x16x32_bf16 v[44:47], v[160:163], v[132:135], v[44:47]\n\t"
    "ds_read_b64_tr_b16 v[160:161], v221 offset:32768\n\t"
    "ds_read_b64_tr_b16 v[162:163], v221 offset:33280\n\t"
    "v_mfma_f32_16x16x32_bf16 a[212:215], v[164:167], v[120:123], a[212:215]\n\t"
    "v_mfma_f32_16x16x32_bf16 a[216:219], v[164:167], v[124:127], a[216:219]\n\t"
    "v_mfma_f32_16x16x32_bf16 a[220:223], v[164:167], v[128:131], a[220:223]\n\t"
    "v_mfma_f32_16x16x32_bf16 a[224:227], v[164:167], v[132:135], a[224:227]\n\t"
    "ds_read_b64_tr_b16 v[164:165], v211 offset:18432\n\t"
    "ds_read_b64_tr_b16 v[166:167], v211 offset:19456\n\t"
    "s_waitcnt lgkmcnt(4)\n\t"
    "v_mfma_f32_16x16x32_bf16 v[48:51], v[168:171], v[120:123], v[48:51]\n\t"
    "v_mfma_f32_16x16x32_bf16 v[52:55], v[168:171], v[124:127], v[52:55]\n\t"
    "v_mfma_f32_16x16x32_bf16 v[56:59], v[168:171], v[128:131], v[56:59]\n\t"
    "v_mfma_f32_16x16x32_bf16 v[60:63], v[168:171], v[132:135], v[60:63]\n\t"
    "ds_read_b64_tr_b16 v[168:169], v217 offset:18432\n\t"
    "ds_read_b64_tr_b16 v[170:171], v217 offset:19456\n\t"
    "v_mfma_f32_16x16x32_bf16 a[228:231], v[172:175], v[120:123], a[228:231]\n\t"
    "v_mfma_f32_16x16x32_bf16 a[232:235], v[172:175], v[124:127], a[232:235]\n\t"
    "v_mfma_f32_16x16x32_bf16 a[236:239], v[172:175], v[128:131], a[236:239]\n\t"
    "v_mfma_f32_16x16x32_bf16 a[240:243], v[172:175], v[132:135], a[240:243]\n\t"
    "ds_read_b64_tr_b16 v[172:173], v209 offset:34816\n\t"
    "ds_read_b64_tr_b16 v[174:175], v209 offset:35840\n\t"
    "v_mfma_f32_16x16x32_bf16 v[0:3], v[152:155], v[120:123], v[0:3]\n\t"
    "v_mfma_f32_16x16x32_bf16 v[4:7], v[152:155], v[124:127], v[4:7]\n\t"
    "v_mfma_f32_16x16x32_bf16 v[8:11], v[152:155], v[128:131], v[8:11]\n\t"
    "v_mfma_f32_16x16x32_bf16 v[12:15], v[152:155], v[132:135], v[12:15]\n\t"
    "ds_read_b64_tr_b16 v[152:153], v215 offset:34816\n\t"
    "ds_read_b64_tr_b16 v[154:155], v215 offset:35840\n\t"
    "v_mfma_f32_16x16x32_bf16 a[180:183], v[156:159], v[120:123], a[180:183]\n\t"
    "v_mfma_f32_16x16x32_bf16 a[184:187], v[156:159], v[124:127], a[184:187]\n\t"
    "v_mfma_f32_16x16x32_bf16 a[188:191], v[156:159], v[128:131], a[188:191]\n\t"
    "v_mfma_f32_16x16x32_bf16 a[192:195], v[156:159], v[132:135], a[192:195]\n\t"
    "ds_read_b64_tr_b16 v[156:157], v209 offset:51200\n\t"
    "ds_read_b64_tr_b16 v[158:159], v209 offset:52224\n\t"
    "s_waitcnt lgkmcnt(4)\n\t"
    "v_mfma_f32_16x16x32_bf16 a[160:163], v[160:163], v[120:123], a[160:163]\n\t"
    "v_mfma_f32_16x16x32_bf16 a[164:167], v[160:163], v[124:127], a[164:167]\n\t"
    "v_mfma_f32_16x16x32_bf16 a[168:171], v[160:163], v[128:131], a[168:171]\n\t"
    "v_mfma_f32_16x16x32_bf16 a[172:175], v[160:163], v[132:135], a[172:175]\n\t"
    "ds_read_b64_tr_b16 v[160:161], v215 offset:51200\n\t"
    "ds_read_b64_tr_b16 v[162:163], v215 offset:52224\n\t"
    "v_mfma_f32_16x16x32_bf16 v[16:19], v[164:167], v[136:139], v[16:19]\n\t"
    "v_mfma_f32_16x16x32_bf16 v[20:23], v[164:167], v[140:143], v[20:23]\n\t"
    "v_mfma_f32_16x16x32_bf16 v[24:27], v[164:167], v[144:147], v[24:27]\n\t"
    "v_mfma_f32_16x16x32_bf16 v[28:31], v[164:167], v[148:151], v[28:31]\n\t"
    "ds_read_b64_tr_b16 v[164:165], v211 offset:34816\n\t"
    "ds_read_b64_tr_b16 v[166:167], v211 offset:35840\n\t"
    "v_mfma_f32_16x16x32_bf16 a[196:199], v[168:171], v[136:139], a[196:199]\n\t"
    "v_mfma_f32_16x16x32_bf16 a[200:203], v[168:171], v[140:143], a[200:203]\n\t"
    "v_mfma_f32_16x16x32_bf16 a[204:207], v[168:171], v[144:147], a[204:207]\n\t"
    "v_mfma_f32_16x16x32_bf16 a[208:211], v[168:171], v[148:151], a[208:211]\n\t"
    "ds_read_b64_tr_b16 v[168:169], v217 offset:34816\n\t"
    "ds_read_b64_tr_b16 v[170:171], v217 offset:35840\n\t"
    "v_mfma_f32_16x16x32_bf16 v[32:35], v[172:175], v[136:139], v[32:35]\n\t"
    "v_mfma_f32_16x16x32_bf16 v[36:39], v[172:175], v[140:143], v[36:39]\n\t"
    "v_mfma_f32_16x16x32_bf16 v[40:43], v[172:175], v[144:147], v[40:43]\n\t"
    "v_mfma_f32_16x16x32_bf16 v[44:47], v[172:175], v[148:151], v[44:47]\n\t"
    "ds_read_b64_tr_b16 v[172:173], v221 offset:33792\n\t"
    "ds_read_b64_tr_b16 v[174:175], v221 offset:34304\n\t"
    "s_waitcnt lgkmcnt(4)\n\t"
    "v_mfma_f32_16x16x32_bf16 a[212:215], v[152:155], v[136:139], a[212:215]\n\t"
    "v_mfma_f32_16x16x32_bf16 a[216:219], v[152:155], v[140:143], a[216:219]\n\t"
    "v_mfma_f32_16x16x32_bf16 a[220:223], v[152:155], v[144:147], a[220:223]\n\t"
    "v_mfma_f32_16x16x32_bf16 a[224:227], v[152:155], v[148:151], a[224:227]\n\t"
    "v_mfma_f32_16x16x32_bf16 v[48:51], v[156:159], v[136:139], v[48:51]\n\t"
    "v_mfma_f32_16x16x32_bf16 v[52:55], v[156:159], v[140:143], v[52:55]\n\t"
    "v_mfma_f32_16x16x32_bf16 v[56:59], v[156:159], v[144:147], v[56:59]\n\t"
    "v_mfma_f32_16x16x32_bf16 v[60:63], v[156:159], v[148:151], v[60:63]\n\t"
    "v_mfma_f32_16x16x32_bf16 a[228:231], v[160:163], v[136:139], a[228:231]\n\t"
    "s_add_u32 m0, s70, 98304\n\t"
    "s_xor_b32 s66, s28, 768\n\t"
    "v_mfma_f32_16x16x32_bf16 a[232:235], v[160:163], v[140:143], a[232:235]\n\t"
    "v_mfma_f32_16x16x32_bf16 a[236:239], v[160:163], v[144:147], a[236:239]\n\t"
    "v_mfma_f32_16x16x32_bf16 a[240:243], v[160:163], v[148:151], a[240:243]\n\t"
    "v_mfma_f32_16x16x32_bf16 v[0:3], v[164:167], v[136:139], v[0:3]\n\t"
    "v_mfma_f32_16x16x32_bf16 v[4:7], v[164:167], v[140:143], v[4:7]\n\t"
    "v_mfma_f32_16x16x32_bf16 v[8:11], v[164:167], v[144:147], v[8:11]\n\t"
    "s_waitcnt vmcnt(0) lgkmcnt(0)\n\t"
    "s_barrier\n\t"
    "buffer_load_dwordx4 v247, s[4:7], s66 offen offset:0 lds\n\t"
    "buffer_load_dwordx4 v248, s[4:7], s66 offen offset:1024 lds\n\t"
    "buffer_load_dwordx4 v249, s[4:7], s66 offen offset:2048 lds\n\t"
    "buffer_load_dwordx4 v250, s[4:7], s66 offen offset:3072 lds\n\t"
    "s_add_u32 m0, s16, 32768\n\t"
    "v_mfma_f32_16x16x32_bf16 v[12:15], v[164:167], v[148:151], v[12:15]\n\t"
    "buffer_load_dwordx4 v251, s[4:7], s28 offen lds\n\t"
    "s_add_u32 m0, s16, 33792\n\t"
    "s_add_u32 s66, s28, 36864\n\t"
    "ds_read_b64_tr_b16 v[152:153], v210 offset:49152\n\t"
    "ds_read_b64_tr_b16 v[154:155], v210 offset:50176\n\t"
    "v_mfma_f32_16x16x32_bf16 a[180:183], v[168:171], v[136:139], a[180:183]\n\t"
    "buffer_load_dwordx4 v251, s[4:7], s66 offen lds\n\t"
    "s_add_u32 m0, s16, 40960\n\t"
    "v_mfma_f32_16x16x32_bf16 a[184:187], v[168:171], v[140:143], a[184:187]\n\t"
    "buffer_load_dwordx4 v251, s[4:7], s29 offen lds\n\t"
    "s_add_u32 m0, s16, 41984\n\t"
    "s_add_u32 s66, s29, 36864\n\t"
    "v_mfma_f32_16x16x32_bf16 a[188:191], v[168:171], v[144:147], a[188:191]\n\t"
    "buffer_load_dwordx4 v251, s[4:7], s66 offen lds\n\t"
    "s_add_u32 m0, s70, 49152\n\t"
    "s_xor_b32 s66, s29, 256\n\t"
    "v_mfma_f32_16x16x32_bf16 a[192:195], v[168:171], v[148:151], a[192:195]\n\t"
    "buffer_load_dwordx4 v247, s[4:7], s66 offen offset:0 lds\n\t"
    "buffer_load_dwordx4 v248, s[4:7], s66 offen offset:1024 lds\n\t"
    "buffer_load_dwordx4 v249, s[4:7], s66 offen offset:2048 lds\n\t"
    "buffer_load_dwordx4 v250, s[4:7], s66 offen offset:3072 lds\n\t"
    "s_add_u32 m0, s70, 81920\n\t"
    "s_xor_b32 s66, s29, 512\n\t"
    "v_mfma_f32_16x16x32_bf16 a[160:163], v[172:175], v[136:139], a[160:163]\n\t"
    "buffer_load_dwordx4 v247, s[4:7], s66 offen offset:0 lds\n\t"
    "buffer_load_dwordx4 v248, s[4:7], s66 offen offset:1024 lds\n\t"
    "buffer_load_dwordx4 v249, s[4:7], s66 offen offset:2048 lds\n\t"
    "buffer_load_dwordx4 v250, s[4:7], s66 offen offset:3072 lds\n\t"
    "global_load_dword v254, v252, s[60:61] sc1\n\t"
    "s_waitcnt vmcnt(0)\n\t"
    "v_cmp_eq_u32 vcc, v254, v223\n\t"
    "s_andn2_b32 s36, 0x88442200, vcc_lo\n\t"
    "s_cbranch_scc1 SL0_%=\n\t"
    "PX0_%=:\n\t"
    "buffer_load_dwordx4 a[0:3], v226, s[8:11], s31 offen offset:0 sc1\n\t"
    "buffer_load_dwordx4 a[4:7], v226, s[8:11], s31 offen offset:1024 sc1\n\t"
    "buffer_load_dwordx4 a[8:11], v226, s[8:11], s31 offen offset:2048 sc1\n\t"
    "buffer_load_dwordx4 a[12:15], v226, s[8:11], s31 offen offset:3072 sc1\n\t"
    "buffer_load_dwordx4 a[16:19], v227, s[8:11], s31 offen offset:0 sc1\n\t"
    "buffer_load_dwordx4 a[20:23], v227, s[8:11], s31 offen offset:1024 sc1\n\t"
    "buffer_load_dwordx4 a[24:27], v227, s[8:11], s31 offen offset:2048 sc1\n\t"
    "buffer_load_dwordx4 a[28:31], v227, s[8:11], s31 offen offset:3072 sc1\n\t"
    "buffer_load_dwordx4 a[32:35], v228, s[8:11], s31 offen offset:0 sc1\n\t"
    "buffer_load_dwordx4 a[36:39], v228, s[8:11], s31 offen offset:1024 sc1\n\t"
    "buffer_load_dwordx4 a[40:43], v228, s[8:11], s31 offen offset:2048 sc1\n\t"
    "buffer_load_dwordx4 a[44:47], v228, s[8:11], s31 offen offset:3072 sc1\n\t"
    "buffer_load_dwordx4 a[48:51], v229, s[8:11], s31 offen offset:0 sc1\n\t"
    "buffer_load_dwordx4 a[52:55], v229, s[8:11], s31 offen offset:1024 sc1\n\t"
    "buffer_load_dwordx4 a[56:59], v229, s[8:11], s31 offen offset:2048 sc1\n\t"
    "buffer_load_dwordx4 a[60:63], v229, s[8:11], s31 offen offset:3072 sc1\n\t"
    "ds_read_b64_tr_b16 v[156:157], v216 offset:49152\n\t"
    "buffer_load_dwordx4 a[64:67], v230, s[8:11], s31 offen offset:0 sc1\n\t"
    "ds_read_b64_tr_b16 v[158:159], v216 offset:50176\n\t"
    "v_mfma_f32_16x16x32_bf16 a[164:167], v[172:175], v[140:143], a[164:167]\n\t"
    "buffer_load_dwordx4 a[68:71], v230, s[8:11], s31 offen offset:1024 sc1\n\t"
    "v_mfma_f32_16x16x32_bf16 a[168:171], v[172:175], v[144:147], a[168:171]\n\t"
    "ds_read_b64_tr_b16 v[160:161], v212 offset:0\n\t"
    "ds_read_b64_tr_b16 v[162:163], v212 offset:1024\n\t"
    "v_mfma_f32_16x16x32_bf16 a[172:175], v[172:175], v[148:151], a[172:175]\n\t"
    "ds_read_b64_tr_b16 v[164:165], v218 offset:0\n\t"
    "ds_read_b64_tr_b16 v[166:167], v218 offset:1024\n\t"
    "s_waitcnt vmcnt(17) lgkmcnt(6)\n\t"
    "v_mfma_f32_16x16x32_bf16 v[16:19], v[152:155], a[0:3], v[16:19]\n\t"
    "ds_read_b64_tr_b16 v[168:169], v212 offset:16384\n\t"
    "ds_read_b64_tr_b16 v[170:171], v212 offset:17408\n\t"
    "s_waitcnt vmcnt(16)\n\t"
    "v_mfma_f32_16x16x32_bf16 v[20:23], v[152:155], a[4:7], v[20:23]\n\t"
    "ds_read_b64_tr_b16 v[172:173], v218 offset:16384\n\t"
    "ds_read_b64_tr_b16 v[174:175], v218 offset:17408\n\t"
    "s_waitcnt vmcnt(15)\n\t"
    "v_mfma_f32_16x16x32_bf16 v[24:27], v[152:155], a[8:11], v[24:27]\n\t"
    "s_waitcnt vmcnt(14)\n\t"
    "v_mfma_f32_16x16x32_bf16 v[28:31], v[152:155], a[12:15], v[28:31]\n\t"
    "s_waitcnt vmcnt(13)\n\t"
    "v_mfma_f32_16x16x32_bf16 v[68:71], v[152:155], a[16:19], v[68:71]\n\t"
    "ds_read_b64_tr_b16 v[152:153], v220 offset:40960\n\t"
    "ds_read_b64_tr_b16 v[154:155], v220 offset:41472\n\t"
    "s_waitcnt lgkmcnt(10)\n\t"
    "v_mfma_f32_16x16x32_bf16 a[196:199], v[156:159], a[0:3], a[196:199]\n\t"
    "v_mfma_f32_16x16x32_bf16 a[200:203], v[156:159], a[4:7], a[200:203]\n\t"
    "v_mfma_f32_16x16x32_bf16 a[204:207], v[156:159], a[8:11], a[204:207]\n\t"
    "v_mfma_f32_16x16x32_bf16 a[208:211], v[156:159], a[12:15], a[208:211]\n\t"
    "v_mfma_f32_16x16x32_bf16 a[148:151], v[156:159], a[16:19], a[148:151]\n\t"
    "ds_read_b64_tr_b16 v[156:157], v210 offset:51200\n\t"
    "ds_read_b64_tr_b16 v[158:159], v210 offset:52224\n\t"
    "s_waitcnt lgkmcnt(4)\n\t"
    "v_mfma_f32_16x16x32_bf16 v[32:35], v[160:163], a[0:3], v[32:35]\n\t"
    "v_mfma_f32_16x16x32_bf16 v[36:39], v[160:163], a[4:7], v[36:39]\n\t"
    "v_mfma_f32_16x16x32_bf16 v[40:43], v[160:163], a[8:11], v[40:43]\n\t"
    "v_mfma_f32_16x16x32_bf16 v[44:47], v[160:163], a[12:15], v[44:47]\n\t"
    "v_mfma_f32_16x16x32_bf16 v[72:75], v[160:163], a[16:19], v[72:75]\n\t"
    "ds_read_b64_tr_b16 v[160:161], v216 offset:51200\n\t"
    "ds_read_b64_tr_b16 v[162:163], v216 offset:52224\n\t"
    "v_mfma_f32_16x16x32_bf16 a[212:215], v[164:167], a[0:3], a[212:215]\n\t"
    "v_mfma_f32_16x16x32_bf16 a[216:219], v[164:167], a[4:7], a[216:219]\n\t"
    "v_mfma_f32_16x16x32_bf16 a[220:223], v[164:167], a[8:11], a[220:223]\n\t"
    "v_mfma_f32_16x16x32_bf16 a[224:227], v[164:167], a[12:15], a[224:227]\n\t"
    "v_mfma_f32_16x16x32_bf16 a[152:155], v[164:167], a[16:19], a[152:155]\n\t"
    "ds_read_b64_tr_b16 v[164:165], v212 offset:2048\n\t"
    "ds_read_b64_tr_b16 v[166:167], v212 offset:3072\n\t"
    "v_mfma_f32_16x16x32_bf16 v[48:51], v[168:171], a[0:3], v[48:51]\n\t"
    "v_mfma_f32_16x16x32_bf16 v[52:55], v[168:171], a[4:7], v[52:55]\n\t"
    "v_mfma_f32_16x16x32_bf16 v[56:59], v[168:171], a[8:11], v[56:59]\n\t"
    "v_mfma_f32_16x16x32_bf16 v[60:63], v[168:171], a[12:15], v[60:63]\n\t"
    "v_mfma_f32_16x16x32_bf16 v[76:79], v[168:171], a[16:19], v[76:79]\n\t"
    "ds_read_b64_tr_b16 v[168:169], v218 offset:2048\n\t"
    "ds_read_b64_tr_b16 v[170:171], v218 offset:3072\n\t"
    "v_mfma_f32_16x16x32_bf16 a[228:231], v[172:175], a[0:3], a[228:231]\n\t"
    "v_mfma_f32_16x16x32_bf16 a[232:235], v[172:175], a[4:7], a[232:235]\n\t"
    "v_mfma_f32_16x16x32_bf16 a[236:239], v[172:175], a[8:11], a[236:239]\n\t"
    "v_mfma_f32_16x16x32_bf16 a[240:243], v[172:175], a[12:15], a[240:243]\n\t"
    "v_mfma_f32_16x16x32_bf16 a[156:159], v[172:175], a[16:19], a[156:159]\n\t"
    "ds_read_b64_tr_b16 v[172:173], v212 offset:18432\n\t"
    "ds_read_b64_tr_b16 v[174:175], v212 offset:19456\n\t"
    "s_waitcnt lgkmcnt(4)\n\t"
    "v_mfma_f32_16x16x32_bf16 a[160:163], v[152:155], a[0:3], a[160:163]\n\t"
    "v_mfma_f32_16x16x32_bf16 a[164:167], v[152:155], a[4:7], a[164:167]\n\t"
    "v_mfma_f32_16x16x32_bf16 a[168:171], v[152:155], a[8:11], a[168:171]\n\t"
    "v_mfma_f32_16x16x32_bf16 a[172:175], v[152:155], a[12:15], a[172:175]\n\t"
    "v_mfma_f32_16x16x32_bf16 a[176:179], v[152:155], a[16:19], a[176:179]\n\t"
    "ds_read_b64_tr_b16 v[152:153], v218 offset:18432\n\t"
    "ds_read_b64_tr_b16 v[154:155], v218 offset:19456\n\t"
    "s_waitcnt vmcnt(12)\n\t"
    "v_mfma_f32_16x16x32_bf16 v[16:19], v[156:159], a[20:23], v[16:19]\n\t"
    "s_waitcnt vmcnt(11)\n\t"
    "v_mfma_f32_16x16x32_bf16 v[20:23], v[156:159], a[24:27], v[20:23]\n\t"
    "s_waitcnt vmcnt(10)\n\t"
    "v_mfma_f32_16x16x32_bf16 v[24:27], v[156:159], a[28:31], v[24:27]\n\t"
    "s_waitcnt vmcnt(9)\n\t"
    "v_mfma_f32_16x16x32_bf16 v[28:31], v[156:159], a[32:35], v[28:31]\n\t"
    "s_waitcnt vmcnt(8)\n\t"
    "v_mfma_f32_16x16x32_bf16 v[68:71], v[156:159], a[36:39], v[68:71]\n\t"
    "ds_read_b64_tr_b16 v[156:157], v220 offset:41984\n\t"
    "ds_read_b64_tr_b16 v[158:159], v220 offset:42496\n\t"
    "v_mfma_f32_16x16x32_bf16 a[196:199], v[160:163], a[20:23], a[196:199]\n\t"
    "v_mfma_f32_16x16x32_bf16 a[200:203], v[160:163], a[24:27], a[200:203]\n\t"
    "buffer_load_dwordx4 a[72:75], v226, s[8:11], s32 offen offset:0 sc1\n\t"
    "v_mfma_f32_16x16x32_bf16 a[204:207], v[160:163], a[28:31], a[204:207]\n\t"
    "v_mfma_f32_16x16x32_bf16 a[208:211], v[160:163], a[32:35], a[208:211]\n\t"
    "v_mfma_f32_16x16x32_bf16 a[148:151], v[160:163], a[36:39], a[148:151]\n\t"
    "buffer_load_dwordx4 a[76:79], v226, s[8:11], s32 offen offset:1024 sc1\n\t"
    "v_mfma_f32_16x16x32_bf16 v[32:35], v[164:167], a[20:23], v[32:35]\n\t"
    "ds_read_b64_tr_b16 v[160:161], v211 offset:49152\n\t"
    "ds_read_b64_tr_b16 v[162:163], v211 offset:50176\n\t"
    "v_mfma_f32_16x16x32_bf16 v[36:39], v[164:167], a[24:27], v[36:39]\n\t"
    "v_mfma_f32_16x16x32_bf16 v[40:43], v[164:167], a[28:31], v[40:43]\n\t"
    "v_mfma_f32_16x16x32_bf16 v[44:47], v[164:167], a[32:35], v[44:47]\n\t"
    "buffer_load_dwordx4 a[80:83], v226, s[8:11], s32 offen offset:2048 sc1\n\t"
    "v_mfma_f32_16x16x32_bf16 v[72:75], v[164:167], a[36:39], v[72:75]\n\t"
    "ds_read_b64_tr_b16 v[164:165], v217 offset:49152\n\t"
    "ds_read_b64_tr_b16 v[166:167], v217 offset:50176\n\t"
    "s_waitcnt lgkmcnt(4)\n\t"
    "v_mfma_f32_16x16x32_bf16 a[212:215], v[168:171], a[20:23], a[212:215]\n\t"
    "v_mfma_f32_16x16x32_bf16 a[216:219], v[168:171], a[24:27], a[216:219]\n\t"
    "v_mfma_f32_16x16x32_bf16 a[220:223], v[168:171], a[28:31], a[220:223]\n\t"
    "buffer_load_dwordx4 a[84:87], v226, s[8:11], s32 offen offset:3072 sc1\n\t"
    "v_mfma_f32_16x16x32_bf16 a[224:227], v[168:171], a[32:35], a[224:227]\n\t"
    "v_mfma_f32_16x16x32_bf16 a[152:155], v[168:171], a[36:39], a[152:155]\n\t"
    "ds_read_b64_tr_b16 v[168:169], v213 offset:0\n\t"
    "ds_read_b64_tr_b16 v[170:171], v213 offset:1024\n\t"
    "v_mfma_f32_16x16x32_bf16 v[48:51], v[172:175], a[20:23], v[48:51]\n\t"
    "v_mfma_f32_16x16x32_bf16 v[52:55], v[172:175], a[24:27], v[52:55]\n\t"
    "buffer_load_dwordx4 a[88:91], v227, s[8:11], s32 offen offset:0 sc1\n\t"
    "v_mfma_f32_16x16x32_bf16 v[56:59], v[172:175], a[28:31], v[56:59]\n\t"
    "v_mfma_f32_16x16x32_bf16 v[60:63], v[172:175], a[32:35], v[60:63]\n\t"
    "v_mfma_f32_16x16x32_bf16 v[76:79], v[172:175], a[36:39], v[76:79]\n\t"
    "ds_read_b64_tr_b16 v[172:173], v219 offset:0\n\t"
    "ds_read_b64_tr_b16 v[174:175], v219 offset:1024\n\t"
    "v_mfma_f32_16x16x32_bf16 a[228:231], v[152:155], a[20:23], a[228:231]\n\t"
    "buffer_load_dwordx4 a[92:95], v227, s[8:11], s32 offen offset:1024 sc1\n\t"
    "v_mfma_f32_16x16x32_bf16 a[232:235], v[152:155], a[24:27], a[232:235]\n\t"
    "v_mfma_f32_16x16x32_bf16 a[236:239], v[152:155], a[28:31], a[236:239]\n\t"
    "v_mfma_f32_16x16x32_bf16 a[240:243], v[152:155], a[32:35], a[240:243]\n\t"
    "v_mfma_f32_16x16x32_bf16 a[156:159], v[152:155], a[36:39], a[156:159]\n\t"
    "buffer_load_dwordx4 a[96:99], v227, s[8:11], s32 offen offset:2048 sc1\n\t"
    "v_mfma_f32_16x16x32_bf16 a[160:163], v[156:159], a[20:23], a[160:163]\n\t"
    "ds_read_b64_tr_b16 v[152:153], v213 offset:16384\n\t"
    "ds_read_b64_tr_b16 v[154:155], v213 offset:17408\n\t"
    "v_mfma_f32_16x16x32_bf16 a[164:167], v[156:159], a[24:27], a[164:167]\n\t"
    "v_mfma_f32_16x16x32_bf16 a[168:171], v[156:159], a[28:31], a[168:171]\n\t"
    "v_mfma_f32_16x16x32_bf16 a[172:175], v[156:159], a[32:35], a[172:175]\n\t"
    "buffer_load_dwordx4 a[100:103], v227, s[8:11], s32 offen offset:3072 sc1\n\t"
    "v_mfma_f32_16x16x32_bf16 a[176:179], v[156:159], a[36:39], a[176:179]\n\t"
    "ds_read_b64_tr_b16 v[156:157], v219 offset:16384\n\t"
    "ds_read_b64_tr_b16 v[158:159], v219 offset:17408\n\t"
    "s_waitcnt vmcnt(15) lgkmcnt(4)\n\t"
    "v_mfma_f32_16x16x32_bf16 v[16:19], v[160:163], a[40:43], v[16:19]\n\t"
    "s_waitcnt vmcnt(14)\n\t"
    "v_mfma_f32_16x16x32_bf16 v[20:23], v[160:163], a[44:47], v[20:23]\n\t"
    "s_waitcnt vmcnt(13)\n\t"
    "v_mfma_f32_16x16x32_bf16 v[24:27], v[160:163], a[48:51], v[24:27]\n\t"
    "buffer_load_dwordx4 a[104:107], v228, s[8:11], s32 offen offset:0 sc1\n\t"
    "s_waitcnt vmcnt(13)\n\t"
    "v_mfma_f32_16x16x32_bf16 v[28:31], v[160:163], a[52:55], v[28:31]\n\t"
    "ds_read_b64_tr_b16 v[160:161], v221 offset:40960\n\t"
    "ds_read_b64_tr_b16 v[162:163], v221 offset:41472\n\t"
    "v_mfma_f32_16x16x32_bf16 a[196:199], v[164:167], a[40:43], a[196:199]\n\t"
    "v_mfma_f32_16x16x32_bf16 a[200:203], v[164:167], a[44:47], a[200:203]\n\t"
    "v_mfma_f32_16x16x32_bf16 a[204:207], v[164:167], a[48:51], a[204:207]\n\t"
    "buffer_load_dwordx4 a[108:111], v228, s[8:11], s32 offen offset:1024 sc1\n\t"
    "v_mfma_f32_16x16x32_bf16 a[208:211], v[164:167], a[52:55], a[208:211]\n\t"
    "ds_read_b64_tr_b16 v[164:165], v211 offset:51200\n\t"
    "ds_read_b64_tr_b16 v[166:167], v211 offset:52224\n\t"
    "v_mfma_f32_16x16x32_bf16 v[32:35], v[168:171], a[40:43], v[32:35]\n\t"
    "v_mfma_f32_16x16x32_bf16 v[36:39], v[168:171], a[44:47], v[36:39]\n\t"
    "buffer_load_dwordx4 a[112:115], v228, s[8:11], s32 offen offset:2048 sc1\n\t"
    "v_mfma_f32_16x16x32_bf16 v[40:43], v[168:171], a[48:51], v[40:43]\n\t"
    "v_mfma_f32_16x16x32_bf16 v[44:47], v[168:171], a[52:55], v[44:47]\n\t"
    "ds_read_b64_tr_b16 v[168:169], v217 offset:51200\n\t"
    "ds_read_b64_tr_b16 v[170:171], v217 offset:52224\n\t"
    "v_mfma_f32_16x16x32_bf16 a[212:215], v[172:175], a[40:43], a[212:215]\n\t"
    "v_mfma_f32_16x16x32_bf16 a[216:219], v[172:175], a[44:47], a[216:219]\n\t"
    "buffer_load_dwordx4 a[116:119], v228, s[8:11], s32 offen offset:3072 sc1\n\t"
    "v_mfma_f32_16x16x32_bf16 a[220:223], v[172:175], a[48:51], a[220:223]\n\t"
    "v_mfma_f32_16x16x32_bf16 a[224:227], v[172:175], a[52:55], a[224:227]\n\t"
    "ds_read_b64_tr_b16 v[172:173], v213 offset:2048\n\t"
    "ds_read_b64_tr_b16 v[174:175], v213 offset:3072\n\t"
    "s_waitcnt lgkmcnt(4)\n\t"
    "v_mfma_f32_16x16x32_bf16 v[48:51], v[152:155], a[40:43], v[48:51]\n\t"
    "v_mfma_f32_16x16x32_bf16 v[52:55], v[152:155], a[44:47], v[52:55]\n\t"
    "buffer_load_dwordx4 a[120:123], v229, s[8:11], s32 offen offset:0 sc1\n\t"
    "v_mfma_f32_16x16x32_bf16 v[56:59], v[152:155], a[48:51], v[56:59]\n\t"
    "v_mfma_f32_16x16x32_bf16 v[60:63], v[152:155], a[52:55], v[60:63]\n\t"
    "ds_read_b64_tr_b16 v[152:153], v219 offset:2048\n\t"
    "ds_read_b64_tr_b16 v[154:155], v219 offset:3072\n\t"
    "v_mfma_f32_16x16x32_bf16 a[228:231], v[156:159], a[40:43], a[228:231]\n\t"
    "v_mfma_f32_16x16x32_bf16 a[232:235], v[156:159], a[44:47], a[232:235]\n\t"
    "buffer_load_dwordx4 a[124:127], v229, s[8:11], s32 offen offset:1024 sc1\n\t"
    "v_mfma_f32_16x16x32_bf16 a[236:239], v[156:159], a[48:51], a[236:239]\n\t"
    "v_mfma_f32_16x16x32_bf16 a[240:243], v[156:159], a[52:55], a[240:243]\n\t"
    "ds_read_b64_tr_b16 v[156:157], v213 offset:18432\n\t"
    "ds_read_b64_tr_b16 v[158:159], v213 offset:19456\n\t"
    "v_mfma_f32_16x16x32_bf16 a[160:163], v[160:163], a[40:43], a[160:163]\n\t"
    "v_mfma_f32_16x16x32_bf16 a[164:167], v[160:163], a[44:47], a[164:167]\n\t"
    "buffer_load_dwordx4 a[128:131], v229, s[8:11], s32 offen offset:2048 sc1\n\t"
    "v_mfma_f32_16x16x32_bf16 a[168:171], v[160:163], a[48:51], a[168:171]\n\t"
    "v_mfma_f32_16x16x32_bf16 a[172:175], v[160:163], a[52:55], a[172:175]\n\t"
    "ds_read_b64_tr_b16 v[160:161], v219 offset:18432\n\t"
    "ds_read_b64_tr_b16 v[162:163], v219 offset:19456\n\t"
    "s_waitcnt vmcnt(18)\n\t"
    "v_mfma_f32_16x16x32_bf16 v[16:19], v[164:167], a[56:59], v[16:19]\n\t"
    "s_waitcnt vmcnt(17)\n\t"
    "v_mfma_f32_16x16x32_bf16 v[20:23], v[164:167], a[60:63], v[20:23]\n\t"
    "buffer_load_dwordx4 a[132:135], v229, s[8:11], s32 offen offset:3072 sc1\n\t"
    "s_waitcnt vmcnt(17)\n\t"
    "v_mfma_f32_16x16x32_bf16 v[24:27], v[164:167], a[64:67], v[24:27]\n\t"
    "s_waitcnt vmcnt(16)\n\t"
    "v_mfma_f32_16x16x32_bf16 v[28:31], v[164:167], a[68:71], v[28:31]\n\t"
    "ds_read_b64_tr_b16 v[164:165], v221 offset:41984\n\t"
    "ds_read_b64_tr_b16 v[166:167], v221 offset:42496\n\t"
    "s_waitcnt lgkmcnt(4)\n\t"
    "v_mfma_f32_16x16x32_bf16 a[196:199], v[168:171], a[56:59], a[196:199]\n\t"
    "v_mfma_f32_16x16x32_bf16 a[200:203], v[168:171], a[60:63], a[200:203]\n\t"
    "buffer_load_dwordx4 a[136:139], v230, s[8:11], s32 offen offset:0 sc1\n\t"
    "v_mfma_f32_16x16x32_bf16 a[204:207], v[168:171], a[64:67], a[204:207]\n\t"
    "v_mfma_f32_16x16x32_bf16 a[208:211], v[168:171], a[68:71], a[208:211]\n\t"
    "ds_read_b64_tr_b16 v[168:169], v208 offset:0\n\t"
    "ds_read_b64_tr_b16 v[170:171], v208 offset:1024\n\t"
    "v_mfma_f32_16x16x32_bf16 v[32:35], v[172:175], a[56:59], v[32:35]\n\t"
    "v_mfma_f32_16x16x32_bf16 v[36:39], v[172:175], a[60:63], v[36:39]\n\t"
    "buffer_load_dwordx4 a[140:143], v230, s[8:11], s32 offen offset:1024 sc1\n\t"
    "v_mfma_f32_16x16x32_bf16 v[40:43], v[172:175], a[64:67], v[40:43]\n\t"
    "v_mfma_f32_16x16x32_bf16 v[44:47], v[172:175], a[68:71], v[44:47]\n\t"
    "ds_read_b64_tr_b16 v[172:173], v214 offset:0\n\t"
    "ds_read_b64_tr_b16 v[174:175], v214 offset:1024\n\t"
    "v_mfma_f32_16x16x32_bf16 a[212:215], v[152:155], a[56:59], a[212:215]\n\t"
    "v_mfma_f32_16x16x32_bf16 a[216:219], v[152:155], a[60:63], a[216:219]\n\t"
    "buffer_load_dwordx4 v[80:83], v226, s[8:11], s33 offen offset:0 sc1\n\t"
    "v_mfma_f32_16x16x32_bf16 a[220:223], v[152:155], a[64:67], a[220:223]\n\t"
    "v_mfma_f32_16x16x32_bf16 a[224:227], v[152:155], a[68:71], a[224:227]\n\t"
    "ds_read_b64_tr_b16 v[152:153], v208 offset:16384\n\t"
    "ds_read_b64_tr_b16 v[154:155], v208 offset:17408\n\t"
    "v_mfma_f32_16x16x32_bf16 v[48:51], v[156:159], a[56:59], v[48:51]\n\t"
    "buffer_load_dwordx4 v[84:87], v226, s[8:11], s33 offen offset:1024 sc1\n\t"
    "v_mfma_f32_16x16x32_bf16 v[52:55], v[156:159], a[60:63], v[52:55]\n\t"
    "v_mfma_f32_16x16x32_bf16 v[56:59], v[156:159], a[64:67], v[56:59]\n\t"
    "v_mfma_f32_16x16x32_bf16 v[60:63], v[156:159], a[68:71], v[60:63]\n\t"
    "ds_read_b64_tr_b16 v[156:157], v214 offset:16384\n\t"
    "ds_read_b64_tr_b16 v[158:159], v214 offset:17408\n\t"
    "s_waitcnt lgkmcnt(4)\n\t"
    "v_mfma_f32_16x16x32_bf16 a[228:231], v[160:163], a[56:59], a[228:231]\n\t"
    "buffer_load_dwordx4 v[88:91], v226, s[8:11], s33 offen offset:2048 sc1\n\t"
    "v_mfma_f32_16x16x32_bf16 a[232:235], v[160:163], a[60:63], a[232:235]\n\t"
    "v_mfma_f32_16x16x32_bf16 a[236:239], v[160:163], a[64:67], a[236:239]\n\t"
    "s_nop 0\n\t"
    "s_barrier\n\t"
    "v_mfma_f32_16x16x32_bf16 a[240:243], v[160:163], a[68:71], a[240:243]\n\t"
    "ds_read_b64_tr_b16 v[160:161], v210 offset:32768\n\t"
    "ds_read_b64_tr_b16 v[162:163], v210 offset:33792\n\t"
    "v_mfma_f32_16x16x32_bf16 a[160:163], v[164:167], a[56:59], a[160:163]\n\t"
    "buffer_load_dwordx4 v[92:95], v226, s[8:11], s33 offen offset:3072 sc1\n\t"
    "v_mfma_f32_16x16x32_bf16 a[164:167], v[164:167], a[60:63], a[164:167]\n\t"
    "v_mfma_f32_16x16x32_bf16 a[168:171], v[164:167], a[64:67], a[168:171]\n\t"
    "v_mfma_f32_16x16x32_bf16 a[172:175], v[164:167], a[68:71], a[172:175]\n\t"
    "ds_read_b64_tr_b16 v[164:165], v216 offset:32768\n\t"
    "ds_read_b64_tr_b16 v[166:167], v216 offset:33792\n\t"
    "s_waitcnt vmcnt(21)\n\t"
    "v_mfma_f32_16x16x32_bf16 v[16:19], v[168:171], a[72:75], v[16:19]\n\t"
    "buffer_load_dwordx4 v[96:99], v227, s[8:11], s33 offen offset:0 sc1\n\t"
    "s_waitcnt vmcnt(21)\n\t"
    "v_mfma_f32_16x16x32_bf16 v[20:23], v[168:171], a[76:79], v[20:23]\n\t"
    "s_waitcnt vmcnt(20)\n\t"
    "v_mfma_f32_16x16x32_bf16 v[24:27], v[168:171], a[80:83], v[24:27]\n\t"
    "s_waitcnt vmcnt(19)\n\t"
    "v_mfma_f32_16x16x32_bf16 v[28:31], v[168:171], a[84:87], v[28:31]\n\t"
    "s_waitcnt vmcnt(18)\n\t"
    "v_mfma_f32_16x16x32_bf16 v[68:71], v[168:171], a[88:91], v[68:71]\n\t"
    "buffer_load_dwordx4 v[100:103], v227, s[8:11], s33 offen offset:1024 sc1\n\t"
    "v_mfma_f32_16x16x32_bf16 a[196:199], v[172:175], a[72:75], a[196:199]\n\t"
    "ds_read_b64_tr_b16 v[168:169], v220 offset:0\n\t"
    "ds_read_b64_tr_b16 v[170:171], v220 offset:512\n\t"
    "v_mfma_f32_16x16x32_bf16 a[200:203], v[172:175], a[76:79], a[200:203]\n\t"
    "v_mfma_f32_16x16x32_bf16 a[204:207], v[172:175], a[80:83], a[204:207]\n\t"
    "v_mfma_f32_16x16x32_bf16 a[208:211], v[172:175], a[84:87], a[208:211]\n\t"
    "buffer_load_dwordx4 v[104:107], v227, s[8:11], s33 offen offset:2048 sc1\n\t"
    "v_mfma_f32_16x16x32_bf16 a[148:151], v[172:175], a[88:91], a[148:151]\n\t"
    "ds_read_b64_tr_b16 v[172:173], v208 offset:2048\n\t"
    "ds_read_b64_tr_b16 v[174:175], v208 offset:3072\n\t"
    "s_waitcnt lgkmcnt(4)\n\t"
    "v_mfma_f32_16x16x32_bf16 v[32:35], v[152:155], a[72:75], v[32:35]\n\t"
    "v_mfma_f32_16x16x32_bf16 v[36:39], v[152:155], a[76:79], v[36:39]\n\t"
    "v_mfma_f32_16x16x32_bf16 v[40:43], v[152:155], a[80:83], v[40:43]\n\t"
    "buffer_load_dwordx4 v[108:111], v227, s[8:11], s33 offen offset:3072 sc1\n\t"
    "v_mfma_f32_16x16x32_bf16 v[44:47], v[152:155], a[84:87], v[44:47]\n\t"
    "v_mfma_f32_16x16x32_bf16 v[72:75], v[152:155], a[88:91], v[72:75]\n\t"
    "ds_read_b64_tr_b16 v[152:153], v214 offset:2048\n\t"
    "ds_read_b64_tr_b16 v[154:155], v214 offset:3072\n\t"
    "v_mfma_f32_16x16x32_bf16 a[212:215], v[156:159], a[72:75], a[212:215]\n\t"
    "v_mfma_f32_16x16x32_bf16 a[216:219], v[156:159], a[76:79], a[216:219]\n\t"
    "buffer_load_dwordx4 v[112:115], v228, s[8:11], s33 offen offset:0 sc1\n\t"
    "v_mfma_f32_16x16x32_bf16 a[220:223], v[156:159], a[80:83], a[220:223]\n\t"
    "v_mfma_f32_16x16x32_bf16 a[224:227], v[156:159], a[84:87], a[224:227]\n\t"
    "v_mfma_f32_16x16x32_bf16 a[152:155], v[156:159], a[88:91], a[152:155]\n\t"
    "ds_read_b64_tr_b16 v[156:157], v208 offset:18432\n\t"
    "ds_read_b64_tr_b16 v[158:159], v208 offset:19456\n\t"
    "v_mfma_f32_16x16x32_bf16 v[48:51], v[160:163], a[72:75], v[48:51]\n\t"
    "buffer_load_dwordx4 v[116:119], v228, s[8:11], s33 offen offset:1024 sc1\n\t"
    "v_mfma_f32_16x16x32_bf16 v[52:55], v[160:163], a[76:79], v[52:55]\n\t"
    "v_mfma_f32_16x16x32_bf16 v[56:59], v[160:163], a[80:83], v[56:59]\n\t"
    "v_mfma_f32_16x16x32_bf16 v[60:63], v[160:163], a[84:87], v[60:63]\n\t"
    "buffer_load_dwordx4 v[120:123], v228, s[8:11], s33 offen offset:2048 sc1\n\t"
    "v_mfma_f32_16x16x32_bf16 v[76:79], v[160:163], a[88:91], v[76:79]\n\t"
    "ds_read_b64_tr_b16 v[160:161], v214 offset:18432\n\t"
    "ds_read_b64_tr_b16 v[162:163], v214 offset:19456\n\t"
    "v_mfma_f32_16x16x32_bf16 a[228:231], v[164:167], a[72:75], a[228:231]\n\t"
    "v_mfma_f32_16x16x32_bf16 a[232:235], v[164:167], a[76:79], a[232:235]\n\t"
    "v_mfma_f32_16x16x32_bf16 a[236:239], v[164:167], a[80:83], a[236:239]\n\t"
    "buffer_load_dwordx4 v[124:127], v228, s[8:11], s33 offen offset:3072 sc1\n\t"
    "v_mfma_f32_16x16x32_bf16 a[240:243], v[164:167], a[84:87], a[240:243]\n\t"
    "v_mfma_f32_16x16x32_bf16 a[156:159], v[164:167], a[88:91], a[156:159]\n\t"
    "ds_read_b64_tr_b16 v[164:165], v210 offset:34816\n\t"
    "ds_read_b64_tr_b16 v[166:167], v210 offset:35840\n\t"
    "s_waitcnt lgkmcnt(4)\n\t"
    "v_mfma_f32_16x16x32_bf16 a[160:163], v[168:171], a[72:75], a[160:163]\n\t"
    "v_mfma_f32_16x16x32_bf16 a[164:167], v[168:171], a[76:79], a[164:167]\n\t"
    "buffer_load_dwordx4 v[128:131], v229, s[8:11], s33 offen offset:0 sc1\n\t"
    "v_mfma_f32_16x16x32_bf16 a[168:171], v[168:171], a[80:83], a[168:171]\n\t"
    "v_mfma_f32_16x16x32_bf16 a[172:175], v[168:171], a[84:87], a[172:175]\n\t"
    "v_mfma_f32_16x16x32_bf16 a[176:179], v[168:171], a[88:91], a[176:179]\n\t"
    "ds_read_b64_tr_b16 v[168:169], v216 offset:34816\n\t"
    "ds_read_b64_tr_b16 v[170:171], v216 offset:35840\n\t"
    "s_waitcnt vmcnt(25)\n\t"
    "v_mfma_f32_16x16x32_bf16 v[16:19], v[172:175], a[92:95], v[16:19]\n\t"
    "buffer_load_dwordx4 v[132:135], v229, s[8:11], s33 offen offset:1024 sc1\n\t"
    "s_waitcnt vmcnt(25)\n\t"
    "v_mfma_f32_16x16x32_bf16 v[20:23], v[172:175], a[96:99], v[20:23]\n\t"
    "s_waitcnt vmcnt(24)\n\t"
    "v_mfma_f32_16x16x32_bf16 v[24:27], v[172:175], a[100:103], v[24:27]\n\t"
    "s_waitcnt vmcnt(23)\n\t"
    "v_mfma_f32_16x16x32_bf16 v[28:31], v[172:175], a[104:107], v[28:31]\n\t"
    "s_waitcnt vmcnt(22)\n\t"
    "v_mfma_f32_16x16x32_bf16 v[68:71], v[172:175], a[108:111], v[68:71]\n\t"
    "buffer_load_dwordx4 v[136:139], v229, s[8:11], s33 offen offset:2048 sc1\n\t"
    "v_mfma_f32_16x16x32_bf16 a[196:199], v[152:155], a[92:95], a[196:199]\n\t"
    "ds_read_b64_tr_b16 v[172:173], v220 offset:1024\n\t"
    "ds_read_b64_tr_b16 v[174:175], v220 offset:1536\n\t"
    "v_mfma_f32_16x16x32_bf16 a[200:203], v[152:155], a[96:99], a[200:203]\n\t"
    "v_mfma_f32_16x16x32_bf16 a[204:207], v[152:155], a[100:103], a[204:207]\n\t"
    "v_mfma_f32_16x16x32_bf16 a[208:211], v[152:155], a[104:107], a[208:211]\n\t"
    "buffer_load_dwordx4 v[140:143], v229, s[8:11], s33 offen offset:3072 sc1\n\t"
    "v_mfma_f32_16x16x32_bf16 a[148:151], v[152:155], a[108:111], a[148:151]\n\t"
    "ds_read_b64_tr_b16 v[152:153], v209 offset:0\n\t"
    "ds_read_b64_tr_b16 v[154:155], v209 offset:1024\n\t"
    "v_mfma_f32_16x16x32_bf16 v[32:35], v[156:159], a[92:95], v[32:35]\n\t"
    "v_mfma_f32_16x16x32_bf16 v[36:39], v[156:159], a[96:99], v[36:39]\n\t"
    "v_mfma_f32_16x16x32_bf16 v[40:43], v[156:159], a[100:103], v[40:43]\n\t"
    "buffer_load_dwordx4 v[144:147], v230, s[8:11], s33 offen offset:0 sc1\n\t"
    "v_mfma_f32_16x16x32_bf16 v[44:47], v[156:159], a[104:107], v[44:47]\n\t"
    "v_mfma_f32_16x16x32_bf16 v[72:75], v[156:159], a[108:111], v[72:75]\n\t"
    "ds_read_b64_tr_b16 v[156:157], v215 offset:0\n\t"
    "ds_read_b64_tr_b16 v[158:159], v215 offset:1024\n\t"
    "s_waitcnt lgkmcnt(4)\n\t"
    "v_mfma_f32_16x16x32_bf16 a[212:215], v[160:163], a[92:95], a[212:215]\n\t"
    "v_mfma_f32_16x16x32_bf16 a[216:219], v[160:163], a[96:99], a[216:219]\n\t"
    "buffer_load_dwordx4 v[148:151], v230, s[8:11], s33 offen offset:1024 sc1\n\t"
    "v_mfma_f32_16x16x32_bf16 a[220:223], v[160:163], a[100:103], a[220:223]\n\t"
    "s_add_u32 m0, s70, 65536\n\t"
    "s_xor_b32 s66, s29, 768\n\t"
    "v_mfma_f32_16x16x32_bf16 a[224:227], v[160:163], a[104:107], a[224:227]\n\t"
    "v_mfma_f32_16x16x32_bf16 a[152:155], v[160:163], a[108:111], a[152:155]\n\t"
    "ds_read_b64_tr_b16 v[160:161], v209 offset:16384\n\t"
    "ds_read_b64_tr_b16 v[162:163], v209 offset:17408\n\t"
    "v_mfma_f32_16x16x32_bf16 v[48:51], v[164:167], a[92:95], v[48:51]\n\t"
    "buffer_load_dwordx4 v247, s[4:7], s66 offen offset:0 lds\n\t"
    "v_mfma_f32_16x16x32_bf16 v[52:55], v[164:167], a[96:99], v[52:55]\n\t"
    "v_mfma_f32_16x16x32_bf16 v[56:59], v[164:167], a[100:103], v[56:59]\n\t"
    "v_mfma_f32_16x16x32_bf16 v[60:63], v[164:167], a[104:107], v[60:63]\n\t"
    "v_mfma_f32_16x16x32_bf16 v[76:79], v[164:167], a[108:111], v[76:79]\n\t"
    "ds_read_b64_tr_b16 v[164:165], v215 offset:16384\n\t"
    "ds_read_b64_tr_b16 v[166:167], v215 offset:17408\n\t"
    "v_mfma_f32_16x16x32_bf16 a[228:231], v[168:171], a[92:95], a[228:231]\n\t"
    "buffer_load_dwordx4 v248, s[4:7], s66 offen offset:1024 lds\n\t"
    "v_mfma_f32_16x16x32_bf16 a[232:235], v[168:171], a[96:99], a[232:235]\n\t"
    "v_mfma_f32_16x16x32_bf16 a[236:239], v[168:171], a[100:103], a[236:239]\n\t"
    "v_mfma_f32_16x16x32_bf16 a[240:243], v[168:171], a[104:107], a[240:243]\n\t"
    "v_mfma_f32_16x16x32_bf16 a[156:159], v[168:171], a[108:111], a[156:159]\n\t"
    "ds_read_b64_tr_b16 v[168:169], v211 offset:32768\n\t"
    "ds_read_b64_tr_b16 v[170:171], v211 offset:33792\n\t"
    "v_mfma_f32_16x16x32_bf16 a[160:163], v[172:175], a[92:95], a[160:163]\n\t"
    "buffer_load_dwordx4 v249, s[4:7], s66 offen offset:2048 lds\n\t"
    "v_mfma_f32_16x16x32_bf16 a[164:167], v[172:175], a[96:99], a[164:167]\n\t"
    "v_mfma_f32_16x16x32_bf16 a[168:171], v[172:175], a[100:103], a[168:171]\n\t"
    "v_mfma_f32_16x16x32_bf16 a[172:175], v[172:175], a[104:107], a[172:175]\n\t"
    "v_mfma_f32_16x16x32_bf16 a[176:179], v[172:175], a[108:111], a[176:179]\n\t"
    "ds_read_b64_tr_b16 v[172:173], v217 offset:32768\n\t"
    "ds_read_b64_tr_b16 v[174:175], v217 offset:33792\n\t"
    "s_waitcnt vmcnt(28) lgkmcnt(4)\n\t"
    "v_mfma_f32_16x16x32_bf16 v[16:19], v[152:155], a[112:115], v[16:19]\n\t"
    "buffer_load_dwordx4 v250, s[4:7], s66 offen offset:3072 lds\n\t"
    "s_waitcnt vmcnt(28)\n\t"
    "v_mfma_f32_16x16x32_bf16 v[20:23], v[152:155], a[116:119], v[20:23]\n\t"
    "s_add_u32 m0, s70, 114688\n\t"
    "s_xor_b32 s66, s27, 0\n\t"
    "s_waitcnt vmcnt(27)\n\t"
    "v_mfma_f32_16x16x32_bf16 v[24:27], v[152:155], a[120:123], v[24:27]\n\t"
    "s_waitcnt vmcnt(26)\n\t"
    "v_mfma_f32_16x16x32_bf16 v[28:31], v[152:155], a[124:127], v[28:31]\n\t"
    "ds_read_b64_tr_b16 v[152:153], v221 offset:0\n\t"
    "ds_read_b64_tr_b16 v[154:155], v221 offset:512\n\t"
    "v_mfma_f32_16x16x32_bf16 a[196:199], v[156:159], a[112:115], a[196:199]\n\t"
    "v_mfma_f32_16x16x32_bf16 a[200:203], v[156:159], a[116:119], a[200:203]\n\t"
    "buffer_load_dwordx4 v247, s[4:7], s66 offen offset:0 lds\n\t"
    "v_mfma_f32_16x16x32_bf16 a[204:207], v[156:159], a[120:123], a[204:207]\n\t"
    "v_mfma_f32_16x16x32_bf16 a[208:211], v[156:159], a[124:127], a[208:211]\n\t"
    "ds_read_b64_tr_b16 v[156:157], v209 offset:2048\n\t"
    "ds_read_b64_tr_b16 v[158:159], v209 offset:3072\n\t"
    "v_mfma_f32_16x16x32_bf16 v[32:35], v[160:163], a[112:115], v[32:35]\n\t"
    "v_mfma_f32_16x16x32_bf16 v[36:39], v[160:163], a[116:119], v[36:39]\n\t"
    "v_mfma_f32_16x16x32_bf16 v[40:43], v[160:163], a[120:123], v[40:43]\n\t"
    "v_mfma_f32_16x16x32_bf16 v[44:47], v[160:163], a[124:127], v[44:47]\n\t"
    "buffer_load_dwordx4 v248, s[4:7], s66 offen offset:1024 lds\n\t"
    "v_mfma_f32_16x16x32_bf16 a[212:215], v[164:167], a[112:115], a[212:215]\n\t"
    "ds_read_b64_tr_b16 v[160:161], v215 offset:2048\n\t"
    "ds_read_b64_tr_b16 v[162:163], v215 offset:3072\n\t"
    "v_mfma_f32_16x16x32_bf16 a[216:219], v[164:167], a[116:119], a[216:219]\n\t"
    "v_mfma_f32_16x16x32_bf16 a[220:223], v[164:167], a[120:123], a[220:223]\n\t"
    "v_mfma_f32_16x16x32_bf16 a[224:227], v[164:167], a[124:127], a[224:227]\n\t"
    "ds_read_b64_tr_b16 v[164:165], v209 offset:18432\n\t"
    "ds_read_b64_tr_b16 v[166:167], v209 offset:19456\n\t"
    "s_waitcnt lgkmcnt(4)\n\t"
    "v_mfma_f32_16x16x32_bf16 v[48:51], v[168:171], a[112:115], v[48:51]\n\t"
    "v_mfma_f32_16x16x32_bf16 v[52:55], v[168:171], a[116:119], v[52:55]\n\t"
    "v_mfma_f32_16x16x32_bf16 v[56:59], v[168:171], a[120:123], v[56:59]\n\t"
    "buffer_load_dwordx4 v249, s[4:7], s66 offen offset:2048 lds\n\t"
    "v_mfma_f32_16x16x32_bf16 v[60:63], v[168:171], a[124:127], v[60:63]\n\t"
    "ds_read_b64_tr_b16 v[168:169], v215 offset:18432\n\t"
    "ds_read_b64_tr_b16 v[170:171], v215 offset:19456\n\t"
    "v_mfma_f32_16x16x32_bf16 a[228:231], v[172:175], a[112:115], a[228:231]\n\t"
    "v_mfma_f32_16x16x32_bf16 a[232:235], v[172:175], a[116:119], a[232:235]\n\t"
    "v_mfma_f32_16x16x32_bf16 a[236:239], v[172:175], a[120:123], a[236:239]\n\t"
    "v_mfma_f32_16x16x32_bf16 a[240:243], v[172:175], a[124:127], a[240:243]\n\t"
    "ds_read_b64_tr_b16 v[172:173], v211 offset:34816\n\t"
    "ds_read_b64_tr_b16 v[174:175], v211 offset:35840\n\t"
    "v_mfma_f32_16x16x32_bf16 a[160:163], v[152:155], a[112:115], a[160:163]\n\t"
    "v_mfma_f32_16x16x32_bf16 a[164:167], v[152:155], a[116:119], a[164:167]\n\t"
    "buffer_load_dwordx4 v250, s[4:7], s66 offen offset:3072 lds\n\t"
    "v_mfma_f32_16x16x32_bf16 a[168:171], v[152:155], a[120:123], a[168:171]\n\t"
    "s_add_u32 m0, s70, 131072\n\t"
    "s_xor_b32 s66, s28, 0\n\t"
    "v_mfma_f32_16x16x32_bf16 a[172:175], v[152:155], a[124:127], a[172:175]\n\t"
    "ds_read_b64_tr_b16 v[152:153], v217 offset:34816\n\t"
    "ds_read_b64_tr_b16 v[154:155], v217 offset:35840\n\t"
    "s_waitcnt vmcnt(29)\n\t"
    "v_mfma_f32_16x16x32_bf16 v[16:19], v[156:159], a[128:131], v[16:19]\n\t"
    "s_waitcnt vmcnt(28)\n\t"
    "v_mfma_f32_16x16x32_bf16 v[20:23], v[156:159], a[132:135], v[20:23]\n\t"
    "s_waitcnt vmcnt(27)\n\t"
    "v_mfma_f32_16x16x32_bf16 v[24:27], v[156:159], a[136:139], v[24:27]\n\t"
    "s_waitcnt vmcnt(26)\n\t"
    "v_mfma_f32_16x16x32_bf16 v[28:31], v[156:159], a[140:143], v[28:31]\n\t"
    "buffer_load_dwordx4 v247, s[4:7], s66 offen offset:0 lds\n\t"
    "s_waitcnt lgkmcnt(2)\n\t"
    "v_mfma_f32_16x16x32_bf16 a[196:199], v[160:163], a[128:131], a[196:199]\n\t"
    "ds_read_b64_tr_b16 v[156:157], v221 offset:1024\n\t"
    "ds_read_b64_tr_b16 v[158:159], v221 offset:1536\n\t"
    "v_mfma_f32_16x16x32_bf16 a[200:203], v[160:163], a[132:135], a[200:203]\n\t"
    "v_mfma_f32_16x16x32_bf16 a[204:207], v[160:163], a[136:139], a[204:207]\n\t"
    "v_mfma_f32_16x16x32_bf16 a[208:211], v[160:163], a[140:143], a[208:211]\n\t"
    "ds_read_b64_tr_b16 v[160:161], v220 offset:8192\n\t"
    "ds_read_b64_tr_b16 v[162:163], v220 offset:8704\n\t"
    "v_mfma_f32_16x16x32_bf16 v[32:35], v[164:167], a[128:131], v[32:35]\n\t"
    "v_mfma_f32_16x16x32_bf16 v[36:39], v[164:167], a[132:135], v[36:39]\n\t"
    "v_mfma_f32_16x16x32_bf16 v[40:43], v[164:167], a[136:139], v[40:43]\n\t"
    "buffer_load_dwordx4 v248, s[4:7], s66 offen offset:1024 lds\n\t"
    "v_mfma_f32_16x16x32_bf16 v[44:47], v[164:167], a[140:143], v[44:47]\n\t"
    "ds_read_b64_tr_b16 v[164:165], v220 offset:9216\n\t"
    "ds_read_b64_tr_b16 v[166:167], v220 offset:9728\n\t"
    "v_mfma_f32_16x16x32_bf16 a[212:215], v[168:171], a[128:131], a[212:215]\n\t"
    "v_mfma_f32_16x16x32_bf16 a[216:219], v[168:171], a[132:135], a[216:219]\n\t"
    "v_mfma_f32_16x16x32_bf16 a[220:223], v[168:171], a[136:139], a[220:223]\n\t"
    "v_mfma_f32_16x16x32_bf16 a[224:227], v[168:171], a[140:143], a[224:227]\n\t"
    "ds_read_b64_tr_b16 v[168:169], v221 offset:8192\n\t"
    "ds_read_b64_tr_b16 v[170:171], v221 offset:8704\n\t"
    "v_mfma_f32_16x16x32_bf16 v[48:51], v[172:175], a[128:131], v[48:51]\n\t"
    "v_mfma_f32_16x16x32_bf16 v[52:55], v[172:175], a[132:135], v[52:55]\n\t"
    "buffer_load_dwordx4 v249, s[4:7], s66 offen offset:2048 lds\n\t"
    "v_mfma_f32_16x16x32_bf16 v[56:59], v[172:175], a[136:139], v[56:59]\n\t"
    "v_mfma_f32_16x16x32_bf16 v[60:63], v[172:175], a[140:143], v[60:63]\n\t"
    "ds_read_b64_tr_b16 v[172:173], v221 offset:9216\n\t"
    "ds_read_b64_tr_b16 v[174:175], v221 offset:9728\n\t"
    "s_waitcnt lgkmcnt(4)\n\t"
    "v_mfma_f32_16x16x32_bf16 a[228:231], v[152:155], a[128:131], a[228:231]\n\t"
    "v_mfma_f32_16x16x32_bf16 a[232:235], v[152:155], a[132:135], a[232:235]\n\t"
    "v_mfma_f32_16x16x32_bf16 a[236:239], v[152:155], a[136:139], a[236:239]\n\t"
    "v_mfma_f32_16x16x32_bf16 a[240:243], v[152:155], a[140:143], a[240:243]\n\t"
    "buffer_load_dwordx4 v250, s[4:7], s66 offen offset:3072 lds\n\t"
    "v_mfma_f32_16x16x32_bf16 a[160:163], v[156:159], a[128:131], a[160:163]\n\t"
    "s_nop 0\n\t"
    "s_barrier\n\t"
    "v_mfma_f32_16x16x32_bf16 a[164:167], v[156:159], a[132:135], a[164:167]\n\t"
    "s_add_u32 m0, s70, 147456\n\t"
    "s_xor_b32 s66, s29, 0\n\t"
    "v_mfma_f32_16x16x32_bf16 a[168:171], v[156:159], a[136:139], a[168:171]\n\t"
    "ds_read_b64_tr_b16 v[152:153], v214 offset:49152\n\t"
    "ds_read_b64_tr_b16 v[154:155], v214 offset:50176\n\t"
    "v_mfma_f32_16x16x32_bf16 a[172:175], v[156:159], a[140:143], a[172:175]\n\t"
    "ds_read_b64_tr_b16 v[156:157], v214 offset:51200\n\t"
    "ds_read_b64_tr_b16 v[158:159], v214 offset:52224\n\t"
    "s_waitcnt vmcnt(29)\n\t"
    "v_mfma_f32_16x16x32_bf16 a[160:163], v[160:163], v[80:83], a[160:163]\n\t"
    "s_waitcnt vmcnt(28)\n\t"
    "v_mfma_f32_16x16x32_bf16 a[164:167], v[160:163], v[84:87], a[164:167]\n\t"
    "s_waitcnt vmcnt(27)\n\t"
    "v_mfma_f32_16x16x32_bf16 a[168:171], v[160:163], v[88:91], a[168:171]\n\t"
    "buffer_load_dwordx4 v247, s[4:7], s66 offen offset:0 lds\n\t"
    "s_waitcnt vmcnt(27)\n\t"
    "v_mfma_f32_16x16x32_bf16 a[172:175], v[160:163], v[92:95], a[172:175]\n\t"
    "s_waitcnt vmcnt(26)\n\t"
    "v_mfma_f32_16x16x32_bf16 a[176:179], v[160:163], v[96:99], a[176:179]\n\t"
    "ds_read_b64_tr_b16 v[160:161], v215 offset:49152\n\t"
    "ds_read_b64_tr_b16 v[162:163], v215 offset:50176\n\t"
    "s_waitcnt vmcnt(25)\n\t"
    "v_mfma_f32_16x16x32_bf16 a[160:163], v[164:167], v[100:103], a[160:163]\n\t"
    "s_waitcnt vmcnt(24)\n\t"
    "v_mfma_f32_16x16x32_bf16 a[164:167], v[164:167], v[104:107], a[164:167]\n\t"
    "s_waitcnt vmcnt(23)\n\t"
    "v_mfma_f32_16x16x32_bf16 a[168:171], v[164:167], v[108:111], a[168:171]\n\t"
    "s_waitcnt vmcnt(22)\n\t"
    "v_mfma_f32_16x16x32_bf16 a[172:175], v[164:167], v[112:115], a[172:175]\n\t"
    "s_waitcnt vmcnt(21)\n\t"
    "v_mfma_f32_16x16x32_bf16 a[176:179], v[164:167], v[116:119], a[176:179]\n\t"
    "buffer_load_dwordx4 v248, s[4:7], s66 offen offset:1024 lds\n\t"
    "s_waitcnt vmcnt(21) lgkmcnt(2)\n\t"
    "v_mfma_f32_16x16x32_bf16 a[160:163], v[168:171], v[120:123], a[160:163]\n\t"
    "ds_read_b64_tr_b16 v[164:165], v215 offset:51200\n\t"
    "ds_read_b64_tr_b16 v[166:167], v215 offset:52224\n\t"
    "s_waitcnt vmcnt(20)\n\t"
    "v_mfma_f32_16x16x32_bf16 a[164:167], v[168:171], v[124:127], a[164:167]\n\t"
    "s_waitcnt vmcnt(19)\n\t"
    "v_mfma_f32_16x16x32_bf16 a[168:171], v[168:171], v[128:131], a[168:171]\n\t"
    "s_waitcnt vmcnt(18)\n\t"
    "v_mfma_f32_16x16x32_bf16 a[172:175], v[168:171], v[132:135], a[172:175]\n\t"
    "ds_read_b64_tr_b16 v[168:169], v208 offset:49152\n\t"
    "ds_read_b64_tr_b16 v[170:171], v208 offset:50176\n\t"
    "s_waitcnt vmcnt(17)\n\t"
    "v_mfma_f32_16x16x32_bf16 a[160:163], v[172:175], v[136:139], a[160:163]\n\t"
    "s_waitcnt vmcnt(16)\n\t"
    "v_mfma_f32_16x16x32_bf16 a[164:167], v[172:175], v[140:143], a[164:167]\n\t"
    "buffer_load_dwordx4 v249, s[4:7], s66 offen offset:2048 lds\n\t"
    "s_waitcnt vmcnt(16)\n\t"
    "v_mfma_f32_16x16x32_bf16 a[168:171], v[172:175], v[144:147], a[168:171]\n\t"
    "s_waitcnt vmcnt(15)\n\t"
    "v_mfma_f32_16x16x32_bf16 a[172:175], v[172:175], v[148:151], a[172:175]\n\t"
    "ds_read_b64_tr_b16 v[172:173], v208 offset:51200\n\t"
    "ds_read_b64_tr_b16 v[174:175], v208 offset:52224\n\t"
    "v_mfma_f32_16x16x32_bf16 a[196:199], v[152:155], v[80:83], a[196:199]\n\t"
    "v_mfma_f32_16x16x32_bf16 a[200:203], v[152:155], v[84:87], a[200:203]\n\t"
    "ds_write_b128 v224, a[176:179] offset:24576\n\t"
    "v_mfma_f32_16x16x32_bf16 a[204:207], v[152:155], v[88:91], a[204:207]\n\t"
    "ds_write_b128 v245, a[160:163] offset:0\n\t"
    "v_mfma_f32_16x16x32_bf16 a[208:211], v[152:155], v[92:95], a[208:211]\n\t"
    "ds_write_b128 v245, a[164:167] offset:4096\n\t"
    "v_mfma_f32_16x16x32_bf16 a[148:151], v[152:155], v[96:99], a[148:151]\n\t"
    "buffer_load_dwordx4 v250, s[4:7], s66 offen offset:3072 lds\n\t"
    "v_mfma_f32_16x16x32_bf16 a[196:199], v[156:159], v[100:103], a[196:199]\n\t"
    "ds_write_b128 v245, a[168:171] offset:8192\n\t"
    "v_mfma_f32_16x16x32_bf16 a[200:203], v[156:159], v[104:107], a[200:203]\n\t"
    "ds_write_b128 v245, a[172:175] offset:12288\n\t"
    "v_mfma_f32_16x16x32_bf16 a[204:207], v[156:159], v[108:111], a[204:207]\n\t"
    "ds_read_b64_tr_b16 v[152:153], v209 offset:49152\n\t"
    "ds_read_b64_tr_b16 v[154:155], v209 offset:50176\n\t"
    "v_mfma_f32_16x16x32_bf16 a[208:211], v[156:159], v[112:115], a[208:211]\n\t"
    "v_mfma_f32_16x16x32_bf16 a[148:151], v[156:159], v[116:119], a[148:151]\n\t"
    "ds_read_b64_tr_b16 v[156:157], v209 offset:51200\n\t"
    "ds_read_b64_tr_b16 v[158:159], v209 offset:52224\n\t"
    "s_waitcnt lgkmcnt(7)\n\t"
    "v_mfma_f32_16x16x32_bf16 a[196:199], v[160:163], v[120:123], a[196:199]\n\t"
    "v_mfma_f32_16x16x32_bf16 a[200:203], v[160:163], v[124:127], a[200:203]\n\t"
    "v_mfma_f32_16x16x32_bf16 a[204:207], v[160:163], v[128:131], a[204:207]\n\t"
    "v_mfma_f32_16x16x32_bf16 a[208:211], v[160:163], v[132:135], a[208:211]\n\t"
    "ds_read_b64_tr_b16 v[160:161], v216 offset:16384\n\t"
    "ds_read_b64_tr_b16 v[162:163], v216 offset:17408\n\t"
    "v_mfma_f32_16x16x32_bf16 a[196:199], v[164:167], v[136:139], a[196:199]\n\t"
    "v_mfma_f32_16x16x32_bf16 a[200:203], v[164:167], v[140:143], a[200:203]\n\t"
    "v_mfma_f32_16x16x32_bf16 a[204:207], v[164:167], v[144:147], a[204:207]\n\t"
    "v_mfma_f32_16x16x32_bf16 a[208:211], v[164:167], v[148:151], a[208:211]\n\t"
    "ds_read_b64_tr_b16 v[164:165], v216 offset:18432\n\t"
    "ds_read_b64_tr_b16 v[166:167], v216 offset:19456\n\t"
    "v_mfma_f32_16x16x32_bf16 v[16:19], v[168:171], v[80:83], v[16:19]\n\t"
    "v_mfma_f32_16x16x32_bf16 v[20:23], v[168:171], v[84:87], v[20:23]\n\t"
    "ds_write_b128 v244, a[148:151] offset:16384\n\t"
    "v_mfma_f32_16x16x32_bf16 v[24:27], v[168:171], v[88:91], v[24:27]\n\t"
    "ds_write_b128 v224, a[196:199] offset:0\n\t"
    "v_mfma_f32_16x16x32_bf16 v[28:31], v[168:171], v[92:95], v[28:31]\n\t"
    "ds_write_b128 v224, a[200:203] offset:4096\n\t"
    "v_mfma_f32_16x16x32_bf16 v[68:71], v[168:171], v[96:99], v[68:71]\n\t"
    "ds_write_b128 v224, a[204:207] offset:8192\n\t"
    "v_mfma_f32_16x16x32_bf16 v[16:19], v[172:175], v[100:103], v[16:19]\n\t"
    "ds_write_b128 v224, a[208:211] offset:12288\n\t"
    "v_mfma_f32_16x16x32_bf16 v[20:23], v[172:175], v[104:107], v[20:23]\n\t"
    "ds_read_b64_tr_b16 v[168:169], v217 offset:16384\n\t"
    "ds_read_b64_tr_b16 v[170:171], v217 offset:17408\n\t"
    "v_mfma_f32_16x16x32_bf16 v[24:27], v[172:175], v[108:111], v[24:27]\n\t"
    "v_mfma_f32_16x16x32_bf16 v[28:31], v[172:175], v[112:115], v[28:31]\n\t"
    "v_mfma_f32_16x16x32_bf16 v[68:71], v[172:175], v[116:119], v[68:71]\n\t"
    "ds_read_b64_tr_b16 v[172:173], v217 offset:18432\n\t"
    "ds_read_b64_tr_b16 v[174:175], v217 offset:19456\n\t"
    "s_waitcnt lgkmcnt(7)\n\t"
    "v_mfma_f32_16x16x32_bf16 v[16:19], v[152:155], v[120:123], v[16:19]\n\t"
    "v_mfma_f32_16x16x32_bf16 v[20:23], v[152:155], v[124:127], v[20:23]\n\t"
    "v_mfma_f32_16x16x32_bf16 v[24:27], v[152:155], v[128:131], v[24:27]\n\t"
    "ds_write_b128 v231, v[68:71] offset:16384\n\t"
    "v_mfma_f32_16x16x32_bf16 v[28:31], v[152:155], v[132:135], v[28:31]\n\t"
    "ds_read_b64_tr_b16 v[152:153], v210 offset:16384\n\t"
    "ds_read_b64_tr_b16 v[154:155], v210 offset:17408\n\t"
    "v_mfma_f32_16x16x32_bf16 v[16:19], v[156:159], v[136:139], v[16:19]\n\t"
    "v_mfma_f32_16x16x32_bf16 v[20:23], v[156:159], v[140:143], v[20:23]\n\t"
    "v_mfma_f32_16x16x32_bf16 v[24:27], v[156:159], v[144:147], v[24:27]\n\t"
    "v_mfma_f32_16x16x32_bf16 v[28:31], v[156:159], v[148:151], v[28:31]\n\t"
    "ds_read_b64_tr_b16 v[156:157], v210 offset:18432\n\t"
    "ds_read_b64_tr_b16 v[158:159], v210 offset:19456\n\t"
    "v_mfma_f32_16x16x32_bf16 a[212:215], v[160:163], v[80:83], a[212:215]\n\t"
    "v_add_u32 v247, s89, v226\n\t"
    "v_add_u32 v248, s42, v247\n\t"
    "v_mfma_f32_16x16x32_bf16 a[216:219], v[160:163], v[84:87], a[216:219]\n\t"
    "s_waitcnt vmcnt(12) lgkmcnt(4)\n\t"
    "s_barrier\n\t"
    "ds_read_b128 v[192:195], v226 offset:24576\n\t"
    "ds_read_b128 v[196:199], v226 offset:25600\n\t"
    "ds_read_b128 v[200:203], v226 offset:26624\n\t"
    "ds_read_b128 v[204:207], v226 offset:27648\n\t"
    "ds_read_b128 v[176:179], v225 offset:0\n\t"
    "v_mfma_f32_16x16x32_bf16 a[220:223], v[160:163], v[88:91], a[220:223]\n\t"
    "ds_read_b128 v[180:183], v225 offset:4096\n\t"
    "ds_read_b128 v[184:187], v225 offset:8192\n\t"
    "v_mfma_f32_16x16x32_bf16 a[224:227], v[160:163], v[92:95], a[224:227]\n\t"
    "ds_read_b128 v[188:191], v225 offset:12288\n\t"
    "v_mfma_f32_16x16x32_bf16 a[152:155], v[160:163], v[96:99], a[152:155]\n\t"
    "ds_read_b64_tr_b16 v[160:161], v211 offset:16384\n\t"
    "ds_read_b64_tr_b16 v[162:163], v211 offset:17408\n\t"
    "v_mfma_f32_16x16x32_bf16 a[212:215], v[164:167], v[100:103], a[212:215]\n\t"
    "v_mfma_f32_16x16x32_bf16 a[216:219], v[164:167], v[104:107], a[216:219]\n\t"
    "v_mfma_f32_16x16x32_bf16 a[220:223], v[164:167], v[108:111], a[220:223]\n\t"
    "v_mfma_f32_16x16x32_bf16 a[224:227], v[164:167], v[112:115], a[224:227]\n\t"
    "v_mfma_f32_16x16x32_bf16 a[152:155], v[164:167], v[116:119], a[152:155]\n\t"
    "s_waitcnt lgkmcnt(5)\n\t"
    "v_add_f32 v192, v192, v196\n\t"
    "v_add_f32 v193, v193, v197\n\t"
    "v_add_f32 v194, v194, v198\n\t"
    "v_add_f32 v195, v195, v199\n\t"
    "v_add_f32 v200, v200, v204\n\t"
    "v_add_f32 v201, v201, v205\n\t"
    "v_add_f32 v202, v202, v206\n\t"
    "v_add_f32 v203, v203, v207\n\t"
    "v_fma_f32 v192, v200, 1.0, v192\n\t"
    "v_fma_f32 v193, v201, 1.0, v193\n\t"
    "v_fma_f32 v194, v202, 1.0, v194\n\t"
    "v_fma_f32 v195, v203, 1.0, v195\n\t"
    "v_cvt_pk_bf16_f32 v242, v192, v193\n\t"
    "v_mfma_f32_16x16x32_bf16 a[212:215], v[168:171], v[120:123], a[212:215]\n\t"
    "v_cvt_pk_bf16_f32 v243, v194, v195\n\t"
    "ds_read_b128 v[192:195], v247 offset:0\n\t"
    "v_mfma_f32_16x16x32_bf16 a[216:219], v[168:171], v[124:127], a[216:219]\n\t"
    "ds_read_b128 v[196:199], v247 offset:1024\n\t"
    "ds_read_b128 v[200:203], v248 offset:0\n\t"
    "v_mfma_f32_16x16x32_bf16 a[220:223], v[168:171], v[128:131], a[220:223]\n\t"
    "ds_read_b128 v[204:207], v248 offset:1024\n\t"
    "v_add_f32 v16, v16, v176\n\t"
    "v_mfma_f32_16x16x32_bf16 a[224:227], v[168:171], v[132:135], a[224:227]\n\t"
    "v_add_f32 v17, v17, v177\n\t"
    "v_add_f32 v18, v18, v178\n\t"
    "v_mfma_f32_16x16x32_bf16 a[212:215], v[172:175], v[136:139], a[212:215]\n\t"
    "v_add_f32 v19, v19, v179\n\t"
    "v_cvt_pk_bf16_f32 v232, v16, v17\n\t"
    "v_mfma_f32_16x16x32_bf16 a[216:219], v[172:175], v[140:143], a[216:219]\n\t"
    "v_cvt_pk_bf16_f32 v233, v18, v19\n\t"
    "s_waitcnt lgkmcnt(4)\n\t"
    "v_add_f32 v20, v20, v180\n\t"
    "v_mfma_f32_16x16x32_bf16 a[220:223], v[172:175], v[144:147], a[220:223]\n\t"
    "v_add_f32 v21, v21, v181\n\t"
    "v_add_f32 v22, v22, v182\n\t"
    "v_mfma_f32_16x16x32_bf16 a[224:227], v[172:175], v[148:151], a[224:227]\n\t"
    "v_add_f32 v23, v23, v183\n\t"
    "v_cvt_pk_bf16_f32 v234, v20, v21\n\t"
    "v_mfma_f32_16x16x32_bf16 v[32:35], v[152:155], v[80:83], v[32:35]\n\t"
    "v_cvt_pk_bf16_f32 v235, v22, v23\n\t"
    "buffer_store_dwordx4 v[232:235], v226, s[8:11], s67 offen offset:0 sc1\n\t"
    "v_mfma_f32_16x16x32_bf16 v[36:39], v[152:155], v[84:87], v[36:39]\n\t"
    "v_add_f32 v24, v24, v184\n\t"
    "v_add_f32 v25, v25, v185\n\t"
    "v_mfma_f32_16x16x32_bf16 v[40:43], v[152:155], v[88:91], v[40:43]\n\t"
    "v_add_f32 v26, v26, v186\n\t"
    "v_add_f32 v27, v27, v187\n\t"
    "v_mfma_f32_16x16x32_bf16 v[44:47], v[152:155], v[92:95], v[44:47]\n\t"
    "v_cvt_pk_bf16_f32 v236, v24, v25\n\t"
    "v_cvt_pk_bf16_f32 v237, v26, v27\n\t"
    "v_mfma_f32_16x16x32_bf16 v[72:75], v[152:155], v[96:99], v[72:75]\n\t"
    "ds_read_b64_tr_b16 v[164:165], v211 offset:18432\n\t"
    "v_add_f32 v28, v28, v188\n\t"
    "v_mfma_f32_16x16x32_bf16 v[32:35], v[156:159], v[100:103], v[32:35]\n\t"
    "ds_read_b64_tr_b16 v[166:167], v211 offset:19456\n\t"
    "v_add_f32 v29, v29, v189\n\t"
    "v_mfma_f32_16x16x32_bf16 v[36:39], v[156:159], v[104:107], v[36:39]\n\t"
    "v_add_f32 v30, v30, v190\n\t"
    "v_add_f32 v31, v31, v191\n\t"
    "v_mfma_f32_16x16x32_bf16 v[40:43], v[156:159], v[108:111], v[40:43]\n\t"
    "v_cvt_pk_bf16_f32 v238, v28, v29\n\t"
    "v_cvt_pk_bf16_f32 v239, v30, v31\n\t"
    "v_mfma_f32_16x16x32_bf16 v[44:47], v[156:159], v[112:115], v[44:47]\n\t"
    "buffer_store_dwordx4 v[236:239], v226, s[8:11], s67 offen offset:1024 sc1\n\t"
    "v_mfma_f32_16x16x32_bf16 v[72:75], v[156:159], v[116:119], v[72:75]\n\t"
    "ds_read_b64_tr_b16 v[168:169], v216 offset:0\n\t"
    "ds_read_b64_tr_b16 v[170:171], v216 offset:1024\n\t"
    "ds_write_b128 v244, a[152:155] offset:49152\n\t"
    "s_waitcnt lgkmcnt(5)\n\t"
    "v_add_f32 v192, v192, v196\n\t"
    "v_mfma_f32_16x16x32_bf16 v[32:35], v[160:163], v[120:123], v[32:35]\n\t"
    "v_add_f32 v193, v193, v197\n\t"
    "ds_write_b128 v224, a[212:215] offset:32768\n\t"
    "v_add_f32 v194, v194, v198\n\t"
    "v_mfma_f32_16x16x32_bf16 v[36:39], v[160:163], v[124:127], v[36:39]\n\t"
    "v_add_f32 v195, v195, v199\n\t"
    "ds_write_b128 v224, a[216:219] offset:36864\n\t"
    "v_add_f32 v200, v200, v204\n\t"
    "v_mfma_f32_16x16x32_bf16 v[40:43], v[160:163], v[128:131], v[40:43]\n\t"
    "v_add_f32 v201, v201, v205\n\t"
    "ds_write_b128 v224, a[220:223] offset:40960\n\t"
    "v_add_f32 v202, v202, v206\n\t"
    "v_mfma_f32_16x16x32_bf16 v[44:47], v[160:163], v[132:135], v[44:47]\n\t"
    "v_add_f32 v203, v203, v207\n\t"
    "ds_write_b128 v224, a[224:227] offset:45056\n\t"
    "v_fma_f32 v192, v200, s41, v192\n\t"
    "s_waitcnt lgkmcnt(7)\n\t"
    "v_mfma_f32_16x16x32_bf16 v[32:35], v[164:167], v[136:139], v[32:35]\n\t"
    "ds_read_b64_tr_b16 v[172:173], v216 offset:2048\n\t"
    "v_fma_f32 v193, v201, s41, v193\n\t"
    "ds_read_b64_tr_b16 v[174:175], v216 offset:3072\n\t"
    "v_fma_f32 v194, v202, s41, v194\n\t"
    "v_mfma_f32_16x16x32_bf16 v[36:39], v[164:167], v[140:143], v[36:39]\n\t"
    "v_fma_f32 v195, v203, s41, v195\n\t"
    "v_cvt_pk_bf16_f32 v240, v192, v193\n\t"
    "v_mfma_f32_16x16x32_bf16 v[40:43], v[164:167], v[144:147], v[40:43]\n\t"
    "v_cvt_pk_bf16_f32 v241, v194, v195\n\t"
    "buffer_store_dwordx4 v[240:243], v226, s[8:11], s67 offen offset:2048 sc1\n\t"
    "v_mfma_f32_16x16x32_bf16 v[44:47], v[164:167], v[148:151], v[44:47]\n\t"
    "ds_read_b64_tr_b16 v[152:153], v217 offset:0\n\t"
    "ds_read_b64_tr_b16 v[154:155], v217 offset:1024\n\t"
    "s_waitcnt lgkmcnt(6)\n\t"
    "v_mfma_f32_16x16x32_bf16 a[228:231], v[168:171], v[80:83], a[228:231]\n\t"
    "ds_read_b64_tr_b16 v[156:157], v217 offset:2048\n\t"
    "ds_read_b64_tr_b16 v[158:159], v217 offset:3072\n\t"
    "v_mfma_f32_16x16x32_bf16 a[232:235], v[168:171], v[84:87], a[232:235]\n\t"
    "ds_write_b128 v231, v[72:75] offset:49152\n\t"
    "v_mfma_f32_16x16x32_bf16 a[236:239], v[168:171], v[88:91], a[236:239]\n\t"
    "s_waitcnt vmcnt(3) lgkmcnt(0)\n\t"
    "s_barrier\n\t"
    "v_mfma_f32_16x16x32_bf16 a[240:243], v[168:171], v[92:95], a[240:243]\n\t"
    "v_add_u32 v247, s90, v226\n\t"
    "v_add_u32 v248, s42, v247\n\t"
    "v_mfma_f32_16x16x32_bf16 a[156:159], v[168:171], v[96:99], a[156:159]\n\t"
    "ds_read_b128 v[192:195], v247 offset:0\n\t"
    "ds_read_b64_tr_b16 v[160:161], v210 offset:0\n\t"
    "v_mfma_f32_16x16x32_bf16 a[228:231], v[172:175], v[100:103], a[228:231]\n\t"
    "ds_read_b128 v[196:199], v247 offset:1024\n\t"
    "ds_read_b64_tr_b16 v[162:163], v210 offset:1024\n\t"
    "v_mfma_f32_16x16x32_bf16 a[232:235], v[172:175], v[104:107], a[232:235]\n\t"
    "ds_read_b128 v[200:203], v248 offset:0\n\t"
    "ds_read_b128 v[204:207], v248 offset:1024\n\t"
    "v_mfma_f32_16x16x32_bf16 a[236:239], v[172:175], v[108:111], a[236:239]\n\t"
    "ds_read_b128 v[176:179], v225 offset:32768\n\t"
    "ds_read_b128 v[180:183], v225 offset:36864\n\t"
    "v_mfma_f32_16x16x32_bf16 a[240:243], v[172:175], v[112:115], a[240:243]\n\t"
    "ds_read_b128 v[184:187], v225 offset:40960\n\t"
    "ds_read_b128 v[188:191], v225 offset:45056\n\t"
    "v_mfma_f32_16x16x32_bf16 a[156:159], v[172:175], v[116:119], a[156:159]\n\t"
    "ds_read_b64_tr_b16 v[164:165], v210 offset:2048\n\t"
    "ds_read_b64_tr_b16 v[166:167], v210 offset:3072\n\t"
    "v_mfma_f32_16x16x32_bf16 a[228:231], v[152:155], v[120:123], a[228:231]\n\t"
    "ds_read_b64_tr_b16 v[168:169], v211 offset:0\n\t"
    "ds_read_b64_tr_b16 v[170:171], v211 offset:1024\n\t"
    "v_mfma_f32_16x16x32_bf16 a[232:235], v[152:155], v[124:127], a[232:235]\n\t"
    "ds_read_b64_tr_b16 v[172:173], v211 offset:2048\n\t"
    "ds_read_b64_tr_b16 v[174:175], v211 offset:3072\n\t"
    "v_mfma_f32_16x16x32_bf16 a[236:239], v[152:155], v[128:131], a[236:239]\n\t"
    "v_mfma_f32_16x16x32_bf16 a[240:243], v[152:155], v[132:135], a[240:243]\n\t"
    "ds_read_b64_tr_b16 v[152:153], v216 offset:49152\n\t"
    "ds_read_b64_tr_b16 v[154:155], v216 offset:50176\n\t"
    "v_mfma_f32_16x16x32_bf16 a[228:231], v[156:159], v[136:139], a[228:231]\n\t"
    "s_waitcnt lgkmcnt(12)\n\t"
    "v_add_f32 v192, v192, v196\n\t"
    "v_add_f32 v193, v193, v197\n\t"
    "v_add_f32 v194, v194, v198\n\t"
    "v_add_f32 v195, v195, v199\n\t"
    "v_add_f32 v200, v200, v204\n\t"
    "v_mfma_f32_16x16x32_bf16 a[232:235], v[156:159], v[140:143], a[232:235]\n\t"
    "v_add_f32 v201, v201, v205\n\t"
    "v_add_f32 v202, v202, v206\n\t"
    "v_mfma_f32_16x16x32_bf16 a[236:239], v[156:159], v[144:147], a[236:239]\n\t"
    "v_add_f32 v203, v203, v207\n\t"
    "v_fma_f32 v192, v200, s41, v192\n\t"
    "v_mfma_f32_16x16x32_bf16 a[240:243], v[156:159], v[148:151], a[240:243]\n\t"
    "v_fma_f32 v193, v201, s41, v193\n\t"
    "v_fma_f32 v194, v202, s41, v194\n\t"
    "v_mfma_f32_16x16x32_bf16 v[48:51], v[160:163], v[80:83], v[48:51]\n\t"
    "v_fma_f32 v195, v203, s41, v195\n\t"
    "v_cvt_pk_bf16_f32 v240, v192, v193\n\t"
    "v_mfma_f32_16x16x32_bf16 v[52:55], v[160:163], v[84:87], v[52:55]\n\t"
    "v_cvt_pk_bf16_f32 v241, v194, v195\n\t"
    "s_waitcnt lgkmcnt(2)\n\t"
    "v_add_f32 v32, v32, v176\n\t"
    "v_mfma_f32_16x16x32_bf16 v[56:59], v[160:163], v[88:91], v[56:59]\n\t"
    "v_add_f32 v33, v33, v177\n\t"
    "v_add_f32 v34, v34, v178\n\t"
    "v_mfma_f32_16x16x32_bf16 v[60:63], v[160:163], v[92:95], v[60:63]\n\t"
    "v_add_f32 v35, v35, v179\n\t"
    "v_cvt_pk_bf16_f32 v232, v32, v33\n\t"
    "v_mfma_f32_16x16x32_bf16 v[76:79], v[160:163], v[96:99], v[76:79]\n\t"
    "v_cvt_pk_bf16_f32 v233, v34, v35\n\t"
    "v_add_f32 v36, v36, v180\n\t"
    "v_mfma_f32_16x16x32_bf16 v[48:51], v[164:167], v[100:103], v[48:51]\n\t"
    "v_add_f32 v37, v37, v181\n\t"
    "v_add_f32 v38, v38, v182\n\t"
    "v_mfma_f32_16x16x32_bf16 v[52:55], v[164:167], v[104:107], v[52:55]\n\t"
    "v_add_f32 v39, v39, v183\n\t"
    "v_cvt_pk_bf16_f32 v234, v36, v37\n\t"
    "v_mfma_f32_16x16x32_bf16 v[56:59], v[164:167], v[108:111], v[56:59]\n\t"
    "v_cvt_pk_bf16_f32 v235, v38, v39\n\t"
    "buffer_store_dwordx4 v[232:235], v226, s[8:11], s68 offen offset:0 sc1\n\t"
    "v_mfma_f32_16x16x32_bf16 v[60:63], v[164:167], v[112:115], v[60:63]\n\t"
    "v_add_f32 v40, v40, v184\n\t"
    "v_add_f32 v41, v41, v185\n\t"
    "v_mfma_f32_16x16x32_bf16 v[76:79], v[164:167], v[116:119], v[76:79]\n\t"
    "ds_write_b128 v244, a[156:159] offset:16384\n\t"
    "v_mfma_f32_16x16x32_bf16 v[48:51], v[168:171], v[120:123], v[48:51]\n\t"
    "v_add_f32 v42, v42, v186\n\t"
    "ds_write_b128 v224, a[228:231] offset:0\n\t"
    "v_add_f32 v43, v43, v187\n\t"
    "v_mfma_f32_16x16x32_bf16 v[52:55], v[168:171], v[124:127], v[52:55]\n\t"
    "v_cvt_pk_bf16_f32 v236, v40, v41\n\t"
    "ds_write_b128 v224, a[232:235] offset:4096\n\t"
    "v_cvt_pk_bf16_f32 v237, v42, v43\n\t"
    "v_mfma_f32_16x16x32_bf16 v[56:59], v[168:171], v[128:131], v[56:59]\n\t"
    "v_add_f32 v44, v44, v188\n\t"
    "ds_write_b128 v224, a[236:239] offset:8192\n\t"
    "v_add_f32 v45, v45, v189\n\t"
    "v_mfma_f32_16x16x32_bf16 v[60:63], v[168:171], v[132:135], v[60:63]\n\t"
    "v_add_f32 v46, v46, v190\n\t"
    "ds_write_b128 v224, a[240:243] offset:12288\n\t"
    "v_add_f32 v47, v47, v191\n\t"
    "v_mfma_f32_16x16x32_bf16 v[48:51], v[172:175], v[136:139], v[48:51]\n\t"
    "ds_read_b64_tr_b16 v[156:157], v216 offset:51200\n\t"
    "v_cvt_pk_bf16_f32 v238, v44, v45\n\t"
    "ds_read_b64_tr_b16 v[158:159], v216 offset:52224\n\t"
    "v_cvt_pk_bf16_f32 v239, v46, v47\n\t"
    "v_mfma_f32_16x16x32_bf16 v[52:55], v[172:175], v[140:143], v[52:55]\n\t"
    "buffer_store_dwordx4 v[236:239], v226, s[8:11], s68 offen offset:1024 sc1\n\t"
    "v_mfma_f32_16x16x32_bf16 v[56:59], v[172:175], v[144:147], v[56:59]\n\t"
    "buffer_store_dwordx4 v[240:243], v226, s[8:11], s68 offen offset:2048 sc1\n\t"
    "v_mfma_f32_16x16x32_bf16 v[60:63], v[172:175], v[148:151], v[60:63]\n\t"
    "ds_read_b64_tr_b16 v[160:161], v217 offset:49152\n\t"
    "ds_read_b64_tr_b16 v[162:163], v217 offset:50176\n\t"
    "s_waitcnt lgkmcnt(6)\n\t"
    "v_mfma_f32_16x16x32_bf16 a[180:183], v[152:155], a[0:3], a[180:183]\n\t"
    "ds_read_b64_tr_b16 v[164:165], v217 offset:51200\n\t"
    "ds_read_b64_tr_b16 v[166:167], v217 offset:52224\n\t"
    "v_mfma_f32_16x16x32_bf16 a[184:187], v[152:155], a[4:7], a[184:187]\n\t"
    "ds_write_b128 v231, v[76:79] offset:16384\n\t"
    "v_mfma_f32_16x16x32_bf16 a[188:191], v[152:155], a[8:11], a[188:191]\n\t"
    "s_waitcnt lgkmcnt(0)\n\t"
    "s_barrier\n\t"
    "v_mfma_f32_16x16x32_bf16 a[192:195], v[152:155], a[12:15], a[192:195]\n\t"
    "v_add_u32 v247, s91, v226\n\t"
    "v_add_u32 v248, s42, v247\n\t"
    "v_mfma_f32_16x16x32_bf16 a[144:147], v[152:155], a[16:19], a[144:147]\n\t"
    "ds_read_b128 v[192:195], v247 offset:0\n\t"
    "ds_read_b64_tr_b16 v[168:169], v218 offset:0\n\t"
    "v_mfma_f32_16x16x32_bf16 a[180:183], v[156:159], a[20:23], a[180:183]\n\t"
    "ds_read_b128 v[196:199], v247 offset:1024\n\t"
    "ds_read_b64_tr_b16 v[170:171], v218 offset:1024\n\t"
    "v_mfma_f32_16x16x32_bf16 a[184:187], v[156:159], a[24:27], a[184:187]\n\t"
    "ds_read_b128 v[200:203], v248 offset:0\n\t"
    "ds_read_b128 v[204:207], v248 offset:1024\n\t"
    "v_mfma_f32_16x16x32_bf16 a[188:191], v[156:159], a[28:31], a[188:191]\n\t"
    "ds_read_b128 v[176:179], v225 offset:0\n\t"
    "ds_read_b128 v[180:183], v225 offset:4096\n\t"
    "v_mfma_f32_16x16x32_bf16 a[192:195], v[156:159], a[32:35], a[192:195]\n\t"
    "ds_read_b128 v[184:187], v225 offset:8192\n\t"
    "ds_read_b128 v[188:191], v225 offset:12288\n\t"
    "v_mfma_f32_16x16x32_bf16 a[144:147], v[156:159], a[36:39], a[144:147]\n\t"
    "ds_read_b64_tr_b16 v[172:173], v218 offset:2048\n\t"
    "ds_read_b64_tr_b16 v[174:175], v218 offset:3072\n\t"
    "v_mfma_f32_16x16x32_bf16 a[180:183], v[160:163], a[40:43], a[180:183]\n\t"
    "ds_read_b64_tr_b16 v[152:153], v219 offset:0\n\t"
    "ds_read_b64_tr_b16 v[154:155], v219 offset:1024\n\t"
    "v_mfma_f32_16x16x32_bf16 a[184:187], v[160:163], a[44:47], a[184:187]\n\t"
    "ds_read_b64_tr_b16 v[156:157], v219 offset:2048\n\t"
    "ds_read_b64_tr_b16 v[158:159], v219 offset:3072\n\t"
    "v_mfma_f32_16x16x32_bf16 a[188:191], v[160:163], a[48:51], a[188:191]\n\t"
    "v_mfma_f32_16x16x32_bf16 a[192:195], v[160:163], a[52:55], a[192:195]\n\t"
    "ds_read_b64_tr_b16 v[160:161], v218 offset:16384\n\t"
    "ds_read_b64_tr_b16 v[162:163], v218 offset:17408\n\t"
    "v_mfma_f32_16x16x32_bf16 a[180:183], v[164:167], a[56:59], a[180:183]\n\t"
    "s_waitcnt lgkmcnt(12)\n\t"
    "v_add_f32 v192, v192, v196\n\t"
    "v_add_f32 v193, v193, v197\n\t"
    "v_add_f32 v194, v194, v198\n\t"
    "v_add_f32 v195, v195, v199\n\t"
    "v_add_f32 v200, v200, v204\n\t"
    "v_mfma_f32_16x16x32_bf16 a[184:187], v[164:167], a[60:63], a[184:187]\n\t"
    "v_add_f32 v201, v201, v205\n\t"
    "v_add_f32 v202, v202, v206\n\t"
    "v_mfma_f32_16x16x32_bf16 a[188:191], v[164:167], a[64:67], a[188:191]\n\t"
    "v_add_f32 v203, v203, v207\n\t"
    "v_fma_f32 v192, v200, s41, v192\n\t"
    "v_mfma_f32_16x16x32_bf16 a[192:195], v[164:167], a[68:71], a[192:195]\n\t"
    "v_fma_f32 v193, v201, s41, v193\n\t"
    "v_fma_f32 v194, v202, s41, v194\n\t"
    "v_mfma_f32_16x16x32_bf16 a[180:183], v[168:171], a[72:75], a[180:183]\n\t"
    "v_fma_f32 v195, v203, s41, v195\n\t"
    "v_cvt_pk_bf16_f32 v240, v192, v193\n\t"
    "v_mfma_f32_16x16x32_bf16 a[184:187], v[168:171], a[76:79], a[184:187]\n\t"
    "v_cvt_pk_bf16_f32 v241, v194, v195\n\t"
    "s_waitcnt lgkmcnt(2)\n\t"
    "v_add_f32 v48, v48, v176\n\t"
    "v_mfma_f32_16x16x32_bf16 a[188:191], v[168:171], a[80:83], a[188:191]\n\t"
    "v_add_f32 v49, v49, v177\n\t"
    "v_add_f32 v50, v50, v178\n\t"
    "v_mfma_f32_16x16x32_bf16 a[192:195], v[168:171], a[84:87], a[192:195]\n\t"
    "v_add_f32 v51, v51, v179\n\t"
    "v_cvt_pk_bf16_f32 v232, v48, v49\n\t"
    "v_mfma_f32_16x16x32_bf16 a[144:147], v[168:171], a[88:91], a[144:147]\n\t"
    "v_cvt_pk_bf16_f32 v233, v50, v51\n\t"
    "v_add_f32 v52, v52, v180\n\t"
    "v_mfma_f32_16x16x32_bf16 a[180:183], v[172:175], a[92:95], a[180:183]\n\t"
    "v_add_f32 v53, v53, v181\n\t"
    "v_add_f32 v54, v54, v182\n\t"
    "v_mfma_f32_16x16x32_bf16 a[184:187], v[172:175], a[96:99], a[184:187]\n\t"
    "v_add_f32 v55, v55, v183\n\t"
    "v_cvt_pk_bf16_f32 v234, v52, v53\n\t"
    "v_mfma_f32_16x16x32_bf16 a[188:191], v[172:175], a[100:103], a[188:191]\n\t"
    "v_cvt_pk_bf16_f32 v235, v54, v55\n\t"
    "buffer_store_dwordx4 v[232:235], v226, s[8:11], s69 offen offset:0 sc1\n\t"
    "v_mfma_f32_16x16x32_bf16 a[192:195], v[172:175], a[104:107], a[192:195]\n\t"
    "v_add_f32 v56, v56, v184\n\t"
    "v_add_f32 v57, v57, v185\n\t"
    "v_mfma_f32_16x16x32_bf16 a[144:147], v[172:175], a[108:111], a[144:147]\n\t"
    "v_add_f32 v58, v58, v186\n\t"
    "v_add_f32 v59, v59, v187\n\t"
    "v_mfma_f32_16x16x32_bf16 a[180:183], v[152:155], a[112:115], a[180:183]\n\t"
    "v_cvt_pk_bf16_f32 v236, v56, v57\n\t"
    "v_cvt_pk_bf16_f32 v237, v58, v59\n\t"
    "v_mfma_f32_16x16x32_bf16 a[184:187], v[152:155], a[116:119], a[184:187]\n\t"
    "v_add_f32 v60, v60, v188\n\t"
    "v_add_f32 v61, v61, v189\n\t"
    "v_mfma_f32_16x16x32_bf16 a[188:191], v[152:155], a[120:123], a[188:191]\n\t"
    "v_add_f32 v62, v62, v190\n\t"
    "v_add_f32 v63, v63, v191\n\t"
    "v_mfma_f32_16x16x32_bf16 a[192:195], v[152:155], a[124:127], a[192:195]\n\t"
    "ds_read_b64_tr_b16 v[164:165], v218 offset:18432\n\t"
    "v_cvt_pk_bf16_f32 v238, v60, v61\n\t"
    "v_mfma_f32_16x16x32_bf16 a[180:183], v[156:159], a[128:131], a[180:183]\n\t"
    "ds_read_b64_tr_b16 v[166:167], v218 offset:19456\n\t"
    "v_cvt_pk_bf16_f32 v239, v62, v63\n\t"
    "v_mfma_f32_16x16x32_bf16 a[184:187], v[156:159], a[132:135], a[184:187]\n\t"
    "buffer_store_dwordx4 v[236:239], v226, s[8:11], s69 offen offset:1024 sc1\n\t"
    "v_mfma_f32_16x16x32_bf16 a[188:191], v[156:159], a[136:139], a[188:191]\n\t"
    "buffer_store_dwordx4 v[240:243], v226, s[8:11], s69 offen offset:2048 sc1\n\t"
    "v_mfma_f32_16x16x32_bf16 a[192:195], v[156:159], a[140:143], a[192:195]\n\t"
    "ds_read_b64_tr_b16 v[168:169], v219 offset:16384\n\t"
    "ds_read_b64_tr_b16 v[170:171], v219 offset:17408\n\t"
    "s_waitcnt lgkmcnt(4)\n\t"
    "v_mfma_f32_16x16x32_bf16 a[180:183], v[160:163], v[80:83], a[180:183]\n\t"
    "ds_read_b64_tr_b16 v[172:173], v219 offset:18432\n\t"
    "ds_read_b64_tr_b16 v[174:175], v219 offset:19456\n\t"
    "v_mfma_f32_16x16x32_bf16 a[184:187], v[160:163], v[84:87], a[184:187]\n\t"
    "ds_read_b64_tr_b16 v[152:153], v210 offset:49152\n\t"
    "ds_read_b64_tr_b16 v[154:155], v210 offset:50176\n\t"
    "v_mfma_f32_16x16x32_bf16 a[188:191], v[160:163], v[88:91], a[188:191]\n\t"
    "ds_read_b64_tr_b16 v[156:157], v210 offset:51200\n\t"
    "ds_read_b64_tr_b16 v[158:159], v210 offset:52224\n\t"
    "v_mfma_f32_16x16x32_bf16 a[192:195], v[160:163], v[92:95], a[192:195]\n\t"
    "v_and_b32 v249, 15, v222\n\t"
    "v_mul_u32_u24 v249, 2304, v249\n\t"
    "v_mfma_f32_16x16x32_bf16 a[144:147], v[160:163], v[96:99], a[144:147]\n\t"
    "ds_read_b64_tr_b16 v[160:161], v212 offset:0\n\t"
    "ds_read_b64_tr_b16 v[162:163], v212 offset:1024\n\t"
    "s_waitcnt lgkmcnt(10)\n\t"
    "v_mfma_f32_16x16x32_bf16 a[180:183], v[164:167], v[100:103], a[180:183]\n\t"
    "v_lshrrev_b32 v250, 4, v222\n\t"
    "v_lshl_add_u32 v249, v250, 3, v249\n\t"
    "v_mfma_f32_16x16x32_bf16 a[184:187], v[164:167], v[104:107], a[184:187]\n\t"
    "v_and_b32 v52, 1, v222\n\t"
    "v_lshlrev_b32 v52, 4, v52\n\t"
    "v_mfma_f32_16x16x32_bf16 a[188:191], v[164:167], v[108:111], a[188:191]\n\t"
    "v_mov_b32 v56, 0x3f80\n\t"
    "v_lshlrev_b32 v52, v52, v56\n\t"
    "v_mfma_f32_16x16x32_bf16 a[192:195], v[164:167], v[112:115], a[192:195]\n\t"
    "v_bfe_u32 v53, v222, 1, 3\n\t"
    "v_lshrrev_b32 v56, 4, v222\n\t"
    "v_mfma_f32_16x16x32_bf16 a[144:147], v[164:167], v[116:119], a[144:147]\n\t"
    "ds_read_b64_tr_b16 v[164:165], v212 offset:2048\n\t"
    "ds_read_b64_tr_b16 v[166:167], v212 offset:3072\n\t"
    "s_waitcnt lgkmcnt(5)\n\t"
    "v_mfma_f32_16x16x32_bf16 a[180:183], v[168:171], v[120:123], a[180:183]\n\t"
    "v_lshlrev_b32 v56, 1, v56\n\t"
    "v_sub_u32 v53, v53, v56\n\t"
    "v_mfma_f32_16x16x32_bf16 a[184:187], v[168:171], v[124:127], a[184:187]\n\t"
    "v_mov_b32 v56, 0\n\t"
    "v_mov_b32 v57, 0\n\t"
    "v_mfma_f32_16x16x32_bf16 a[188:191], v[168:171], v[128:131], a[188:191]\n\t"
    "v_cmp_eq_u32 vcc, 0, v53\n\t"
    "s_nop 1\n\t"
    "v_cndmask_b32 v58, 0, v52, vcc\n\t"
    "v_mfma_f32_16x16x32_bf16 a[192:195], v[168:171], v[132:135], a[192:195]\n\t"
    "ds_read_b64_tr_b16 v[168:169], v212 offset:16384\n\t"
    "ds_read_b64_tr_b16 v[170:171], v212 offset:17408\n\t"
    "v_mfma_f32_16x16x32_bf16 a[180:183], v[172:175], v[136:139], a[180:183]\n\t"
    "v_mov_b32 v60, 0\n\t"
    "v_mov_b32 v61, 0\n\t"
    "v_mfma_f32_16x16x32_bf16 a[184:187], v[172:175], v[140:143], a[184:187]\n\t"
    "v_cmp_eq_u32 vcc, 1, v53\n\t"
    "s_nop 1\n\t"
    "v_cndmask_b32 v59, 0, v52, vcc\n\t"
    "v_mfma_f32_16x16x32_bf16 a[188:191], v[172:175], v[144:147], a[188:191]\n\t"
    "s_add_u32 s53, s40, 24576\n\t"
    "v_mfma_f32_16x16x32_bf16 a[192:195], v[172:175], v[148:151], a[192:195]\n\t"
    "ds_read_b64_tr_b16 v[172:173], v212 offset:18432\n\t"
    "ds_read_b64_tr_b16 v[174:175], v212 offset:19456\n\t"
    "v_mfma_f32_16x16x32_bf16 v[0:3], v[152:155], a[0:3], v[0:3]\n\t"
    "v_mfma_f32_16x16x32_bf16 v[4:7], v[152:155], a[4:7], v[4:7]\n\t"
    "ds_write_b128 v244, a[144:147] offset:49152\n\t"
    "v_mfma_f32_16x16x32_bf16 v[8:11], v[152:155], a[8:11], v[8:11]\n\t"
    "ds_write_b128 v224, a[180:183] offset:32768\n\t"
    "v_mfma_f32_16x16x32_bf16 v[12:15], v[152:155], a[12:15], v[12:15]\n\t"
    "ds_write_b128 v224, a[184:187] offset:36864\n\t"
    "v_mfma_f32_16x16x32_bf16 v[64:67], v[152:155], a[16:19], v[64:67]\n\t"
    "ds_write_b128 v224, a[188:191] offset:40960\n\t"
    "s_waitcnt lgkmcnt(6)\n\t"
    "v_mfma_f32_16x16x32_bf16 v[0:3], v[156:159], a[20:23], v[0:3]\n\t"
    "ds_write_b128 v224, a[192:195] offset:45056\n\t"
    "v_mfma_f32_16x16x32_bf16 v[4:7], v[156:159], a[24:27], v[4:7]\n\t"
    "s_waitcnt vmcnt(0)\n\t"
    "s_mov_b32 exec_hi, 0x8040200\n\t"
    "s_mov_b32 exec_lo, 0\n\t"
    "global_store_dword v253, v254, s[60:61] sc1\n\t"
    "v_mov_b32 v223, v254\n\t"
    "s_mov_b64 exec, -1\n\t"
    "v_mfma_f32_16x16x32_bf16 v[8:11], v[156:159], a[28:31], v[8:11]\n\t"
    "ds_read_b64_tr_b16 v[152:153], v211 offset:49152\n\t"
    "ds_read_b64_tr_b16 v[154:155], v211 offset:50176\n\t"
    "v_mfma_f32_16x16x32_bf16 v[12:15], v[156:159], a[32:35], v[12:15]\n\t"
    "v_mfma_f32_16x16x32_bf16 v[64:67], v[156:159], a[36:39], v[64:67]\n\t"
    "ds_read_b64_tr_b16 v[156:157], v211 offset:51200\n\t"
    "ds_read_b64_tr_b16 v[158:159], v211 offset:52224\n\t"
    "v_mfma_f32_16x16x32_bf16 v[0:3], v[160:163], a[72:75], v[0:3]\n\t"
    "v_mfma_f32_16x16x32_bf16 v[4:7], v[160:163], a[76:79], v[4:7]\n\t"
    "v_mfma_f32_16x16x32_bf16 v[8:11], v[160:163], a[80:83], v[8:11]\n\t"
    "v_mfma_f32_16x16x32_bf16 v[12:15], v[160:163], a[84:87], v[12:15]\n\t"
    "v_mfma_f32_16x16x32_bf16 v[64:67], v[160:163], a[88:91], v[64:67]\n\t"
    "ds_read_b64_tr_b16 v[160:161], v213 offset:0\n\t"
    "ds_read_b64_tr_b16 v[162:163], v213 offset:1024\n\t"
    "v_mfma_f32_16x16x32_bf16 v[0:3], v[164:167], a[92:95], v[0:3]\n\t"
    "v_mfma_f32_16x16x32_bf16 v[4:7], v[164:167], a[96:99], v[4:7]\n\t"
    "v_mfma_f32_16x16x32_bf16 v[8:11], v[164:167], a[100:103], v[8:11]\n\t"
    "v_mfma_f32_16x16x32_bf16 v[12:15], v[164:167], a[104:107], v[12:15]\n\t"
    "v_mfma_f32_16x16x32_bf16 v[64:67], v[164:167], a[108:111], v[64:67]\n\t"
    "ds_read_b64_tr_b16 v[164:165], v213 offset:2048\n\t"
    "ds_read_b64_tr_b16 v[166:167], v213 offset:3072\n\t"
    "v_mfma_f32_16x16x32_bf16 v[0:3], v[168:171], v[80:83], v[0:3]\n\t"
    "v_mfma_f32_16x16x32_bf16 v[4:7], v[168:171], v[84:87], v[4:7]\n\t"
    "v_mfma_f32_16x16x32_bf16 v[8:11], v[168:171], v[88:91], v[8:11]\n\t"
    "v_mfma_f32_16x16x32_bf16 v[12:15], v[168:171], v[92:95], v[12:15]\n\t"
    "v_mfma_f32_16x16x32_bf16 v[64:67], v[168:171], v[96:99], v[64:67]\n\t"
    "ds_read_b64_tr_b16 v[168:169], v213 offset:16384\n\t"
    "ds_read_b64_tr_b16 v[170:171], v213 offset:17408\n\t"
    "s_waitcnt lgkmcnt(4)\n\t"
    "v_mfma_f32_16x16x32_bf16 v[0:3], v[172:175], v[100:103], v[0:3]\n\t"
    "v_mfma_f32_16x16x32_bf16 v[4:7], v[172:175], v[104:107], v[4:7]\n\t"
    "v_mfma_f32_16x16x32_bf16 v[8:11], v[172:175], v[108:111], v[8:11]\n\t"
    "v_mfma_f32_16x16x32_bf16 v[12:15], v[172:175], v[112:115], v[12:15]\n\t"
    "v_mfma_f32_16x16x32_bf16 v[64:67], v[172:175], v[116:119], v[64:67]\n\t"
    "ds_read_b64_tr_b16 v[172:173], v213 offset:18432\n\t"
    "ds_read_b64_tr_b16 v[174:175], v213 offset:19456\n\t"
    "v_mfma_f32_16x16x32_bf16 v[0:3], v[152:155], a[40:43], v[0:3]\n\t"
    "v_mfma_f32_16x16x32_bf16 v[4:7], v[152:155], a[44:47], v[4:7]\n\t"
    "v_mfma_f32_16x16x32_bf16 v[8:11], v[152:155], a[48:51], v[8:11]\n\t"
    "ds_write_b128 v231, v[64:67] offset:49152\n\t"
    "v_mfma_f32_16x16x32_bf16 v[12:15], v[152:155], a[52:55], v[12:15]\n\t"
    "v_mfma_f32_16x16x32_bf16 v[0:3], v[156:159], a[56:59], v[0:3]\n\t"
    "v_mfma_f32_16x16x32_bf16 v[4:7], v[156:159], a[60:63], v[4:7]\n\t"
    "v_mfma_f32_16x16x32_bf16 v[8:11], v[156:159], a[64:67], v[8:11]\n\t"
    "v_mfma_f32_16x16x32_bf16 v[12:15], v[156:159], a[68:71], v[12:15]\n\t"
    "v_mfma_f32_16x16x32_bf16 v[0:3], v[160:163], a[112:115], v[0:3]\n\t"
    "s_waitcnt lgkmcnt(0)\n\t"
    "s_barrier\n\t"
    "v_mfma_f32_16x16x32_bf16 v[4:7], v[160:163], a[116:119], v[4:7]\n\t"
    "v_add_u32 v247, s88, v226\n\t"
    "v_add_u32 v248, s42, v247\n\t"
    "v_mfma_f32_16x16x32_bf16 v[8:11], v[160:163], a[120:123], v[8:11]\n\t"
    "ds_read_b128 v[192:195], v247 offset:0\n\t"
    "ds_read_b128 v[196:199], v247 offset:1024\n\t"
    "v_mfma_f32_16x16x32_bf16 v[12:15], v[160:163], a[124:127], v[12:15]\n\t"
    "ds_read_b128 v[200:203], v248 offset:0\n\t"
    "ds_read_b128 v[204:207], v248 offset:1024\n\t"
    "v_mfma_f32_16x16x32_bf16 v[0:3], v[164:167], a[128:131], v[0:3]\n\t"
    "ds_read_b128 v[176:179], v225 offset:32768\n\t"
    "ds_read_b128 v[180:183], v225 offset:36864\n\t"
    "v_mfma_f32_16x16x32_bf16 v[4:7], v[164:167], a[132:135], v[4:7]\n\t"
    "ds_read_b128 v[184:187], v225 offset:40960\n\t"
    "ds_read_b128 v[188:191], v225 offset:45056\n\t"
    "v_mfma_f32_16x16x32_bf16 v[8:11], v[164:167], a[136:139], v[8:11]\n\t"
    "v_mfma_f32_16x16x32_bf16 v[12:15], v[164:167], a[140:143], v[12:15]\n\t"
    "v_mfma_f32_16x16x32_bf16 v[0:3], v[168:171], v[120:123], v[0:3]\n\t"
    "global_load_dword v254, v252, s[60:61] sc1\n\t"
    "v_mfma_f32_16x16x32_bf16 v[4:7], v[168:171], v[124:127], v[4:7]\n\t"
    "v_mfma_f32_16x16x32_bf16 v[8:11], v[168:171], v[128:131], v[8:11]\n\t"
    "v_mfma_f32_16x16x32_bf16 v[12:15], v[168:171], v[132:135], v[12:15]\n\t"
    "s_waitcnt lgkmcnt(4)\n\t"
    "v_add_f32 v192, v192, v196\n\t"
    "v_add_f32 v193, v193, v197\n\t"
    "v_mfma_f32_16x16x32_bf16 v[0:3], v[172:175], v[136:139], v[0:3]\n\t"
    "v_add_f32 v194, v194, v198\n\t"
    "v_add_f32 v195, v195, v199\n\t"
    "v_mfma_f32_16x16x32_bf16 v[4:7], v[172:175], v[140:143], v[4:7]\n\t"
    "v_add_f32 v200, v200, v204\n\t"
    "v_add_f32 v201, v201, v205\n\t"
    "v_mfma_f32_16x16x32_bf16 v[8:11], v[172:175], v[144:147], v[8:11]\n\t"
    "v_add_f32 v202, v202, v206\n\t"
    "v_add_f32 v203, v203, v207\n\t"
    "v_mfma_f32_16x16x32_bf16 v[12:15], v[172:175], v[148:151], v[12:15]\n\t"
    "v_fma_f32 v192, v200, s41, v192\n\t"
    "v_fma_f32 v193, v201, s41, v193\n\t"
    "v_fma_f32 v194, v202, s41, v194\n\t"
    "v_fma_f32 v195, v203, s41, v195\n\t"
    "s_waitcnt lgkmcnt(0)\n\t"
    "v_add_f32 v0, v0, v176\n\t"
    "v_add_f32 v1, v1, v177\n\t"
    "v_add_f32 v2, v2, v178\n\t"
    "v_add_f32 v3, v3, v179\n\t"
    "v_add_f32 v4, v4, v180\n\t"
    "v_add_f32 v5, v5, v181\n\t"
    "v_add_f32 v6, v6, v182\n\t"
    "v_add_f32 v7, v7, v183\n\t"
    "v_add_f32 v8, v8, v184\n\t"
    "v_add_f32 v9, v9, v185\n\t"
    "v_add_f32 v10, v10, v186\n\t"
    "v_add_f32 v11, v11, v187\n\t"
    "v_add_f32 v12, v12, v188\n\t"
    "v_add_f32 v13, v13, v189\n\t"
    "v_add_f32 v14, v14, v190\n\t"
    "v_add_f32 v15, v15, v191\n\t"
    "v_mfma_f32_16x16x32_bf16 v[196:199], v[56:59], v[240:243], 0\n\t"
    "s_waitcnt vmcnt(0)\n\t"
    "v_cmp_eq_u32 vcc, v254, v223\n\t"
    "s_andn2_b32 s36, 0x88442200, vcc_hi\n\t"
    "s_cbranch_scc1 SL1_%=\n\t"
    "PX1_%=:\n\t"
    "buffer_load_dwordx4 v[16:19], v226, s[8:11], s40 offen offset:0 sc1\n\t"
    "buffer_load_dwordx4 v[28:31], v226, s[8:11], s40 offen offset:1024 sc1\n\t"
    "buffer_load_dwordx4 v[40:43], v226, s[8:11], s40 offen offset:2048 sc1\n\t"
    "buffer_load_dwordx4 v[20:23], v229, s[8:11], s40 offen offset:0 sc1\n\t"
    "buffer_load_dwordx4 v[32:35], v229, s[8:11], s40 offen offset:1024 sc1\n\t"
    "buffer_load_dwordx4 v[44:47], v229, s[8:11], s40 offen offset:2048 sc1\n\t"
    "buffer_load_dwordx4 v[24:27], v226, s[8:11], s53 offen offset:0 sc1\n\t"
    "buffer_load_dwordx4 v[36:39], v226, s[8:11], s53 offen offset:1024 sc1\n\t"
    "buffer_load_dwordx4 v[48:51], v226, s[8:11], s53 offen offset:2048 sc1\n\t"
    "s_waitcnt vmcnt(8)\n\t"
    "v_mfma_f32_16x16x32_bf16 v[0:3], v[58:61], v[16:19], v[0:3]\n\t"
    "v_mfma_f32_16x16x32_bf16 v[4:7], v[56:59], v[16:19], v[4:7]\n\t"
    "s_waitcnt vmcnt(7)\n\t"
    "v_mfma_f32_16x16x32_bf16 v[8:11], v[58:61], v[28:31], v[8:11]\n\t"
    "v_mfma_f32_16x16x32_bf16 v[12:15], v[56:59], v[28:31], v[12:15]\n\t"
    "s_waitcnt vmcnt(6)\n\t"
    "v_mfma_f32_16x16x32_bf16 v[192:195], v[58:61], v[40:43], v[192:195]\n\t"
    "v_mfma_f32_16x16x32_bf16 v[196:199], v[56:59], v[40:43], v[196:199]\n\t"
    "s_waitcnt vmcnt(5)\n\t"
    "v_mfma_f32_16x16x32_bf16 v[0:3], v[58:61], v[20:23], v[0:3]\n\t"
    "v_mfma_f32_16x16x32_bf16 v[4:7], v[56:59], v[20:23], v[4:7]\n\t"
    "s_waitcnt vmcnt(4)\n\t"
    "v_mfma_f32_16x16x32_bf16 v[8:11], v[58:61], v[32:35], v[8:11]\n\t"
    "v_mfma_f32_16x16x32_bf16 v[12:15], v[56:59], v[32:35], v[12:15]\n\t"
    "s_waitcnt vmcnt(3)\n\t"
    "v_mfma_f32_16x16x32_bf16 v[192:195], v[58:61], v[44:47], v[192:195]\n\t"
    "v_mfma_f32_16x16x32_bf16 v[196:199], v[56:59], v[44:47], v[196:199]\n\t"
    "s_waitcnt vmcnt(2)\n\t"
    "v_mfma_f32_16x16x32_bf16 v[0:3], v[58:61], v[24:27], v[0:3]\n\t"
    "v_mfma_f32_16x16x32_bf16 v[4:7], v[56:59], v[24:27], v[4:7]\n\t"
    "s_waitcnt vmcnt(1)\n\t"
    "v_mfma_f32_16x16x32_bf16 v[8:11], v[58:61], v[36:39], v[8:11]\n\t"
    "v_mfma_f32_16x16x32_bf16 v[12:15], v[56:59], v[36:39], v[12:15]\n\t"
    "s_waitcnt vmcnt(0)\n\t"
    "v_mfma_f32_16x16x32_bf16 v[192:195], v[58:61], v[48:51], v[192:195]\n\t"
    "v_mfma_f32_16x16x32_bf16 v[196:199], v[56:59], v[48:51], v[196:199]\n\t"
    "v_cvt_pk_bf16_f32 v0, v0, v1\n\t"
    "v_cvt_pk_bf16_f32 v1, v2, v3\n\t"
    "buffer_store_dwordx2 v[0:1], v249, s[20:23], s92 offen\n\t"
    "v_cvt_pk_bf16_f32 v4, v4, v5\n\t"
    "v_cvt_pk_bf16_f32 v5, v6, v7\n\t"
    "buffer_store_dwordx2 v[4:5], v249, s[20:23], s93 offen\n\t"
    "v_cvt_pk_bf16_f32 v8, v8, v9\n\t"
    "v_cvt_pk_bf16_f32 v9, v10, v11\n\t"
    "buffer_store_dwordx2 v[8:9], v249, s[20:23], s94 offen\n\t"
    "v_cvt_pk_bf16_f32 v12, v12, v13\n\t"
    "v_cvt_pk_bf16_f32 v13, v14, v15\n\t"
    "buffer_store_dwordx2 v[12:13], v249, s[20:23], s95 offen\n\t"
    "v_cvt_pk_bf16_f32 v192, v192, v193\n\t"
    "v_cvt_pk_bf16_f32 v193, v194, v195\n\t"
    "buffer_store_dwordx2 v[192:193], v249, s[20:23], s96 offen\n\t"
    "s_cmp_lg_u32 s43, 0\n\t"
    "s_cbranch_scc1 END_%=\n\t"
    "v_cvt_pk_bf16_f32 v196, v196, v197\n\t"
    "v_cvt_pk_bf16_f32 v197, v198, v199\n\t"
    "buffer_store_dwordx2 v[196:197], v249, s[20:23], s97 offen\n\t"
    "s_branch END_%=\n\t"
    "SL0_%=:\n\t"
    "s_andn2_b32 exec_lo, 0x8040200, vcc_lo\n\t"
    "s_mov_b32 exec_hi, 0\n\t"
    "s_cbranch_execz NE0_%=\n\t"
    "global_store_dword v253, v254, s[60:61] sc1\n\t"
    "v_mov_b32 v223, v254\n\t"
    "NE0_%=:\n\t"
    "s_mov_b64 exec, -1\n\t"
    "s_nop 1\n\t"
    "global_load_dword v254, v252, s[60:61] sc1\n\t"
    "s_waitcnt vmcnt(0)\n\t"
    "v_cmp_eq_u32 vcc, v254, v223\n\t"
    "s_add_u32 s45, s45, 16384\n\t"
    "s_cbranch_scc1 PX0_%=\n\t"
    "s_andn2_b32 s36, 0x88442200, vcc_lo\n\t"
    "s_cbranch_scc1 SL0_%=\n\t"
    "s_branch PX0_%=\n\t"
    "SL1_%=:\n\t"
    "s_andn2_b32 exec_hi, 0x8040200, vcc_hi\n\t"
    "s_mov_b32 exec_lo, 0\n\t"
    "s_cbranch_execz NE1_%=\n\t"
    "global_store_dword v253, v254, s[60:61] sc1\n\t"
    "v_mov_b32 v223, v254\n\t"
    "NE1_%=:\n\t"
    "s_mov_b64 exec, -1\n\t"
    "s_nop 1\n\t"
    "global_load_dword v254, v252, s[60:61] sc1\n\t"
    "s_waitcnt vmcnt(0)\n\t"
    "v_cmp_eq_u32 vcc, v254, v223\n\t"
    "s_add_u32 s45, s45, 16384\n\t"
    "s_cbranch_scc1 PX1_%=\n\t"
    "s_andn2_b32 s36, 0x88442200, vcc_hi\n\t"
    "s_cbranch_scc1 SL1_%=\n\t"
    "s_branch PX1_%=\n\t"
    "TCH_A_%=:\n\t"
    "s_ff1_i32_b32 s52, s44\n\t"
    "v_min_u32 v245, 47, v222\n\t"
    "s_sub_u32 s52, s52, 1\n\t"
    "s_lshr_b32 s52, s52, 1\n\t"
    "s_mul_i32 s52, s52, 6144\n\t"
    "v_lshl_add_u32 v245, v245, 7, s52\n\t"
    "global_load_dword v246, v245, s[12:13] \n\t"
    "global_load_dword v246, v245, s[14:15] \n\t"
    "s_branch TCB_A_%=\n\t"
    "TCH_B_%=:\n\t"
    "s_ff1_i32_b32 s52, s44\n\t"
    "v_min_u32 v245, 47, v222\n\t"
    "s_sub_u32 s52, s52, 1\n\t"
    "s_lshr_b32 s52, s52, 1\n\t"
    "s_mul_i32 s52, s52, 6144\n\t"
    "v_lshl_add_u32 v245, v245, 7, s52\n\t"
    "global_load_dword v246, v245, s[12:13] \n\t"
    "global_load_dword v246, v245, s[14:15] \n\t"
    "s_branch TCB_B_%=\n\t"
    "REDO_%=:\n\t"
    "s_mov_b32 s63, 1\n\t"
    "s_waitcnt vmcnt(0) lgkmcnt(0)\n\t"
    "s_barrier\n\t"
    "s_cmp_eq_u32 s65, 0\n\t"
    "s_cbranch_scc1 RESTART_A_%=\n\t"
    "s_branch RESTART_B_%=\n\t"
    "TR_PApeel_%=:\n\t"
    "s_call_b64 s[46:47], PACEF_%=\n\t"
    "s_branch BK_PApeel_%=\n\t"
    "TR_AloopE_%=:\n\t"
    "s_call_b64 s[46:47], HOOKE_%=\n\t"
    "s_branch BK_AloopE_%=\n\t"
    "TR_AloopO_%=:\n\t"
    "s_call_b64 s[46:47], HOOKO_%=\n\t"
    "s_branch BK_AloopO_%=\n\t"
    "TR_PAloop_%=:\n\t"
    "s_call_b64 s[46:47], PACEF_%=\n\t"
    "s_branch BK_PAloop_%=\n\t"
    "TR_AdrainE_%=:\n\t"
    "s_call_b64 s[46:47], HOOKD_%=\n\t"
    "s_branch BK_AdrainE_%=\n\t"
    "TR_AdrainO_%=:\n\t"
    "s_call_b64 s[46:47], HOOKD_%=\n\t"
    "s_branch BK_AdrainO_%=\n\t"
    "TR_PBpeel_%=:\n\t"
    "s_call_b64 s[46:47], PACEF_%=\n\t"
    "s_branch BK_PBpeel_%=\n\t"
    "TR_BloopE_%=:\n\t"
    "s_call_b64 s[46:47], HOOKE_%=\n\t"
    "s_branch BK_BloopE_%=\n\t"
    "TR_BloopO_%=:\n\t"
    "s_call_b64 s[46:47], HOOKO_%=\n\t"
    "s_branch BK_BloopO_%=\n\t"
    "TR_PBloop_%=:\n\t"
    "s_call_b64 s[46:47], PACEF_%=\n\t"
    "s_branch BK_PBloop_%=\n\t"
    "TR_BdrainE_%=:\n\t"
    "s_call_b64 s[46:47], HOOKD_%=\n\t"
    "s_branch BK_BdrainE_%=\n\t"
    "TR_BdrainO_%=:\n\t"
    "s_call_b64 s[46:47], HOOKD_%=\n\t"
    "s_branch BK_BdrainO_%=\n\t"
    "RESTART_A_%=:\n\t"
    "s_mul_i32 s41, s50, 16384\n\t"
    "s_add_u32 s41, s41, s42\n\t"
    "s_mov_b32 s40, 0\n\t"
    "s_waitcnt vmcnt(0)\n\t"
    "PL_A_%=:\n\t"
    "s_mov_b32 m0, s41\n\t"
    "s_nop 0\n\t"
    "buffer_load_dwordx4 v214, s[36:39], s40 offen offset:0 nt lds\n\t"
    "buffer_load_dwordx4 v215, s[36:39], s40 offen offset:1024 nt lds\n\t"
    "buffer_load_dwordx4 v216, s[36:39], s40 offen offset:2048 nt lds\n\t"
    "buffer_load_dwordx4 v217, s[36:39], s40 offen offset:3072 nt lds\n\t"
    "s_add_u32 s40, s40, 8192\n\t"
    "s_add_u32 s41, s41, 16384\n\t"
    "s_cmp_lg_u32 s41, s43\n\t"
    "s_cbranch_scc1 PL_A_%=\n\t"
    "v_mfma_f32_32x32x16_bf16 a[0:15], a[248:251], a[248:251], 0\n\t"
    "v_mfma_f32_32x32x16_bf16 a[16:31], a[248:251], a[248:251], 0\n\t"
    "v_mfma_f32_32x32x16_bf16 a[32:47], a[248:251], a[248:251], 0\n\t"
    "v_mfma_f32_32x32x16_bf16 a[48:63], a[248:251], a[248:251], 0\n\t"
    "v_mfma_f32_32x32x16_bf16 a[64:79], a[248:251], a[248:251], 0\n\t"
    "v_mfma_f32_32x32x16_bf16 a[80:95], a[248:251], a[248:251], 0\n\t"
    "v_mfma_f32_32x32x16_bf16 a[96:111], a[248:251], a[248:251], 0\n\t"
    "v_mfma_f32_32x32x16_bf16 a[112:127], a[248:251], a[248:251], 0\n\t"
    "v_mfma_f32_32x32x16_bf16 a[128:143], a[248:251], a[248:251], 0\n\t"
    "v_mfma_f32_32x32x16_bf16 a[144:159], a[248:251], a[248:251], 0\n\t"
    "v_mfma_f32_32x32x16_bf16 a[160:175], a[248:251], a[248:251], 0\n\t"
    "v_mfma_f32_16x16x32_bf16 a[176:179], a[248:251], a[248:251], 0\n\t"
    "v_mov_b32 v240, 1.0\n\t"
    "v_mov_b32 v241, 1.0\n\t"
    "v_mov_b32 v242, 1.0\n\t"
    "v_mov_b32 v243, 1.0\n\t"
    "v_mov_b32 v244, 1.0\n\t"
    "s_mov_b32 s44, 0x40000000\n\t"
    "v_add_u32 v208, s42, v211\n\t"
    "v_add_u32 v209, s42, v212\n\t"
    "s_mov_b32 m0, s42\n\t"
    "s_add_u32 s41, s42, 16384\n\t"
    "v_add_u32 v210, s41, v213\n\t"
    "s_mov_b64 vcc, -1\n\t"
    "s_waitcnt vmcnt(36)\n\t"
    "s_barrier\n\t"
    "ds_read_b128 a[180:183], v208 offset:0\n\t"
    "ds_read_b128 a[184:187], v209 offset:0\n\t"
    "ds_read_b128 a[188:191], v208 offset:1024\n\t"
    "ds_read_b128 a[192:195], v209 offset:1024\n\t"
    "ds_read_b128 a[196:199], v208 offset:4096\n\t"
    "ds_read_b128 a[200:203], v209 offset:4096\n\t"
    "ds_read_b128 a[204:207], v208 offset:5120\n\t"
    "ds_read_b128 a[208:211], v209 offset:5120\n\t"
    "s_waitcnt lgkmcnt(0)\n\t"
    "v_add_u32 v208, s41, v211\n\t"
    "v_add_u32 v209, s41, v212\n\t"
    "s_cmp_eq_u32 s50, 0\n\t"
    "s_cbranch_scc0 PEEL_O_A_%=\n\t"
    "s_branch PEEL_E_A_%=\n\t"
    "RESTART_B_%=:\n\t"
    "s_mul_i32 s41, s50, 32768\n\t"
    "s_add_u32 s41, s41, s42\n\t"
    "s_mov_b32 s40, 0\n\t"
    "s_waitcnt vmcnt(0)\n\t"
    "PL_B_%=:\n\t"
    "s_mov_b32 m0, s41\n\t"
    "s_nop 0\n\t"
    "buffer_load_dwordx4 v214, s[36:39], s40 offen offset:0 nt lds\n\t"
    "buffer_load_dwordx4 v215, s[36:39], s40 offen offset:1024 nt lds\n\t"
    "buffer_load_dwordx4 v216, s[36:39], s40 offen offset:2048 nt lds\n\t"
    "buffer_load_dwordx4 v217, s[36:39], s40 offen offset:3072 nt lds\n\t"
    "s_add_u32 m0, m0, 4096\n\t"
    "s_nop 0\n\t"
    "buffer_load_dwordx4 v218, s[36:39], s40 offen offset:0 nt lds\n\t"
    "buffer_load_dwordx4 v219, s[36:39], s40 offen offset:1024 nt lds\n\t"
    "buffer_load_dwordx4 v220, s[36:39], s40 offen offset:2048 nt lds\n\t"
    "buffer_load_dwordx4 v221, s[36:39], s40 offen offset:3072 nt lds\n\t"
    "s_add_u32 s40, s40, 8192\n\t"
    "s_add_u32 s41, s41, 32768\n\t"
    "s_cmp_lg_u32 s41, s43\n\t"
    "s_cbranch_scc1 PL_B_%=\n\t"
    "v_mfma_f32_32x32x16_bf16 a[0:15], a[248:251], a[248:251], 0\n\t"
    "v_mfma_f32_32x32x16_bf16 a[16:31], a[248:251], a[248:251], 0\n\t"
    "v_mfma_f32_32x32x16_bf16 a[32:47], a[248:251], a[248:251], 0\n\t"
    "v_mfma_f32_32x32x16_bf16 a[48:63], a[248:251], a[248:251], 0\n\t"
    "v_mfma_f32_32x32x16_bf16 a[64:79], a[248:251], a[248:251], 0\n\t"
    "v_mfma_f32_32x32x16_bf16 a[80:95], a[248:251], a[248:251], 0\n\t"
    "v_mfma_f32_32x32x16_bf16 a[96:111], a[248:251], a[248:251], 0\n\t"
    "v_mfma_f32_32x32x16_bf16 a[112:127], a[248:251], a[248:251], 0\n\t"
    "v_mfma_f32_32x32x16_bf16 a[128:143], a[248:251], a[248:251], 0\n\t"
    "v_mfma_f32_32x32x16_bf16 a[144:159], a[248:251], a[248:251], 0\n\t"
    "v_mfma_f32_32x32x16_bf16 a[160:175], a[248:251], a[248:251], 0\n\t"
    "v_mfma_f32_16x16x32_bf16 a[176:179], a[248:251], a[248:251], 0\n\t"
    "v_mov_b32 v240, 1.0\n\t"
    "v_mov_b32 v241, 1.0\n\t"
    "v_mov_b32 v242, 1.0\n\t"
    "v_mov_b32 v243, 1.0\n\t"
    "v_mov_b32 v244, 1.0\n\t"
    "s_mov_b32 s44, 0x40000000\n\t"
    "v_add_u32 v208, s42, v211\n\t"
    "v_add_u32 v209, s42, v212\n\t"
    "s_mov_b32 m0, s42\n\t"
    "s_add_u32 s41, s42, 32768\n\t"
    "v_add_u32 v210, s41, v213\n\t"
    "s_mov_b64 vcc, -1\n\t"
    "s_waitcnt vmcnt(32)\n\t"
    "s_barrier\n\t"
    "ds_read_b128 a[180:183], v208 offset:0\n\t"
    "ds_read_b128 a[184:187], v209 offset:0\n\t"
    "ds_read_b128 a[188:191], v208 offset:1024\n\t"
    "ds_read_b128 a[192:195], v209 offset:1024\n\t"
    "ds_read_b128 a[196:199], v208 offset:4096\n\t"
    "ds_read_b128 a[200:203], v209 offset:4096\n\t"
    "ds_read_b128 a[204:207], v208 offset:5120\n\t"
    "ds_read_b128 a[208:211], v209 offset:5120\n\t"
    "s_waitcnt lgkmcnt(0)\n\t"
    "v_add_u32 v208, s41, v211\n\t"
    "v_add_u32 v209, s41, v212\n\t"
    "s_cmp_eq_u32 s50, 0\n\t"
    "s_cbranch_scc0 PEEL_O_B_%=\n\t"
    "s_branch PEEL_E_B_%=\n\t"
    "HOOKE_%=:\n\t"
    "s_call_b64 s[48:49], APPLY_%=\n\t"
    "v_max3_f32 v224, v80, v81, 0\n\t"
    "v_max3_f32 v224, v224, v82, v83\n\t"
    "v_max3_f32 v224, v224, v84, v85\n\t"
    "v_max3_f32 v224, v224, v86, v87\n\t"
    "v_mov_b32 v225, v224\n\t"
    "s_nop 1\n\t"
    "v_permlane16_swap_b32 v224, v225\n\t"
    "v_max_f32 v224, v224, v225\n\t"
    "v_mov_b32 v225, v224\n\t"
    "s_nop 1\n\t"
    "v_permlane32_swap_b32 v224, v225\n\t"
    "v_max_f32 v224, v224, v225\n\t"
    "v_sub_f32 v80, v80, v224\n\t"
    "v_sub_f32 v81, v81, v224\n\t"
    "v_sub_f32 v82, v82, v224\n\t"
    "v_sub_f32 v83, v83, v224\n\t"
    "v_sub_f32 v84, v84, v224\n\t"
    "v_sub_f32 v85, v85, v224\n\t"
    "v_sub_f32 v86, v86, v224\n\t"
    "v_sub_f32 v87, v87, v224\n\t"
    "v_sub_f32 v188, v188, v224\n\t"
    "v_sub_f32 v189, v189, v224\n\t"
    "v_sub_f32 v190, v190, v224\n\t"
    "v_sub_f32 v191, v191, v224\n\t"
    "v_exp_f32 v240, -v224\n\t"
    "v_max3_f32 v224, v88, v89, 0\n\t"
    "v_max3_f32 v224, v224, v90, v91\n\t"
    "v_max3_f32 v224, v224, v92, v93\n\t"
    "v_max3_f32 v224, v224, v94, v95\n\t"
    "v_mov_b32 v225, v224\n\t"
    "s_nop 1\n\t"
    "v_permlane16_swap_b32 v224, v225\n\t"
    "v_max_f32 v224, v224, v225\n\t"
    "v_mov_b32 v225, v224\n\t"
    "s_nop 1\n\t"
    "v_permlane32_swap_b32 v224, v225\n\t"
    "v_max_f32 v224, v224, v225\n\t"
    "v_sub_f32 v88, v88, v224\n\t"
    "v_sub_f32 v89, v89, v224\n\t"
    "v_sub_f32 v90, v90, v224\n\t"
    "v_sub_f32 v91, v91, v224\n\t"
    "v_sub_f32 v92, v92, v224\n\t"
    "v_sub_f32 v93, v93, v224\n\t"
    "v_sub_f32 v94, v94, v224\n\t"
    "v_sub_f32 v95, v95, v224\n\t"
    "v_sub_f32 v192, v192, v224\n\t"
    "v_sub_f32 v193, v193, v224\n\t"
    "v_sub_f32 v194, v194, v224\n\t"
    "v_sub_f32 v195, v195, v224\n\t"
    "v_exp_f32 v241, -v224\n\t"
    "v_max3_f32 v224, v96, v97, 0\n\t"
    "v_max3_f32 v224, v224, v98, v99\n\t"
    "v_max3_f32 v224, v224, v100, v101\n\t"
    "v_max3_f32 v224, v224, v102, v103\n\t"
    "v_mov_b32 v225, v224\n\t"
    "s_nop 1\n\t"
    "v_permlane16_swap_b32 v224, v225\n\t"
    "v_max_f32 v224, v224, v225\n\t"
    "v_mov_b32 v225, v224\n\t"
    "s_nop 1\n\t"
    "v_permlane32_swap_b32 v224, v225\n\t"
    "v_max_f32 v224, v224, v225\n\t"
    "v_sub_f32 v96, v96, v224\n\t"
    "v_sub_f32 v97, v97, v224\n\t"
    "v_sub_f32 v98, v98, v224\n\t"
    "v_sub_f32 v99, v99, v224\n\t"
    "v_sub_f32 v100, v100, v224\n\t"
    "v_sub_f32 v101, v101, v224\n\t"
    "v_sub_f32 v102, v102, v224\n\t"
    "v_sub_f32 v103, v103, v224\n\t"
    "v_sub_f32 v196, v196, v224\n\t"
    "v_sub_f32 v197, v197, v224\n\t"
    "v_sub_f32 v198, v198, v224\n\t"
    "v_sub_f32 v199, v199, v224\n\t"
    "v_exp_f32 v242, -v224\n\t"
    "v_max3_f32 v224, v104, v105, 0\n\t"
    "v_max3_f32 v224, v224, v106, v107\n\t"
    "v_max3_f32 v224, v224, v108, v109\n\t"
    "v_max3_f32 v224, v224, v110, v111\n\t"
    "v_mov_b32 v225, v224\n\t"
    "s_nop 1\n\t"
    "v_permlane16_swap_b32 v224, v225\n\t"
    "v_max_f32 v224, v224, v225\n\t"
    "v_mov_b32 v225, v224\n\t"
    "s_nop 1\n\t"
    "v_permlane32_swap_b32 v224, v225\n\t"
    "v_max_f32 v224, v224, v225\n\t"
    "v_sub_f32 v104, v104, v224\n\t"
    "v_sub_f32 v105, v105, v224\n\t"
    "v_sub_f32 v106, v106, v224\n\t"
    "v_sub_f32 v107, v107, v224\n\t"
    "v_sub_f32 v108, v108, v224\n\t"
    "v_sub_f32 v109, v109, v224\n\t"
    "v_sub_f32 v110, v110, v224\n\t"
    "v_sub_f32 v111, v111, v224\n\t"
    "v_sub_f32 v200, v200, v224\n\t"
    "v_sub_f32 v201, v201, v224\n\t"
    "v_sub_f32 v202, v202, v224\n\t"
    "v_sub_f32 v203, v203, v224\n\t"
    "v_exp_f32 v243, -v224\n\t"
    "v_max3_f32 v224, v112, v113, 0\n\t"
    "v_max3_f32 v224, v224, v114, v115\n\t"
    "v_max3_f32 v224, v224, v116, v117\n\t"
    "v_max3_f32 v224, v224, v118, v119\n\t"
    "v_mov_b32 v225, v224\n\t"
    "s_nop 1\n\t"
    "v_permlane16_swap_b32 v224, v225\n\t"
    "v_max_f32 v224, v224, v225\n\t"
    "v_mov_b32 v225, v224\n\t"
    "s_nop 1\n\t"
    "v_permlane32_swap_b32 v224, v225\n\t"
    "v_max_f32 v224, v224, v225\n\t"
    "v_sub_f32 v112, v112, v224\n\t"
    "v_sub_f32 v113, v113, v224\n\t"
    "v_sub_f32 v114, v114, v224\n\t"
    "v_sub_f32 v115, v115, v224\n\t"
    "v_sub_f32 v116, v116, v224\n\t"
    "v_sub_f32 v117, v117, v224\n\t"
    "v_sub_f32 v118, v118, v224\n\t"
    "v_sub_f32 v119, v119, v224\n\t"
    "v_sub_f32 v204, v204, v224\n\t"
    "v_sub_f32 v205, v205, v224\n\t"
    "v_sub_f32 v206, v206, v224\n\t"
    "v_sub_f32 v207, v207, v224\n\t"
    "v_exp_f32 v244, -v224\n\t"
    "s_nop 1\n\t"
    "s_setpc_b64 s[46:47]\n\t"
    "HOOKO_%=:\n\t"
    "s_call_b64 s[48:49], APPLY_%=\n\t"
    "v_max3_f32 v224, v120, v121, 0\n\t"
    "v_max3_f32 v224, v224, v122, v123\n\t"
    "v_max3_f32 v224, v224, v124, v125\n\t"
    "v_max3_f32 v224, v224, v126, v127\n\t"
    "v_mov_b32 v225, v224\n\t"
    "s_nop 1\n\t"
    "v_permlane16_swap_b32 v224, v225\n\t"
    "v_max_f32 v224, v224, v225\n\t"
    "v_mov_b32 v225, v224\n\t"
    "s_nop 1\n\t"
    "v_permlane32_swap_b32 v224, v225\n\t"
    "v_max_f32 v224, v224, v225\n\t"
    "v_sub_f32 v120, v120, v224\n\t"
    "v_sub_f32 v121, v121, v224\n\t"
    "v_sub_f32 v122, v122, v224\n\t"
    "v_sub_f32 v123, v123, v224\n\t"
    "v_sub_f32 v124, v124, v224\n\t"
    "v_sub_f32 v125, v125, v224\n\t"
    "v_sub_f32 v126, v126, v224\n\t"
    "v_sub_f32 v127, v127, v224\n\t"
    "v_sub_f32 v188, v188, v224\n\t"
    "v_sub_f32 v189, v189, v224\n\t"
    "v_sub_f32 v190, v190, v224\n\t"
    "v_sub_f32 v191, v191, v224\n\t"
    "v_exp_f32 v240, -v224\n\t"
    "v_max3_f32 v224, v128, v129, 0\n\t"
    "v_max3_f32 v224, v224, v130, v131\n\t"
    "v_max3_f32 v224, v224, v132, v133\n\t"
    "v_max3_f32 v224, v224, v134, v135\n\t"
    "v_mov_b32 v225, v224\n\t"
    "s_nop 1\n\t"
    "v_permlane16_swap_b32 v224, v225\n\t"
    "v_max_f32 v224, v224, v225\n\t"
    "v_mov_b32 v225, v224\n\t"
    "s_nop 1\n\t"
    "v_permlane32_swap_b32 v224, v225\n\t"
    "v_max_f32 v224, v224, v225\n\t"
    "v_sub_f32 v128, v128, v224\n\t"
    "v_sub_f32 v129, v129, v224\n\t"
    "v_sub_f32 v130, v130, v224\n\t"
    "v_sub_f32 v131, v131, v224\n\t"
    "v_sub_f32 v132, v132, v224\n\t"
    "v_sub_f32 v133, v133, v224\n\t"
    "v_sub_f32 v134, v134, v224\n\t"
    "v_sub_f32 v135, v135, v224\n\t"
    "v_sub_f32 v192, v192, v224\n\t"
    "v_sub_f32 v193, v193, v224\n\t"
    "v_sub_f32 v194, v194, v224\n\t"
    "v_sub_f32 v195, v195, v224\n\t"
    "v_exp_f32 v241, -v224\n\t"
    "v_max3_f32 v224, v136, v137, 0\n\t"
    "v_max3_f32 v224, v224, v138, v139\n\t"
    "v_max3_f32 v224, v224, v140, v141\n\t"
    "v_max3_f32 v224, v224, v142, v143\n\t"
    "v_mov_b32 v225, v224\n\t"
    "s_nop 1\n\t"
    "v_permlane16_swap_b32 v224, v225\n\t"
    "v_max_f32 v224, v224, v225\n\t"
    "v_mov_b32 v225, v224\n\t"
    "s_nop 1\n\t"
    "v_permlane32_swap_b32 v224, v225\n\t"
    "v_max_f32 v224, v224, v225\n\t"
    "v_sub_f32 v136, v136, v224\n\t"
    "v_sub_f32 v137, v137, v224\n\t"
    "v_sub_f32 v138, v138, v224\n\t"
    "v_sub_f32 v139, v139, v224\n\t"
    "v_sub_f32 v140, v140, v224\n\t"
    "v_sub_f32 v141, v141, v224\n\t"
    "v_sub_f32 v142, v142, v224\n\t"
    "v_sub_f32 v143, v143, v224\n\t"
    "v_sub_f32 v196, v196, v224\n\t"
    "v_sub_f32 v197, v197, v224\n\t"
    "v_sub_f32 v198, v198, v224\n\t"
    "v_sub_f32 v199, v199, v224\n\t"
    "v_exp_f32 v242, -v224\n\t"
    "v_max3_f32 v224, v144, v145, 0\n\t"
    "v_max3_f32 v224, v224, v146, v147\n\t"
    "v_max3_f32 v224, v224, v148, v149\n\t"
    "v_max3_f32 v224, v224, v150, v151\n\t"
    "v_mov_b32 v225, v224\n\t"
    "s_nop 1\n\t"
    "v_permlane16_swap_b32 v224, v225\n\t"
    "v_max_f32 v224, v224, v225\n\t"
    "v_mov_b32 v225, v224\n\t"
    "s_nop 1\n\t"
    "v_permlane32_swap_b32 v224, v225\n\t"
    "v_max_f32 v224, v224, v225\n\t"
    "v_sub_f32 v144, v144, v224\n\t"
    "v_sub_f32 v145, v145, v224\n\t"
    "v_sub_f32 v146, v146, v224\n\t"
    "v_sub_f32 v147, v147, v224\n\t"
    "v_sub_f32 v148, v148, v224\n\t"
    "v_sub_f32 v149, v149, v224\n\t"
    "v_sub_f32 v150, v150, v224\n\t"
    "v_sub_f32 v151, v151, v224\n\t"
    "v_sub_f32 v200, v200, v224\n\t"
    "v_sub_f32 v201, v201, v224\n\t"
    "v_sub_f32 v202, v202, v224\n\t"
    "v_sub_f32 v203, v203, v224\n\t"
    "v_exp_f32 v243, -v224\n\t"
    "s_nop 1\n\t"
    "s_setpc_b64 s[46:47]\n\t"
    "PACEF_%=:\n\t"
    "s_memrealtime s[56:57]\n\t"
    "s_waitcnt lgkmcnt(0)\n\t"
    "s_sub_u32 s54, s34, s56\n\t"
    "s_cmp_gt_i32 s54, 0\n\t"
    "s_cbranch_scc1 PACEF_%=\n\t"
    "s_mov_b32 s56, s34\n\t"
    "s_setpc_b64 s[46:47]\n\t"
    "HOOKD_%=:\n\t"
    "s_mov_b64 s[48:49], s[46:47]\n\t"
    "APPLY_%=:\n\t"
    "s_nop 7\n\t"
    "s_nop 7\n\t"
    "v_accvgpr_read_b32 v224, a0\n\t"
    "v_accvgpr_read_b32 v225, a1\n\t"
    "v_accvgpr_read_b32 v226, a2\n\t"
    "v_accvgpr_read_b32 v227, a3\n\t"
    "v_mul_f32 v224, v224, v240\n\t"
    "v_mul_f32 v225, v225, v240\n\t"
    "v_mul_f32 v226, v226, v240\n\t"
    "v_mul_f32 v227, v227, v240\n\t"
    "v_accvgpr_write_b32 a0, v224\n\t"
    "v_accvgpr_write_b32 a1, v225\n\t"
    "v_accvgpr_write_b32 a2, v226\n\t"
    "v_accvgpr_write_b32 a3, v227\n\t"
    "v_accvgpr_read_b32 v224, a4\n\t"
    "v_accvgpr_read_b32 v225, a5\n\t"
    "v_accvgpr_read_b32 v226, a6\n\t"
    "v_accvgpr_read_b32 v227, a7\n\t"
    "v_mul_f32 v224, v224, v240\n\t"
    "v_mul_f32 v225, v225, v240\n\t"
    "v_mul_f32 v226, v226, v240\n\t"
    "v_mul_f32 v227, v227, v240\n\t"
    "v_accvgpr_write_b32 a4, v224\n\t"
    "v_accvgpr_write_b32 a5, v225\n\t"
    "v_accvgpr_write_b32 a6, v226\n\t"
    "v_accvgpr_write_b32 a7, v227\n\t"
    "v_accvgpr_read_b32 v224, a8\n\t"
    "v_accvgpr_read_b32 v225, a9\n\t"
    "v_accvgpr_read_b32 v226, a10\n\t"
    "v_accvgpr_read_b32 v227, a11\n\t"
    "v_mul_f32 v224, v224, v240\n\t"
    "v_mul_f32 v225, v225, v240\n\t"
    "v_mul_f32 v226, v226, v240\n\t"
    "v_mul_f32 v227, v227, v240\n\t"
    "v_accvgpr_write_b32 a8, v224\n\t"
    "v_accvgpr_write_b32 a9, v225\n\t"
    "v_accvgpr_write_b32 a10, v226\n\t"
    "v_accvgpr_write_b32 a11, v227\n\t"
    "v_accvgpr_read_b32 v224, a12\n\t"
    "v_accvgpr_read_b32 v225, a13\n\t"
    "v_accvgpr_read_b32 v226, a14\n\t"
    "v_accvgpr_read_b32 v227, a15\n\t"
    "v_mul_f32 v224, v224, v240\n\t"
    "v_mul_f32 v225, v225, v240\n\t"
    "v_mul_f32 v226, v226, v240\n\t"
    "v_mul_f32 v227, v227, v240\n\t"
    "v_accvgpr_write_b32 a12, v224\n\t"
    "v_accvgpr_write_b32 a13, v225\n\t"
    "v_accvgpr_write_b32 a14, v226\n\t"
    "v_accvgpr_write_b32 a15, v227\n\t"
    "v_accvgpr_read_b32 v224, a16\n\t"
    "v_accvgpr_read_b32 v225, a17\n\t"
    "v_accvgpr_read_b32 v226, a18\n\t"
    "v_accvgpr_read_b32 v227, a19\n\t"
    "v_mul_f32 v224, v224, v240\n\t"
    "v_mul_f32 v225, v225, v240\n\t"
    "v_mul_f32 v226, v226, v240\n\t"
    "v_mul_f32 v227, v227, v240\n\t"
    "v_accvgpr_write_b32 a16, v224\n\t"
    "v_accvgpr_write_b32 a17, v225\n\t"
    "v_accvgpr_write_b32 a18, v226\n\t"
    "v_accvgpr_write_b32 a19, v227\n\t"
    "v_accvgpr_read_b32 v224, a20\n\t"
    "v_accvgpr_read_b32 v225, a21\n\t"
    "v_accvgpr_read_b32 v226, a22\n\t"
    "v_accvgpr_read_b32 v227, a23\n\t"
    "v_mul_f32 v224, v224, v240\n\t"
    "v_mul_f32 v225, v225, v240\n\t"
    "v_mul_f32 v226, v226, v240\n\t"
    "v_mul_f32 v227, v227, v240\n\t"
    "v_accvgpr_write_b32 a20, v224\n\t"
    "v_accvgpr_write_b32 a21, v225\n\t"
    "v_accvgpr_write_b32 a22, v226\n\t"
    "v_accvgpr_write_b32 a23, v227\n\t"
    "v_accvgpr_read_b32 v224, a24\n\t"
    "v_accvgpr_read_b32 v225, a25\n\t"
    "v_accvgpr_read_b32 v226, a26\n\t"
    "v_accvgpr_read_b32 v227, a27\n\t"
    "v_mul_f32 v224, v224, v240\n\t"
    "v_mul_f32 v225, v225, v240\n\t"
    "v_mul_f32 v226, v226, v240\n\t"
    "v_mul_f32 v227, v227, v240\n\t"
    "v_accvgpr_write_b32 a24, v224\n\t"
    "v_accvgpr_write_b32 a25, v225\n\t"
    "v_accvgpr_write_b32 a26, v226\n\t"
    "v_accvgpr_write_b32 a27, v227\n\t"
    "v_accvgpr_read_b32 v224, a28\n\t"
    "v_accvgpr_read_b32 v225, a29\n\t"
    "v_accvgpr_read_b32 v226, a30\n\t"
    "v_accvgpr_read_b32 v227, a31\n\t"
    "v_mul_f32 v224, v224, v240\n\t"
    "v_mul_f32 v225, v225, v240\n\t"
    "v_mul_f32 v226, v226, v240\n\t"
    "v_mul_f32 v227, v227, v240\n\t"
    "v_accvgpr_write_b32 a28, v224\n\t"
    "v_accvgpr_write_b32 a29, v225\n\t"
    "v_accvgpr_write_b32 a30, v226\n\t"
    "v_accvgpr_write_b32 a31, v227\n\t"
    "v_accvgpr_read_b32 v224, a160\n\t"
    "v_accvgpr_read_b32 v225, a161\n\t"
    "v_accvgpr_read_b32 v226, a162\n\t"
    "v_accvgpr_read_b32 v227, a163\n\t"
    "v_mul_f32 v224, v224, v240\n\t"
    "v_mul_f32 v225, v225, v240\n\t"
    "v_mul_f32 v226, v226, v240\n\t"
    "v_mul_f32 v227, v227, v240\n\t"
    "v_accvgpr_write_b32 a160, v224\n\t"
    "v_accvgpr_write_b32 a161, v225\n\t"
    "v_accvgpr_write_b32 a162, v226\n\t"
    "v_accvgpr_write_b32 a163, v227\n\t"
    "v_mov_b32 v240, 1.0\n\t"
    "v_accvgpr_read_b32 v224, a32\n\t"
    "v_accvgpr_read_b32 v225, a33\n\t"
    "v_accvgpr_read_b32 v226, a34\n\t"
    "v_accvgpr_read_b32 v227, a35\n\t"
    "v_mul_f32 v224, v224, v241\n\t"
    "v_mul_f32 v225, v225, v241\n\t"
    "v_mul_f32 v226, v226, v241\n\t"
    "v_mul_f32 v227, v227, v241\n\t"
    "v_accvgpr_write_b32 a32, v224\n\t"
    "v_accvgpr_write_b32 a33, v225\n\t"
    "v_accvgpr_write_b32 a34, v226\n\t"
    "v_accvgpr_write_b32 a35, v227\n\t"
    "v_accvgpr_read_b32 v224, a36\n\t"
    "v_accvgpr_read_b32 v225, a37\n\t"
    "v_accvgpr_read_b32 v226, a38\n\t"
    "v_accvgpr_read_b32 v227, a39\n\t"
    "v_mul_f32 v224, v224, v241\n\t"
    "v_mul_f32 v225, v225, v241\n\t"
    "v_mul_f32 v226, v226, v241\n\t"
    "v_mul_f32 v227, v227, v241\n\t"
    "v_accvgpr_write_b32 a36, v224\n\t"
    "v_accvgpr_write_b32 a37, v225\n\t"
    "v_accvgpr_write_b32 a38, v226\n\t"
    "v_accvgpr_write_b32 a39, v227\n\t"
    "v_accvgpr_read_b32 v224, a40\n\t"
    "v_accvgpr_read_b32 v225, a41\n\t"
    "v_accvgpr_read_b32 v226, a42\n\t"
    "v_accvgpr_read_b32 v227, a43\n\t"
    "v_mul_f32 v224, v224, v241\n\t"
    "v_mul_f32 v225, v225, v241\n\t"
    "v_mul_f32 v226, v226, v241\n\t"
    "v_mul_f32 v227, v227, v241\n\t"
    "v_accvgpr_write_b32 a40, v224\n\t"
    "v_accvgpr_write_b32 a41, v225\n\t"
    "v_accvgpr_write_b32 a42, v226\n\t"
    "v_accvgpr_write_b32 a43, v227\n\t"
    "v_accvgpr_read_b32 v224, a44\n\t"
    "v_accvgpr_read_b32 v225, a45\n\t"
    "v_accvgpr_read_b32 v226, a46\n\t"
    "v_accvgpr_read_b32 v227, a47\n\t"
    "v_mul_f32 v224, v224, v241\n\t"
    "v_mul_f32 v225, v225, v241\n\t"
    "v_mul_f32 v226, v226, v241\n\t"
    "v_mul_f32 v227, v227, v241\n\t"
    "v_accvgpr_write_b32 a44, v224\n\t"
    "v_accvgpr_write_b32 a45, v225\n\t"
    "v_accvgpr_write_b32 a46, v226\n\t"
    "v_accvgpr_write_b32 a47, v227\n\t"
    "v_accvgpr_read_b32 v224, a48\n\t"
    "v_accvgpr_read_b32 v225, a49\n\t"
    "v_accvgpr_read_b32 v226, a50\n\t"
    "v_accvgpr_read_b32 v227, a51\n\t"
    "v_mul_f32 v224, v224, v241\n\t"
    "v_mul_f32 v225, v225, v241\n\t"
    "v_mul_f32 v226, v226, v241\n\t"
    "v_mul_f32 v227, v227, v241\n\t"
    "v_accvgpr_write_b32 a48, v224\n\t"
    "v_accvgpr_write_b32 a49, v225\n\t"
    "v_accvgpr_write_b32 a50, v226\n\t"
    "v_accvgpr_write_b32 a51, v227\n\t"
    "v_accvgpr_read_b32 v224, a52\n\t"
    "v_accvgpr_read_b32 v225, a53\n\t"
    "v_accvgpr_read_b32 v226, a54\n\t"
    "v_accvgpr_read_b32 v227, a55\n\t"
    "v_mul_f32 v224, v224, v241\n\t"
    "v_mul_f32 v225, v225, v241\n\t"
    "v_mul_f32 v226, v226, v241\n\t"
    "v_mul_f32 v227, v227, v241\n\t"
    "v_accvgpr_write_b32 a52, v224\n\t"
    "v_accvgpr_write_b32 a53, v225\n\t"
    "v_accvgpr_write_b32 a54, v226\n\t"
    "v_accvgpr_write_b32 a55, v227\n\t"
    "v_accvgpr_read_b32 v224, a56\n\t"
    "v_accvgpr_read_b32 v225, a57\n\t"
    "v_accvgpr_read_b32 v226, a58\n\t"
    "v_accvgpr_read_b32 v227, a59\n\t"
    "v_mul_f32 v224, v224, v241\n\t"
    "v_mul_f32 v225, v225, v241\n\t"
    "v_mul_f32 v226, v226, v241\n\t"
    "v_mul_f32 v227, v227, v241\n\t"
    "v_accvgpr_write_b32 a56, v224\n\t"
    "v_accvgpr_write_b32 a57, v225\n\t"
    "v_accvgpr_write_b32 a58, v226\n\t"
    "v_accvgpr_write_b32 a59, v227\n\t"
    "v_accvgpr_read_b32 v224, a60\n\t"
    "v_accvgpr_read_b32 v225, a61\n\t"
    "v_accvgpr_read_b32 v226, a62\n\t"
    "v_accvgpr_read_b32 v227, a63\n\t"
    "v_mul_f32 v224, v224, v241\n\t"
    "v_mul_f32 v225, v225, v241\n\t"
    "v_mul_f32 v226, v226, v241\n\t"
    "v_mul_f32 v227, v227, v241\n\t"
    "v_accvgpr_write_b32 a60, v224\n\t"
    "v_accvgpr_write_b32 a61, v225\n\t"
    "v_accvgpr_write_b32 a62, v226\n\t"
    "v_accvgpr_write_b32 a63, v227\n\t"
    "v_accvgpr_read_b32 v224, a164\n\t"
    "v_accvgpr_read_b32 v225, a165\n\t"
    "v_accvgpr_read_b32 v226, a166\n\t"
    "v_accvgpr_read_b32 v227, a167\n\t"
    "v_mul_f32 v224, v224, v241\n\t"
    "v_mul_f32 v225, v225, v241\n\t"
    "v_mul_f32 v226, v226, v241\n\t"
    "v_mul_f32 v227, v227, v241\n\t"
    "v_accvgpr_write_b32 a164, v224\n\t"
    "v_accvgpr_write_b32 a165, v225\n\t"
    "v_accvgpr_write_b32 a166, v226\n\t"
    "v_accvgpr_write_b32 a167, v227\n\t"
    "v_mov_b32 v241, 1.0\n\t"
    "v_accvgpr_read_b32 v224, a64\n\t"
    "v_accvgpr_read_b32 v225, a65\n\t"
    "v_accvgpr_read_b32 v226, a66\n\t"
    "v_accvgpr_read_b32 v227, a67\n\t"
    "v_mul_f32 v224, v224, v242\n\t"
    "v_mul_f32 v225, v225, v242\n\t"
    "v_mul_f32 v226, v226, v242\n\t"
    "v_mul_f32 v227, v227, v242\n\t"
    "v_accvgpr_write_b32 a64, v224\n\t"
    "v_accvgpr_write_b32 a65, v225\n\t"
    "v_accvgpr_write_b32 a66, v226\n\t"
    "v_accvgpr_write_b32 a67, v227\n\t"
    "v_accvgpr_read_b32 v224, a68\n\t"
    "v_accvgpr_read_b32 v225, a69\n\t"
    "v_accvgpr_read_b32 v226, a70\n\t"
    "v_accvgpr_read_b32 v227, a71\n\t"
    "v_mul_f32 v224, v224, v242\n\t"
    "v_mul_f32 v225, v225, v242\n\t"
    "v_mul_f32 v226, v226, v242\n\t"
    "v_mul_f32 v227, v227, v242\n\t"
    "v_accvgpr_write_b32 a68, v224\n\t"
    "v_accvgpr_write_b32 a69, v225\n\t"
    "v_accvgpr_write_b32 a70, v226\n\t"
    "v_accvgpr_write_b32 a71, v227\n\t"
    "v_accvgpr_read_b32 v224, a72\n\t"
    "v_accvgpr_read_b32 v225, a73\n\t"
    "v_accvgpr_read_b32 v226, a74\n\t"
    "v_accvgpr_read_b32 v227, a75\n\t"
    "v_mul_f32 v224, v224, v242\n\t"
    "v_mul_f32 v225, v225, v242\n\t"
    "v_mul_f32 v226, v226, v242\n\t"
    "v_mul_f32 v227, v227, v242\n\t"
    "v_accvgpr_write_b32 a72, v224\n\t"
    "v_accvgpr_write_b32 a73, v225\n\t"
    "v_accvgpr_write_b32 a74, v226\n\t"
    "v_accvgpr_write_b32 a75, v227\n\t"
    "v_accvgpr_read_b32 v224, a76\n\t"
    "v_accvgpr_read_b32 v225, a77\n\t"
    "v_accvgpr_read_b32 v226, a78\n\t"
    "v_accvgpr_read_b32 v227, a79\n\t"
    "v_mul_f32 v224, v224, v242\n\t"
    "v_mul_f32 v225, v225, v242\n\t"
    "v_mul_f32 v226, v226, v242\n\t"
    "v_mul_f32 v227, v227, v242\n\t"
    "v_accvgpr_write_b32 a76, v224\n\t"
    "v_accvgpr_write_b32 a77, v225\n\t"
    "v_accvgpr_write_b32 a78, v226\n\t"
    "v_accvgpr_write_b32 a79, v227\n\t"
    "v_accvgpr_read_b32 v224, a80\n\t"
    "v_accvgpr_read_b32 v225, a81\n\t"
    "v_accvgpr_read_b32 v226, a82\n\t"
    "v_accvgpr_read_b32 v227, a83\n\t"
    "v_mul_f32 v224, v224, v242\n\t"
    "v_mul_f32 v225, v225, v242\n\t"
    "v_mul_f32 v226, v226, v242\n\t"
    "v_mul_f32 v227, v227, v242\n\t"
    "v_accvgpr_write_b32 a80, v224\n\t"
    "v_accvgpr_write_b32 a81, v225\n\t"
    "v_accvgpr_write_b32 a82, v226\n\t"
    "v_accvgpr_write_b32 a83, v227\n\t"
    "v_accvgpr_read_b32 v224, a84\n\t"
    "v_accvgpr_read_b32 v225, a85\n\t"
    "v_accvgpr_read_b32 v226, a86\n\t"
    "v_accvgpr_read_b32 v227, a87\n\t"
    "v_mul_f32 v224, v224, v242\n\t"
    "v_mul_f32 v225, v225, v242\n\t"
    "v_mul_f32 v226, v226, v242\n\t"
    "v_mul_f32 v227, v227, v242\n\t"
    "v_accvgpr_write_b32 a84, v224\n\t"
    "v_accvgpr_write_b32 a85, v225\n\t"
    "v_accvgpr_write_b32 a86, v226\n\t"
    "v_accvgpr_write_b32 a87, v227\n\t"
    "v_accvgpr_read_b32 v224, a88\n\t"
    "v_accvgpr_read_b32 v225, a89\n\t"
    "v_accvgpr_read_b32 v226, a90\n\t"
    "v_accvgpr_read_b32 v227, a91\n\t"
    "v_mul_f32 v224, v224, v242\n\t"
    "v_mul_f32 v225, v225, v242\n\t"
    "v_mul_f32 v226, v226, v242\n\t"
    "v_mul_f32 v227, v227, v242\n\t"
    "v_accvgpr_write_b32 a88, v224\n\t"
    "v_accvgpr_write_b32 a89, v225\n\t"
    "v_accvgpr_write_b32 a90, v226\n\t"
    "v_accvgpr_write_b32 a91, v227\n\t"
    "v_accvgpr_read_b32 v224, a92\n\t"
    "v_accvgpr_read_b32 v225, a93\n\t"
    "v_accvgpr_read_b32 v226, a94\n\t"
    "v_accvgpr_read_b32 v227, a95\n\t"
    "v_mul_f32 v224, v224, v242\n\t"
    "v_mul_f32 v225, v225, v242\n\t"
    "v_mul_f32 v226, v226, v242\n\t"
    "v_mul_f32 v227, v227, v242\n\t"
    "v_accvgpr_write_b32 a92, v224\n\t"
    "v_accvgpr_write_b32 a93, v225\n\t"
    "v_accvgpr_write_b32 a94, v226\n\t"
    "v_accvgpr_write_b32 a95, v227\n\t"
    "v_accvgpr_read_b32 v224, a168\n\t"
    "v_accvgpr_read_b32 v225, a169\n\t"
    "v_accvgpr_read_b32 v226, a170\n\t"
    "v_accvgpr_read_b32 v227, a171\n\t"
    "v_mul_f32 v224, v224, v242\n\t"
    "v_mul_f32 v225, v225, v242\n\t"
    "v_mul_f32 v226, v226, v242\n\t"
    "v_mul_f32 v227, v227, v242\n\t"
    "v_accvgpr_write_b32 a168, v224\n\t"
    "v_accvgpr_write_b32 a169, v225\n\t"
    "v_accvgpr_write_b32 a170, v226\n\t"
    "v_accvgpr_write_b32 a171, v227\n\t"
    "v_mov_b32 v242, 1.0\n\t"
    "v_accvgpr_read_b32 v224, a96\n\t"
    "v_accvgpr_read_b32 v225, a97\n\t"
    "v_accvgpr_read_b32 v226, a98\n\t"
    "v_accvgpr_read_b32 v227, a99\n\t"
    "v_mul_f32 v224, v224, v243\n\t"
    "v_mul_f32 v225, v225, v243\n\t"
    "v_mul_f32 v226, v226, v243\n\t"
    "v_mul_f32 v227, v227, v243\n\t"
    "v_accvgpr_write_b32 a96, v224\n\t"
    "v_accvgpr_write_b32 a97, v225\n\t"
    "v_accvgpr_write_b32 a98, v226\n\t"
    "v_accvgpr_write_b32 a99, v227\n\t"
    "v_accvgpr_read_b32 v224, a100\n\t"
    "v_accvgpr_read_b32 v225, a101\n\t"
    "v_accvgpr_read_b32 v226, a102\n\t"
    "v_accvgpr_read_b32 v227, a103\n\t"
    "v_mul_f32 v224, v224, v243\n\t"
    "v_mul_f32 v225, v225, v243\n\t"
    "v_mul_f32 v226, v226, v243\n\t"
    "v_mul_f32 v227, v227, v243\n\t"
    "v_accvgpr_write_b32 a100, v224\n\t"
    "v_accvgpr_write_b32 a101, v225\n\t"
    "v_accvgpr_write_b32 a102, v226\n\t"
    "v_accvgpr_write_b32 a103, v227\n\t"
    "v_accvgpr_read_b32 v224, a104\n\t"
    "v_accvgpr_read_b32 v225, a105\n\t"
    "v_accvgpr_read_b32 v226, a106\n\t"
    "v_accvgpr_read_b32 v227, a107\n\t"
    "v_mul_f32 v224, v224, v243\n\t"
    "v_mul_f32 v225, v225, v243\n\t"
    "v_mul_f32 v226, v226, v243\n\t"
    "v_mul_f32 v227, v227, v243\n\t"
    "v_accvgpr_write_b32 a104, v224\n\t"
    "v_accvgpr_write_b32 a105, v225\n\t"
    "v_accvgpr_write_b32 a106, v226\n\t"
    "v_accvgpr_write_b32 a107, v227\n\t"
    "v_accvgpr_read_b32 v224, a108\n\t"
    "v_accvgpr_read_b32 v225, a109\n\t"
    "v_accvgpr_read_b32 v226, a110\n\t"
    "v_accvgpr_read_b32 v227, a111\n\t"
    "v_mul_f32 v224, v224, v243\n\t"
    "v_mul_f32 v225, v225, v243\n\t"
    "v_mul_f32 v226, v226, v243\n\t"
    "v_mul_f32 v227, v227, v243\n\t"
    "v_accvgpr_write_b32 a108, v224\n\t"
    "v_accvgpr_write_b32 a109, v225\n\t"
    "v_accvgpr_write_b32 a110, v226\n\t"
    "v_accvgpr_write_b32 a111, v227\n\t"
    "v_accvgpr_read_b32 v224, a112\n\t"
    "v_accvgpr_read_b32 v225, a113\n\t"
    "v_accvgpr_read_b32 v226, a114\n\t"
    "v_accvgpr_read_b32 v227, a115\n\t"
    "v_mul_f32 v224, v224, v243\n\t"
    "v_mul_f32 v225, v225, v243\n\t"
    "v_mul_f32 v226, v226, v243\n\t"
    "v_mul_f32 v227, v227, v243\n\t"
    "v_accvgpr_write_b32 a112, v224\n\t"
    "v_accvgpr_write_b32 a113, v225\n\t"
    "v_accvgpr_write_b32 a114, v226\n\t"
    "v_accvgpr_write_b32 a115, v227\n\t"
    "v_accvgpr_read_b32 v224, a116\n\t"
    "v_accvgpr_read_b32 v225, a117\n\t"
    "v_accvgpr_read_b32 v226, a118\n\t"
    "v_accvgpr_read_b32 v227, a119\n\t"
    "v_mul_f32 v224, v224, v243\n\t"
    "v_mul_f32 v225, v225, v243\n\t"
    "v_mul_f32 v226, v226, v243\n\t"
    "v_mul_f32 v227, v227, v243\n\t"
    "v_accvgpr_write_b32 a116, v224\n\t"
    "v_accvgpr_write_b32 a117, v225\n\t"
    "v_accvgpr_write_b32 a118, v226\n\t"
    "v_accvgpr_write_b32 a119, v227\n\t"
    "v_accvgpr_read_b32 v224, a120\n\t"
    "v_accvgpr_read_b32 v225, a121\n\t"
    "v_accvgpr_read_b32 v226, a122\n\t"
    "v_accvgpr_read_b32 v227, a123\n\t"
    "v_mul_f32 v224, v224, v243\n\t"
    "v_mul_f32 v225, v225, v243\n\t"
    "v_mul_f32 v226, v226, v243\n\t"
    "v_mul_f32 v227, v227, v243\n\t"
    "v_accvgpr_write_b32 a120, v224\n\t"
    "v_accvgpr_write_b32 a121, v225\n\t"
    "v_accvgpr_write_b32 a122, v226\n\t"
    "v_accvgpr_write_b32 a123, v227\n\t"
    "v_accvgpr_read_b32 v224, a124\n\t"
    "v_accvgpr_read_b32 v225, a125\n\t"
    "v_accvgpr_read_b32 v226, a126\n\t"
    "v_accvgpr_read_b32 v227, a127\n\t"
    "v_mul_f32 v224, v224, v243\n\t"
    "v_mul_f32 v225, v225, v243\n\t"
    "v_mul_f32 v226, v226, v243\n\t"
    "v_mul_f32 v227, v227, v243\n\t"
    "v_accvgpr_write_b32 a124, v224\n\t"
    "v_accvgpr_write_b32 a125, v225\n\t"
    "v_accvgpr_write_b32 a126, v226\n\t"
    "v_accvgpr_write_b32 a127, v227\n\t"
    "v_accvgpr_read_b32 v224, a172\n\t"
    "v_accvgpr_read_b32 v225, a173\n\t"
    "v_accvgpr_read_b32 v226, a174\n\t"
    "v_accvgpr_read_b32 v227, a175\n\t"
    "v_mul_f32 v224, v224, v243\n\t"
    "v_mul_f32 v225, v225, v243\n\t"
    "v_mul_f32 v226, v226, v243\n\t"
    "v_mul_f32 v227, v227, v243\n\t"
    "v_accvgpr_write_b32 a172, v224\n\t"
    "v_accvgpr_write_b32 a173, v225\n\t"
    "v_accvgpr_write_b32 a174, v226\n\t"
    "v_accvgpr_write_b32 a175, v227\n\t"
    "v_mov_b32 v243, 1.0\n\t"
    "v_accvgpr_read_b32 v224, a128\n\t"
    "v_accvgpr_read_b32 v225, a129\n\t"
    "v_accvgpr_read_b32 v226, a130\n\t"
    "v_accvgpr_read_b32 v227, a131\n\t"
    "v_mul_f32 v224, v224, v244\n\t"
    "v_mul_f32 v225, v225, v244\n\t"
    "v_mul_f32 v226, v226, v244\n\t"
    "v_mul_f32 v227, v227, v244\n\t"
    "v_accvgpr_write_b32 a128, v224\n\t"
    "v_accvgpr_write_b32 a129, v225\n\t"
    "v_accvgpr_write_b32 a130, v226\n\t"
    "v_accvgpr_write_b32 a131, v227\n\t"
    "v_accvgpr_read_b32 v224, a132\n\t"
    "v_accvgpr_read_b32 v225, a133\n\t"
    "v_accvgpr_read_b32 v226, a134\n\t"
    "v_accvgpr_read_b32 v227, a135\n\t"
    "v_mul_f32 v224, v224, v244\n\t"
    "v_mul_f32 v225, v225, v244\n\t"
    "v_mul_f32 v226, v226, v244\n\t"
    "v_mul_f32 v227, v227, v244\n\t"
    "v_accvgpr_write_b32 a132, v224\n\t"
    "v_accvgpr_write_b32 a133, v225\n\t"
    "v_accvgpr_write_b32 a134, v226\n\t"
    "v_accvgpr_write_b32 a135, v227\n\t"
    "v_accvgpr_read_b32 v224, a136\n\t"
    "v_accvgpr_read_b32 v225, a137\n\t"
    "v_accvgpr_read_b32 v226, a138\n\t"
    "v_accvgpr_read_b32 v227, a139\n\t"
    "v_mul_f32 v224, v224, v244\n\t"
    "v_mul_f32 v225, v225, v244\n\t"
    "v_mul_f32 v226, v226, v244\n\t"
    "v_mul_f32 v227, v227, v244\n\t"
    "v_accvgpr_write_b32 a136, v224\n\t"
    "v_accvgpr_write_b32 a137, v225\n\t"
    "v_accvgpr_write_b32 a138, v226\n\t"
    "v_accvgpr_write_b32 a139, v227\n\t"
    "v_accvgpr_read_b32 v224, a140\n\t"
    "v_accvgpr_read_b32 v225, a141\n\t"
    "v_accvgpr_read_b32 v226, a142\n\t"
    "v_accvgpr_read_b32 v227, a143\n\t"
    "v_mul_f32 v224, v224, v244\n\t"
    "v_mul_f32 v225, v225, v244\n\t"
    "v_mul_f32 v226, v226, v244\n\t"
    "v_mul_f32 v227, v227, v244\n\t"
    "v_accvgpr_write_b32 a140, v224\n\t"
    "v_accvgpr_write_b32 a141, v225\n\t"
    "v_accvgpr_write_b32 a142, v226\n\t"
    "v_accvgpr_write_b32 a143, v227\n\t"
    "v_accvgpr_read_b32 v224, a144\n\t"
    "v_accvgpr_read_b32 v225, a145\n\t"
    "v_accvgpr_read_b32 v226, a146\n\t"
    "v_accvgpr_read_b32 v227, a147\n\t"
    "v_mul_f32 v224, v224, v244\n\t"
    "v_mul_f32 v225, v225, v244\n\t"
    "v_mul_f32 v226, v226, v244\n\t"
    "v_mul_f32 v227, v227, v244\n\t"
    "v_accvgpr_write_b32 a144, v224\n\t"
    "v_accvgpr_write_b32 a145, v225\n\t"
    "v_accvgpr_write_b32 a146, v226\n\t"
    "v_accvgpr_write_b32 a147, v227\n\t"
    "v_accvgpr_read_b32 v224, a148\n\t"
    "v_accvgpr_read_b32 v225, a149\n\t"
    "v_accvgpr_read_b32 v226, a150\n\t"
    "v_accvgpr_read_b32 v227, a151\n\t"
    "v_mul_f32 v224, v224, v244\n\t"
    "v_mul_f32 v225, v225, v244\n\t"
    "v_mul_f32 v226, v226, v244\n\t"
    "v_mul_f32 v227, v227, v244\n\t"
    "v_accvgpr_write_b32 a148, v224\n\t"
    "v_accvgpr_write_b32 a149, v225\n\t"
    "v_accvgpr_write_b32 a150, v226\n\t"
    "v_accvgpr_write_b32 a151, v227\n\t"
    "v_accvgpr_read_b32 v224, a152\n\t"
    "v_accvgpr_read_b32 v225, a153\n\t"
    "v_accvgpr_read_b32 v226, a154\n\t"
    "v_accvgpr_read_b32 v227, a155\n\t"
    "v_mul_f32 v224, v224, v244\n\t"
    "v_mul_f32 v225, v225, v244\n\t"
    "v_mul_f32 v226, v226, v244\n\t"
    "v_mul_f32 v227, v227, v244\n\t"
    "v_accvgpr_write_b32 a152, v224\n\t"
    "v_accvgpr_write_b32 a153, v225\n\t"
    "v_accvgpr_write_b32 a154, v226\n\t"
    "v_accvgpr_write_b32 a155, v227\n\t"
    "v_accvgpr_read_b32 v224, a156\n\t"
    "v_accvgpr_read_b32 v225, a157\n\t"
    "v_accvgpr_read_b32 v226, a158\n\t"
    "v_accvgpr_read_b32 v227, a159\n\t"
    "v_mul_f32 v224, v224, v244\n\t"
    "v_mul_f32 v225, v225, v244\n\t"
    "v_mul_f32 v226, v226, v244\n\t"
    "v_mul_f32 v227, v227, v244\n\t"
    "v_accvgpr_write_b32 a156, v224\n\t"
    "v_accvgpr_write_b32 a157, v225\n\t"
    "v_accvgpr_write_b32 a158, v226\n\t"
    "v_accvgpr_write_b32 a159, v227\n\t"
    "v_accvgpr_read_b32 v224, a176\n\t"
    "v_accvgpr_read_b32 v225, a177\n\t"
    "v_accvgpr_read_b32 v226, a178\n\t"
    "v_accvgpr_read_b32 v227, a179\n\t"
    "v_mul_f32 v224, v224, v244\n\t"
    "v_mul_f32 v225, v225, v244\n\t"
    "v_mul_f32 v226, v226, v244\n\t"
    "v_mul_f32 v227, v227, v244\n\t"
    "v_accvgpr_write_b32 a176, v224\n\t"
    "v_accvgpr_write_b32 a177, v225\n\t"
    "v_accvgpr_write_b32 a178, v226\n\t"
    "v_accvgpr_write_b32 a179, v227\n\t"
    "v_mov_b32 v244, 1.0\n\t"
    "s_nop 1\n\t"
    "s_setpc_b64 s[48:49]\n\t"
    "END_%=:\n\t"
    :
    : "s"(ka), "s"(id), "s"(w)
    : "memory", "m0", "scc", "vcc", "v0","v1","v2","v3","v4","v5","v6","v7","v8","v9","v10","v11","v12","v13","v14","v15","v16","v17","v18","v19","v20","v21","v22","v23","v24","v25","v26","v27","v28","v29","v30","v31","v32","v33","v34","v35","v36","v37","v38","v39","v40","v41","v42","v43","v44","v45","v46","v47","v48","v49","v50","v51","v52","v53","v54","v55","v56","v57","v58","v59","v60","v61","v62","v63","v64","v65","v66","v67","v68","v69","v70","v71","v72","v73","v74","v75","v76","v77","v78","v79","v80","v81","v82","v83","v84","v85","v86","v87","v88","v89","v90","v91","v92","v93","v94","v95","v96","v97","v98","v99","v100","v101","v102","v103","v104","v105","v106","v107","v108","v109","v110","v111","v112","v113","v114","v115","v116","v117","v118","v119","v120","v121","v122","v123","v124","v125","v126","v127","v128","v129","v130","v131","v132","v133","v134","v135","v136","v137","v138","v139","v140","v141","v142","v143","v144","v145","v146","v147","v148","v149","v150","v151","v152","v153","v154","v155","v156","v157","v158","v159","v160","v161","v162","v163","v164","v165","v166","v167","v168","v169","v170","v171","v172","v173","v174","v175","v176","v177","v178","v179","v180","v181","v182","v183","v184","v185","v186","v187","v188","v189","v190","v191","v192","v193","v194","v195","v196","v197","v198","v199","v200","v201","v202","v203","v204","v205","v206","v207","v208","v209","v210","v211","v212","v213","v214","v215","v216","v217","v218","v219","v220","v221","v222","v223","v224","v225","v226","v227","v228","v229","v230","v231","v232","v233","v234","v235","v236","v237","v238","v239","v240","v241","v242","v243","v244","v245","v246","v247","v248","v249","v250","v251","v252","v253","v254","v255","a0","a1","a2","a3","a4","a5","a6","a7","a8","a9","a10","a11","a12","a13","a14","a15","a16","a17","a18","a19","a20","a21","a22","a23","a24","a25","a26","a27","a28","a29","a30","a31","a32","a33","a34","a35","a36","a37","a38","a39","a40","a41","a42","a43","a44","a45","a46","a47","a48","a49","a50","a51","a52","a53","a54","a55","a56","a57","a58","a59","a60","a61","a62","a63","a64","a65","a66","a67","a68","a69","a70","a71","a72","a73","a74","a75","a76","a77","a78","a79","a80","a81","a82","a83","a84","a85","a86","a87","a88","a89","a90","a91","a92","a93","a94","a95","a96","a97","a98","a99","a100","a101","a102","a103","a104","a105","a106","a107","a108","a109","a110","a111","a112","a113","a114","a115","a116","a117","a118","a119","a120","a121","a122","a123","a124","a125","a126","a127","a128","a129","a130","a131","a132","a133","a134","a135","a136","a137","a138","a139","a140","a141","a142","a143","a144","a145","a146","a147","a148","a149","a150","a151","a152","a153","a154","a155","a156","a157","a158","a159","a160","a161","a162","a163","a164","a165","a166","a167","a168","a169","a170","a171","a172","a173","a174","a175","a176","a177","a178","a179","a180","a181","a182","a183","a184","a185","a186","a187","a188","a189","a190","a191","a192","a193","a194","a195","a196","a197","a198","a199","a200","a201","a202","a203","a204","a205","a206","a207","a208","a209","a210","a211","a212","a213","a214","a215","a216","a217","a218","a219","a220","a221","a222","a223","a224","a225","a226","a227","a228","a229","a230","a231","a232","a233","a234","a235","a236","a237","a238","a239","a240","a241","a242","a243","a244","a245","a246","a247","a248","a249","a250","a251","a252","a253","a254","a255","s4","s5","s6","s7","s8","s9","s10","s11","s12","s13","s14","s15","s16","s17","s18","s19","s20","s21","s22","s23","s24","s25","s26","s27","s28","s29","s30","s31","s32","s33","s34","s35","s36","s37","s38","s39","s40","s41","s42","s43","s44","s45","s46","s47","s48","s49","s50","s51","s52","s53","s54","s55","s56","s57","s58","s59","s60","s61","s62","s63","s64","s65","s66","s67","s68","s69","s70","s71","s72","s73","s74","s75","s76","s77","s78","s79","s80","s81","s82","s83","s84","s85","s86","s87","s88","s89","s90","s91","s92","s93","s94","s95","s96","s97","s98","s99");
}

extern "C" void kernel_launch(void* const* d_in, const int* in_sizes, int n_in, void* d_out, int out_size, void* d_ws, size_t ws_size, hipStream_t stream) {
  const char* const* p = (const char* const*)d_in;
  attn_k1<<<256, 256, 0, stream>>>(p[1], p[2], p[0], p[3], p[4], p[9], (char*)d_ws, (char*)d_out, p[6], p[7], p[5], p[8], p[9], p[9], (char*)d_ws, (char*)d_out);
}
```
